```python
import torch

def _softmax(x, axis=-1):
    return torch.softmax(x, dim=axis)
def _gelu(x):
    return torch.nn.functional.gelu(x, approximate="tanh")
def _silu(x):
    return torch.nn.functional.silu(x)
def _sigmoid(x):
    return torch.sigmoid(x)
def _layernorm(x, axis=-1, eps=1e-5):
    m = x.mean(dim=axis, keepdim=True)
    v = x.var(dim=axis, keepdim=True, unbiased=False)
    return (x - m) / torch.sqrt(v + eps)
def _scatter_add(tgt, idx, src, axis=0):
    return tgt.clone().index_add_(axis, idx, src)
def _segment_sum(src, seg, n):
    out = torch.zeros((n,) + src.shape[1:], dtype=src.dtype)
    return out.index_add_(0, seg, src)
def _pad(x, pw):
    flat = tuple(v for pair in reversed(pw) for v in pair)
    return torch.nn.functional.pad(x, flat)

kernel_name = 'gated_mix__ssm_conv_s5__linear_attn_block__prompt'

def setup_inputs():

    _w = (lambda a: (lambda t: t.to(torch.bfloat16) if t.is_floating_point() else t)(torch.as_tensor(a)))
    def _A_setup():
        B, S, D = 1, 2048, 640
        P = D
        Lambda_re = -0.5 * (lambda _x: torch.exp(torch.as_tensor(_x)))((lambda *s: torch.randn(*s, dtype=torch.bfloat16) * 0.2)(P))
        Lambda_im = (lambda *s: torch.randn(*s, dtype=torch.bfloat16) * 0.8)(P) * 1.0
        B_re = (lambda *s: torch.randn(*s, dtype=torch.bfloat16) * 0.4)(P, D) * (D ** -0.5)
        B_im = (lambda *s: torch.randn(*s, dtype=torch.bfloat16) * 0.4)(P, D) * (D ** -0.5)
        C_re = (lambda *s: torch.randn(*s, dtype=torch.bfloat16) * 0.4)(D, P) * (P ** -0.5)
        C_im = (lambda *s: torch.randn(*s, dtype=torch.bfloat16) * 0.4)(D, P) * (P ** -0.5)
        log_dt = (lambda *s: torch.randn(*s, dtype=torch.bfloat16) * 0.2)(P) * 0.3 - 2.0
        Dskip = (lambda *s: torch.randn(*s, dtype=torch.bfloat16) * 0.05)(D)
        return {
            "x": _w((lambda *s: torch.randn(*s, dtype=torch.bfloat16) * 0.05)(B, S, D)),
            "Lambda_re": _w(Lambda_re),
            "Lambda_im": _w(Lambda_im),
            "B_re": _w(B_re),
            "B_im": _w(B_im),
            "C_re": _w(C_re),
            "C_im": _w(C_im),
            "log_dt": _w(log_dt),
            "Dskip": _w(Dskip),
        }
    _A_in = _A_setup()

    def _B_setup():
        B, S, D = 1, 2048, 640
        return {
            "x": _w((lambda *s: torch.randn(*s, dtype=torch.bfloat16) * 0.05)(B, S, D)),
            "wqkv": _w((lambda *s: torch.randn(*s, dtype=torch.bfloat16) * 0.8)(D, 3 * D)),
            "wo": _w((lambda *s: torch.randn(*s, dtype=torch.bfloat16) * 0.05)(D, D)),
        }
    _B_in = _B_setup()

    return {
        "A_x": _A_in["x"],
        "A_Lambda_re": _A_in["Lambda_re"],
        "A_Lambda_im": _A_in["Lambda_im"],
        "A_B_re": _A_in["B_re"],
        "A_B_im": _A_in["B_im"],
        "A_C_re": _A_in["C_re"],
        "A_C_im": _A_in["C_im"],
        "A_log_dt": _A_in["log_dt"],
        "A_Dskip": _A_in["Dskip"],
        "B_x": _B_in["x"],
        "B_wqkv": _B_in["wqkv"],
        "B_wo": _B_in["wo"],
        "gate_w": _w((lambda *s: torch.randn(*s, dtype=torch.bfloat16) * 0.05)(640) * 16.0),
    }

def reference(A_x, A_Lambda_re, A_Lambda_im, A_B_re, A_B_im, A_C_re, A_C_im, A_log_dt, A_Dskip, B_x, B_wqkv, B_wo, gate_w):
    def _A_ref(x, Lambda_re, Lambda_im, B_re, B_im, C_re, C_im, log_dt, Dskip):
        Bsz, L, H = x.shape
        Lam = Lambda_re + 1j * Lambda_im
        Bmat = B_re + 1j * B_im
        Cmat = C_re + 1j * C_im
        dt = torch.exp(log_dt)
        Lam_bar = torch.exp(dt * Lam)
        B_bar = ((Lam_bar - 1.0) / Lam)[:, None] * Bmat
        Bu = torch.einsum("bld,pd->blp", x + 0j, B_bar)
        h = torch.zeros_like(Bu[:, 0, :])
        hs = []
        for t in range(L):
            h = Lam_bar[None, :] * h + Bu[:, t, :]
            hs.append(h)
        hseq = torch.stack(hs, 1)
        y = 2.0 * torch.einsum("blp,dp->bld", hseq, Cmat).real + x * Dskip[None, None, :]
        return y

    def _B_ref(x, wqkv, wo):
        B, S, D = x.shape
        H = 32; Dh = D // H
        qkv = (lambda x, *d: x.permute(*d))((x @ wqkv).reshape(B, S, 3, H, Dh), 2, 0, 3, 1, 4)
        q, k, v = qkv[0], qkv[1], qkv[2]
        phi = lambda t: torch.where(t > 0, t + 1, torch.exp(t)) + 1e-6
        pq, pk = phi(q), phi(k)
        kv = torch.einsum("bhsd,bhse->bhde", pk, v)
        z = torch.einsum("bhsd->bhd", pk)
        num = torch.einsum("bhqd,bhde->bhqe", pq, kv)
        den = torch.einsum("bhqd,bhd->bhq", pq, z)[..., None] + 1e-6
        o = num / den
        return (lambda x, *d: x.permute(*d))(o, 0, 2, 1, 3).reshape(B, S, D) @ wo

    x = A_x
    g = _sigmoid((x * gate_w).sum(-1, keepdims=True))
    a = _A_ref(x, A_Lambda_re, A_Lambda_im, A_B_re, A_B_im, A_C_re, A_C_im, A_log_dt, A_Dskip)
    b = _B_ref(x, B_wqkv, B_wo)
    return g * a + (1 - g) * b
```

```cpp
#include <hip/hip_runtime.h>
#include <hip/hip_bf16.h>
#include <stdint.h>

typedef unsigned short u16;
using bf16x8 = __attribute__((ext_vector_type(8))) short;
using f32x4 = __attribute__((ext_vector_type(4))) float;

constexpr int S_ = 2048, D_ = 640, P_ = 640, H_ = 32, DH = 20;

__device__ __forceinline__ float bf2f(u16 v) { return __uint_as_float(((unsigned)v) << 16); }
__device__ __forceinline__ u16 f2bf(float f) { __hip_bfloat16 b = __float2bfloat16(f); return *reinterpret_cast<u16*>(&b); }
__device__ __forceinline__ float rbf(float f) { return bf2f(f2bf(f)); }

template <bool BT>
__global__ __launch_bounds__(256) void k_gemm(const u16* __restrict__ A, int lda, const u16* __restrict__ B, int ldb,
                                              float* __restrict__ C, int ldc, int K) {
  __shared__ u16 sA[64][40];
  __shared__ u16 sB[64][40];
  const int bm = blockIdx.y * 64, bn = blockIdx.x * 64;
  const int tid = threadIdx.x, lane = tid & 63, w = tid >> 6, wr = w >> 1, wc = w & 1;
  f32x4 acc[2][2] = {};
  for (int k0 = 0; k0 < K; k0 += 32) {
    {
      int r = tid >> 2, c = (tid & 3) * 8;
      *(uint4*)&sA[r][c] = *(const uint4*)&A[(size_t)(bm + r) * lda + k0 + c];
      if (BT) {
        *(uint4*)&sB[r][c] = *(const uint4*)&B[(size_t)(bn + r) * ldb + k0 + c];
      } else {
        int kk = tid >> 3, nn = (tid & 7) * 8;
        uint4 v = *(const uint4*)&B[(size_t)(k0 + kk) * ldb + bn + nn];
        const u16* pv = (const u16*)&v;
        for (int j = 0; j < 8; ++j) sB[nn + j][kk] = pv[j];
      }
    }
    __syncthreads();
    bf16x8 a[2], b[2];
    for (int i = 0; i < 2; ++i) {
      a[i] = *(const bf16x8*)&sA[wr * 32 + i * 16 + (lane & 15)][(lane >> 4) * 8];
      b[i] = *(const bf16x8*)&sB[wc * 32 + i * 16 + (lane & 15)][(lane >> 4) * 8];
    }
    for (int i = 0; i < 2; ++i)
      for (int j = 0; j < 2; ++j) acc[i][j] = __builtin_amdgcn_mfma_f32_16x16x32_bf16(a[i], b[j], acc[i][j], 0, 0, 0);
    __syncthreads();
  }
  for (int i = 0; i < 2; ++i)
    for (int j = 0; j < 2; ++j)
      for (int r = 0; r < 4; ++r)
        C[(size_t)(bm + wr * 32 + i * 16 + (lane >> 4) * 4 + r) * ldc + bn + wc * 32 + j * 16 + (lane & 15)] = acc[i][j][r];
}

__global__ void k_gate(const u16* __restrict__ x, const u16* __restrict__ gw, float* __restrict__ g) {
  int row = blockIdx.x * 4 + (threadIdx.x >> 6), lane = threadIdx.x & 63;
  float s = 0.f;
  for (int d = lane; d < D_; d += 64) s += rbf(bf2f(x[row * D_ + d]) * bf2f(gw[d]));
  for (int m = 32; m >= 1; m >>= 1) s += __shfl_xor(s, m);
  s = rbf(s);
  if (lane == 0) g[row] = rbf(1.f / (1.f + expf(-s)));
}

__global__ void k_scan(const float* __restrict__ bu, const u16* __restrict__ lre, const u16* __restrict__ lim,
                       const u16* __restrict__ logdt, u16* __restrict__ h) {
  int p = blockIdx.x * 64 + threadIdx.x;
  float lr = bf2f(lre[p]), li = bf2f(lim[p]);
  float dt = rbf(expf(bf2f(logdt[p])));
  float er = expf(dt * lr), an = dt * li;
  float ar = er * cosf(an), ai = er * sinf(an);
  float nr = ar - 1.f, ni = ai;
  float den = lr * lr + li * li;
  float cr = (nr * lr + ni * li) / den, ci = (ni * lr - nr * li) / den;
  float hr = 0.f, hi = 0.f;
  for (int t = 0; t < S_; ++t) {
    float br = bu[t * 1280 + p], bi = bu[t * 1280 + 640 + p];
    float ur = cr * br - ci * bi, ui = cr * bi + ci * br;
    float nhr = ar * hr - ai * hi + ur, nhi = ar * hi + ai * hr + ui;
    hr = nhr; hi = nhi;
    h[t * 1280 + p] = f2bf(hr);
    h[t * 1280 + 640 + p] = f2bf(-hi);
  }
}

__device__ __forceinline__ float phi(float t) {
  float v = t > 0.f ? rbf(t + 1.f) : rbf(expf(t));
  return rbf(v + 1e-6f);
}

__global__ __launch_bounds__(256) void k_kv(const float* __restrict__ qkv, float* __restrict__ kv, float* __restrict__ z) {
  int h = blockIdx.x;
  __shared__ float sk[64][20], sv[64][20];
  int tid = threadIdx.x;
  float acc0 = 0.f, acc1 = 0.f, zz = 0.f;
  for (int s0 = 0; s0 < S_; s0 += 64) {
    for (int e = tid; e < 64 * 20; e += 256) {
      int r = e / 20, c = e % 20;
      sk[r][c] = phi(rbf(qkv[(s0 + r) * 1920 + 640 + h * 20 + c]));
      sv[r][c] = rbf(qkv[(s0 + r) * 1920 + 1280 + h * 20 + c]);
    }
    __syncthreads();
    for (int r = 0; r < 64; ++r) {
      acc0 += sk[r][tid / 20] * sv[r][tid % 20];
      if (tid + 256 < 400) acc1 += sk[r][(tid + 256) / 20] * sv[r][(tid + 256) % 20];
      if (tid < 20) zz += sk[r][tid];
    }
    __syncthreads();
  }
  kv[h * 400 + tid] = rbf(acc0);
  if (tid + 256 < 400) kv[h * 400 + tid + 256] = rbf(acc1);
  if (tid < 20) z[h * 20 + tid] = rbf(zz);
}

__global__ void k_o(const float* __restrict__ qkv, const float* __restrict__ kv, const float* __restrict__ z, u16* __restrict__ o) {
  int idx = blockIdx.x * 256 + threadIdx.x;
  int s = idx / 32, h = idx % 32;
  float pq[20];
  float den = 0.f;
  for (int d = 0; d < 20; ++d) { pq[d] = phi(rbf(qkv[s * 1920 + h * 20 + d])); den += pq[d] * z[h * 20 + d]; }
  den = rbf(rbf(den) + 1e-6f);
  for (int e = 0; e < 20; ++e) {
    float n = 0.f;
    for (int d = 0; d < 20; ++d) n += pq[d] * kv[h * 400 + d * 20 + e];
    o[s * 640 + h * 20 + e] = f2bf(rbf(n) / den);
  }
}

__global__ void k_final(const float* __restrict__ y0, const float* __restrict__ y1, const float* __restrict__ bb,
                        const u16* __restrict__ x, const u16* __restrict__ dskip, const float* __restrict__ g,
                        float* __restrict__ out) {
  int idx = blockIdx.x * 256 + threadIdx.x;
  int s = idx / 640, d = idx % 640;
  float a = 2.f * (y0[idx] + y1[idx]) + rbf(bf2f(x[idx]) * bf2f(dskip[d]));
  float gg = g[s];
  float b = rbf(bb[idx]);
  out[idx] = gg * a + rbf(rbf(1.f - gg) * b);
}

extern "C" void kernel_launch(void* const* d_in, const int* in_sizes, int n_in, void* d_out, int out_size, void* d_ws,
                              size_t ws_size, hipStream_t stream) {
  const u16* A_x = (const u16*)d_in[0];
  const u16* Lre = (const u16*)d_in[1];
  const u16* Lim = (const u16*)d_in[2];
  const u16* Bre = (const u16*)d_in[3];
  const u16* Bim = (const u16*)d_in[4];
  const u16* Cre = (const u16*)d_in[5];
  const u16* Cim = (const u16*)d_in[6];
  const u16* logdt = (const u16*)d_in[7];
  const u16* Dskip = (const u16*)d_in[8];
  const u16* B_x = (const u16*)d_in[9];
  const u16* wqkv = (const u16*)d_in[10];
  const u16* wo = (const u16*)d_in[11];
  const u16* gw = (const u16*)d_in[12];
  float* out = (float*)d_out;

  char* ws = (char*)d_ws;
  float* bu = (float*)ws;                 ws += (size_t)S_ * 1280 * 4;
  float* qkv = (float*)ws;                ws += (size_t)S_ * 1920 * 4;
  float* y0 = (float*)ws;                 ws += (size_t)S_ * 640 * 4;
  float* y1 = (float*)ws;                 ws += (size_t)S_ * 640 * 4;
  float* bb = (float*)ws;                 ws += (size_t)S_ * 640 * 4;
  u16* h = (u16*)ws;                      ws += (size_t)S_ * 1280 * 2;
  u16* o = (u16*)ws;                      ws += (size_t)S_ * 640 * 2;
  float* g = (float*)ws;                  ws += S_ * 4;
  float* kv = (float*)ws;                 ws += 32 * 400 * 4;
  float* z = (float*)ws;                  ws += 640 * 4;

  k_gate<<<S_ / 4, 256, 0, stream>>>(A_x, gw, g);
  k_gemm<true><<<dim3(10, 32), 256, 0, stream>>>(A_x, 640, Bre, 640, bu, 1280, 640);
  k_gemm<true><<<dim3(10, 32), 256, 0, stream>>>(A_x, 640, Bim, 640, bu + 640, 1280, 640);
  k_gemm<false><<<dim3(30, 32), 256, 0, stream>>>(A_x, 640, wqkv, 1920, qkv, 1920, 640);
  k_scan<<<10, 64, 0, stream>>>(bu, Lre, Lim, logdt, h);
  k_kv<<<32, 256, 0, stream>>>(qkv, kv, z);
  k_o<<<S_ * 32 / 256, 256, 0, stream>>>(qkv, kv, z, o);
  k_gemm<true><<<dim3(10, 32), 256, 0, stream>>>(h, 1280, Cre, 640, y0, 640, 640);
  k_gemm<true><<<dim3(10, 32), 256, 0, stream>>>(h + 640, 1280, Cim, 640, y1, 640, 640);
  k_gemm<false><<<dim3(10, 32), 256, 0, stream>>>(o, 640, wo, 640, bb, 640, 640);
  k_final<<<S_ * 640 / 256, 256, 0, stream>>>(y0, y1, bb, A_x, Dskip, g, out);
}
```

```cpp
#include <hip/hip_runtime.h>
#include <stdint.h>

typedef unsigned short u16;
typedef unsigned long long u64;
typedef short bf16x8 __attribute__((ext_vector_type(8)));
typedef short bf16x4 __attribute__((ext_vector_type(4)));
typedef float f32x4 __attribute__((ext_vector_type(4)));
typedef float f32x2 __attribute__((ext_vector_type(2)));
typedef unsigned u32x4 __attribute__((ext_vector_type(4)));
typedef unsigned u32x2 __attribute__((ext_vector_type(2)));
typedef __bf16 bf16v2 __attribute__((ext_vector_type(2)));
#define LAS __attribute__((address_space(3)))
#define GAS __attribute__((address_space(1)))
#define DI __device__ __forceinline__

constexpr unsigned WS_CFLAG = 0;
constexpr unsigned WS_TMO   = 2048;
constexpr unsigned WS_AGG   = 4096;
constexpr unsigned WS_PART  = WS_AGG + 16 * 16 * 80 * 8;
constexpr unsigned WS_FIN   = WS_PART + 16 * 16 * 848 * 8;
constexpr unsigned WS_ZEND  = WS_FIN + 16 * 848 * 8;
constexpr unsigned WS_WKV   = (WS_ZEND + 4095) / 4096 * 4096;
constexpr unsigned WS_WQ    = WS_WKV + 16 * 102400;
constexpr unsigned WS_A     = WS_WQ + 16 * 61440;
constexpr unsigned WS_G     = WS_A + 128 * 240 * 256;
constexpr unsigned TAG = 1u;
constexpr unsigned SPIN_CAP = 1u << 15;

DI unsigned pk2(float a, float b) { f32x2 p = {a, b}; return __builtin_bit_cast(unsigned, __builtin_convertvector(p, bf16v2)); }
DI float blo(unsigned u) { return __uint_as_float(u << 16); }
DI float bhi(unsigned u) { return __uint_as_float(u & 0xffff0000u); }
DI float rbf(float a) { return blo(pk2(a, a)); }
DI float bf2f(unsigned v) { return __uint_as_float(v << 16); }
DI bf16x8 frag4(unsigned a, unsigned b, unsigned c, unsigned d) { u32x4 v = {a, b, c, d}; return __builtin_bit_cast(bf16x8, v); }
DI f32x4 mfma(bf16x8 a, bf16x8 b, f32x4 c) { return __builtin_amdgcn_mfma_f32_16x16x32_bf16(a, b, c, 0, 0, 0); }
DI bf16x8 lds_frag(LAS char* p) { return *(LAS bf16x8*)p; }
DI bf16x8 lds_frag_tr(LAS char* p0, LAS char* p1) {
  bf16x4 a = __builtin_amdgcn_ds_read_tr16_b64_v4i16((LAS bf16x4*)p0);
  bf16x4 b = __builtin_amdgcn_ds_read_tr16_b64_v4i16((LAS bf16x4*)p1);
  return __builtin_shufflevector(a, b, 0, 1, 2, 3, 4, 5, 6, 7);
}
template <int SC1>
DI void glds(const void* sbase, unsigned voff, unsigned ldsaddr) {
  if constexpr (SC1)
    asm volatile("s_mov_b32 m0, %2\n\ts_nop 0\n\tglobal_load_lds_dwordx4 %0, %1 sc1" ::"v"(voff), "s"(sbase), "s"(ldsaddr) : "memory");
  else
    asm volatile("s_mov_b32 m0, %2\n\ts_nop 0\n\tglobal_load_lds_dwordx4 %0, %1" ::"v"(voff), "s"(sbase), "s"(ldsaddr) : "memory");
}
DI void store16_sc1(void* sbase, unsigned voff, u32x4 v) {
  asm volatile("global_store_dwordx4 %0, %1, %2 sc1\n\ts_nop 1" ::"v"(voff), "v"(v), "s"(sbase) : "memory");
}
#define SYNC(N) do { asm volatile("s_waitcnt vmcnt(" #N ") lgkmcnt(0)\n\ts_barrier" ::: "memory"); __builtin_amdgcn_sched_barrier(0); } while (0)
#define LSYNC() do { asm volatile("s_waitcnt lgkmcnt(0)\n\ts_barrier" ::: "memory"); __builtin_amdgcn_sched_barrier(0); } while (0)
DI u64 gload(const GAS u64* p) { return __hip_atomic_load(p, __ATOMIC_RELAXED, __HIP_MEMORY_SCOPE_AGENT); }
DI void gstore(GAS u64* p, float v) {
  __hip_atomic_store(p, ((u64)TAG << 32) | (u64)__float_as_uint(v), __ATOMIC_RELAXED, __HIP_MEMORY_SCOPE_AGENT);
}
DI bool spin_fail(unsigned& spins, GAS unsigned* tmo) {
  __builtin_amdgcn_s_sleep(1);
  if (++spins > SPIN_CAP) { __hip_atomic_store(tmo, 1u, __ATOMIC_RELAXED, __HIP_MEMORY_SCOPE_AGENT); return true; }
  if ((spins & 63u) == 0u)
    return __builtin_amdgcn_readfirstlane(__hip_atomic_load(tmo, __ATOMIC_RELAXED, __HIP_MEMORY_SCOPE_AGENT)) != 0;
  return false;
}
DI float phi1(float t) {
  return (__builtin_amdgcn_exp2f(fminf(t, 0.f) * 1.4426950408889634f) + fmaxf(t, 0.f)) + 1e-6f;
}
DI void cmul(float& r, float& i, float ar, float ai) { float t = r * ar - i * ai; i = r * ai + i * ar; r = t; }

__global__ __launch_bounds__(256, 1) void k1(const u16* __restrict__ x, const u16* __restrict__ lre, const u16* __restrict__ lim,
                                             const u16* __restrict__ ldt, const u16* __restrict__ bre, const u16* __restrict__ bim,
                                             const u16* __restrict__ wqkv, const u16* __restrict__ gw, char* __restrict__ ws) {
  extern __shared__ __attribute__((aligned(16))) char lds_[];
  LAS char* lds = (LAS char*)lds_;
  const unsigned ldsb = (unsigned)(size_t)lds;
  const int tid = threadIdx.x, lane = tid & 63;
  const int w = __builtin_amdgcn_readfirstlane(tid >> 6);
  const int xcd = blockIdx.x & 7, idx = blockIdx.x >> 3;
  const int rb = 8 * (xcd & 1) + (idx & 7), cg = 4 * (xcd >> 1) + (idx >> 3);
  const int r = lane & 15, g = lane >> 4;
  GAS unsigned* tmo = (GAS unsigned*)(ws + WS_TMO);

  constexpr unsigned SLOT = 20480;
  constexpr unsigned A0 = 0, A1 = SLOT, A2 = 2 * SLOT, A3 = 3 * SLOT, B0 = 4 * SLOT, B1 = 5 * SLOT, B2 = 6 * SLOT;
  constexpr unsigned MISC = 7 * SLOT;
  constexpr unsigned L_E = MISC;
  constexpr unsigned L_G = MISC + 5120;
  constexpr unsigned L_KVT = MISC + 5632;
  constexpr unsigned ASLOT[4] = {A0, A1, A2, A3};
  constexpr unsigned BSLOT[3] = {B0, B1, B2};
  constexpr unsigned KVSLOT[5] = {B2, A2, B0, A3, A0};
  constexpr unsigned QSLOT[5] = {A1, B1, B2, A2, B0};

  u32x2 cq[3][2];
  unsigned cdst[3];
  bool cgate[3];
  int ck[3];
#pragma unroll
  for (int it = 0; it < 3; ++it) {
    const int c = it < 2 ? tid + 256 * it : 512 + (tid & 127);
    const int row = c >> 4, j = c & 15, k = 40 * rb + row;
    const int isq = j >= 10, jj = isq ? j - 10 : j, tt = jj >> 1, half = jj & 1;
    const int tail = isq ? (tt == 2) : (tt == 4);
    const int sec = isq ? 0 : (tail ? 1 + half : 1 + (tt >> 1));
    const int b = sec * 640 + 40 * cg;
    const int col0 = tail ? b + 16 : b + 20 * (tt & 1) + 8 * half;
    const int col1 = tail ? b + 36 : col0 + 4;
    const bool gate = isq && tail && half;
    const u16* p0 = gate ? gw + (k & ~3) : wqkv + k * 1920 + col0;
    const u16* p1 = gate ? gw + (k & ~3) : wqkv + k * 1920 + col1;
    asm volatile("global_load_dwordx2 %0, %1, off" : "=v"(cq[it][0]) : "v"(p0) : "memory");
    asm volatile("global_load_dwordx2 %0, %1, off" : "=v"(cq[it][1]) : "v"(p1) : "memory");
    const int pk = k ^ (((k >> 3) & 1) << 2);
    cdst[it] = isq ? WS_WQ + cg * 61440 + pk * 96 + jj * 16 : WS_WKV + cg * 102400 + pk * 160 + jj * 16;
    cgate[it] = gate; ck[it] = k;
  }
  unsigned p_lre, p_lim, p_ldt;
  {
    const int ch = 40 * cg + (lane < 40 ? lane : 39);
    asm volatile("global_load_ushort %0, %1, off" : "=v"(p_lre) : "v"(lre + ch) : "memory");
    asm volatile("global_load_ushort %0, %1, off" : "=v"(p_lim) : "v"(lim + ch) : "memory");
    asm volatile("global_load_ushort %0, %1, off" : "=v"(p_ldt) : "v"(ldt + ch) : "memory");
  }

  unsigned xoff[4];
#pragma unroll
  for (int n = 0; n < 4; ++n) {
    const int mt = n >> 1, rr = 8 * (n & 1) + (lane >> 3);
    const int row = 128 * rb + 32 * w + 8 * (rr >> 2) + 4 * mt + (rr & 3);
    xoff[n] = row * 1280 + (((lane & 7) ^ ((rr >> 1) & 7)) << 4);
  }
  unsigned boff[5];
  const u16* bbase[5];
#pragma unroll
  for (int i = 0; i < 5; ++i) {
    const int n = 5 * w + i;
    const int tt = n >> 2, rho = 4 * n + (lane >> 4), rr = rho & 15;
    const int isim = tt < 4 ? (tt & 1) : (n >= 18);
    const int chl = tt < 4 ? 16 * (tt >> 1) + rr : 32 + (rr & 7);
    bbase[i] = isim ? bim : bre;
    boff[i] = (40 * cg + chl) * 1280 + (((lane & 15) ^ rr) << 4);
  }
  auto issue_x = [&](int s) {
#pragma unroll
    for (int n = 0; n < 4; ++n) glds<0>(x + s * 64, xoff[n], ldsb + ASLOT[s & 3] + (32 * w + 8 * n) * 128);
  };
  auto issue_b = [&](int be) {
#pragma unroll
    for (int i = 0; i < 5; ++i) glds<0>(bbase[i] + be * 128, boff[i], ldsb + BSLOT[be % 3] + (5 * w + i) * 1024);
  };
  const unsigned lin = lane * 16;
  auto issue_kv = [&](int j) {
    const char* src = ws + WS_WKV + cg * 102400 + j * 20480 + w * 5120;
#pragma unroll
    for (int i = 0; i < 5; ++i) glds<1>(src + i * 1024, lin, ldsb + KVSLOT[j] + (5 * w + i) * 1024);
  };
  auto issue_q = [&](int m) {
    const char* src = ws + WS_WQ + cg * 61440 + m * 12288 + w * 3072;
#pragma unroll
    for (int i = 0; i < 3; ++i) glds<1>(src + i * 1024, lin, ldsb + QSLOT[m] + (3 * w + i) * 1024);
  };

  issue_x(0); issue_b(0); issue_x(1); issue_x(2); issue_b(1);

  asm volatile("s_waitcnt vmcnt(22)"
               : "+v"(cq[0][0]), "+v"(cq[0][1]), "+v"(cq[1][0]), "+v"(cq[1][1]), "+v"(cq[2][0]), "+v"(cq[2][1]),
                 "+v"(p_lre), "+v"(p_lim), "+v"(p_ldt)::"memory");
#pragma unroll
  for (int it = 0; it < 3; ++it) {
    u32x4 v = {cq[it][0][0], cq[it][0][1], cq[it][1][0], cq[it][1][1]};
    if (cgate[it]) {
      const unsigned d = (ck[it] & 2) ? cq[it][0][1] : cq[it][0][0];
      v[0] = (ck[it] & 1) ? (d >> 16) : (d & 0xffffu);
      v[1] = 0; v[2] = 0; v[3] = 0;
    }
    store16_sc1(ws, cdst[it], v);
  }

  bf16x8 xa[20][2];
  f32x4 accS[5][2];
#pragma unroll
  for (int t = 0; t < 5; ++t) { accS[t][0] = f32x4{0, 0, 0, 0}; accS[t][1] = f32x4{0, 0, 0, 0}; }
  bf16x8 bq[2][5];
  LAS char* xrd = lds + (32 * w + r) * 128;
  const unsigned xsw = (r >> 1) & 7;
  LAS char* brd = lds + r * 256;
  auto read1 = [&](int ks) {
    const int s = ks >> 1, kk = ks & 1, be = ks >> 2, kq = ks & 3;
#pragma unroll
    for (int mt = 0; mt < 2; ++mt) xa[ks][mt] = lds_frag(xrd + ASLOT[s & 3] + mt * 2048 + (((4 * kk + g) ^ xsw) << 4));
#pragma unroll
    for (int t = 0; t < 5; ++t) bq[ks & 1][t] = lds_frag(brd + BSLOT[be % 3] + t * 4096 + (((4 * kq + g) ^ r) << 4));
  };
  unsigned cf = 0;
  const GAS unsigned* cfp = (const GAS unsigned*)(ws + WS_CFLAG) + cg * 16 + (lane & 15);

  float c_ar, c_ai, c_a8r, c_a8i, c_a128r, c_a128i;
  float par[3], pai[3], pcr[3], pci[3];

  SYNC(16); issue_x(3);
  read1(0);
#pragma unroll
  for (int ks = 0; ks < 20; ++ks) {
    if (ks + 1 < 20) {
      if (((ks + 1) & 1) == 0) {
        const int s = (ks + 1) >> 1;
        if (s == 1) { SYNC(16); issue_x(4); issue_b(2); }
        if (s == 2) { SYNC(16); issue_x(5); }
        if (s == 3) {
          SYNC(13);
          if (tid == 0) __hip_atomic_store((GAS unsigned*)(ws + WS_CFLAG) + cg * 16 + rb, TAG, __ATOMIC_RELAXED, __HIP_MEMORY_SCOPE_AGENT);
          issue_x(6); issue_b(3);
        }
        if (s == 4) { SYNC(13); issue_x(7); }
        if (s == 5) { SYNC(13); issue_x(8); issue_b(4); }
        if (s == 6) {
          SYNC(13);
          asm volatile("global_load_dword %0, %1, off sc1" : "=v"(cf) : "v"(cfp) : "memory");
          issue_x(9);
        }
        if (s == 7) {
          asm volatile("s_waitcnt vmcnt(4)" : "+v"(cf)::"memory");
          SYNC(4);
          if (!__all(cf == TAG)) {
            unsigned spins = 0;
            for (;;) {
              cf = __hip_atomic_load(cfp, __ATOMIC_RELAXED, __HIP_MEMORY_SCOPE_AGENT);
              if (__all(cf == TAG)) break;
              if (spin_fail(spins, tmo)) break;
            }
          }
          issue_kv(0);
        }
        if (s == 8) { SYNC(5); issue_kv(1); issue_kv(2); }
        if (s == 9) { SYNC(15); issue_kv(3); }
      }
      read1(ks + 1);
    }
#pragma unroll
    for (int t = 0; t < 5; ++t)
#pragma unroll
      for (int mt = 0; mt < 2; ++mt) accS[t][mt] = mfma(xa[ks][mt], bq[ks & 1][t], accS[t][mt]);
    if (ks == 1) {
      const float lr = bf2f(p_lre), li = bf2f(p_lim);
      const float dt = rbf(expf(bf2f(p_ldt)));
      const float er = expf(dt * lr), an = dt * li;
      c_ar = er * cosf(an); c_ai = er * sinf(an);
      const float nr = c_ar - 1.f, ni = c_ai, den = lr * lr + li * li;
      const float cr = (nr * lr + ni * li) / den, ci = (ni * lr - nr * li) / den;
      c_a8r = c_ar; c_a8i = c_ai;
#pragma unroll
      for (int q = 0; q < 3; ++q) cmul(c_a8r, c_a8i, c_a8r, c_a8i);
      c_a128r = c_a8r; c_a128i = c_a8i;
#pragma unroll
      for (int q = 0; q < 4; ++q) cmul(c_a128r, c_a128i, c_a128r, c_a128i);
#pragma unroll
      for (int p = 0; p < 3; ++p) {
        const int src = p < 2 ? 16 * p + r : 32 + (r & 7);
        par[p] = __shfl(c_ar, src); pai[p] = __shfl(c_ai, src);
        pcr[p] = __shfl(cr, src);   pci[p] = __shfl(ci, src);
      }
    }
  }

  float ur[3][8], ui[3][8];
#pragma unroll
  for (int p = 0; p < 3; ++p) {
#pragma unroll
    for (int t = 0; t < 8; ++t) {
      float br, bi;
      if (p < 2) { br = accS[2 * p][t >> 2][t & 3]; bi = accS[2 * p + 1][t >> 2][t & 3]; }
      else {
        const float v = accS[4][t >> 2][t & 3];
        const float o = __builtin_amdgcn_update_dpp(0.f, v, 0x128, 0xf, 0xf, false);
        br = r < 8 ? v : o; bi = r < 8 ? o : v;
      }
      ur[p][t] = pcr[p] * br - pci[p] * bi;
      ui[p][t] = pcr[p] * bi + pci[p] * br;
    }
    float er = ur[p][0], ei = ui[p][0];
#pragma unroll
    for (int t = 1; t < 8; ++t) {
      const float nr = par[p] * er - pai[p] * ei + ur[p][t];
      ei = par[p] * ei + pai[p] * er + ui[p][t];
      er = nr;
    }
    const int chl = p < 2 ? 16 * p + r : 32 + (r & 7);
    if (p < 2 || r < 8) *(LAS f32x2*)(lds + L_E + ((4 * w + g) * 40 + chl) * 8) = f32x2{er, ei};
  }
  LSYNC();
  if (w == 1 && rb < 15) {
    if (lane < 40) {
      f32x2 e[16];
#pragma unroll
      for (int s = 0; s < 16; ++s) e[s] = *(LAS f32x2*)(lds + L_E + (s * 40 + lane) * 8);
      float er = e[0][0], ei = e[0][1];
#pragma unroll
      for (int s = 1; s < 16; ++s) {
        const float nr = c_a8r * er - c_a8i * ei + e[s][0];
        ei = c_a8r * ei + c_a8i * er + e[s][1];
        er = nr;
      }
      GAS u64* ag = (GAS u64*)(ws + WS_AGG) + (cg * 16 + rb) * 80 + 2 * lane;
      gstore(ag, er); gstore(ag + 1, ei);
    }
  }

  f32x4 accK[5][2];
#pragma unroll
  for (int t = 0; t < 5; ++t) { accK[t][0] = f32x4{0, 0, 0, 0}; accK[t][1] = f32x4{0, 0, 0, 0}; }
  const int q4 = r >> 2, pp = r & 3;
  LAS char* kv0 = lds + (8 * g + q4) * 160 + 8 * pp + 640 * (g & 1);
  LAS char* kv1 = lds + (8 * g + q4) * 160 + 8 * pp + 640 * (1 - (g & 1));
  auto read2 = [&](int ks) {
    const unsigned o = KVSLOT[ks >> 2] + (ks & 3) * 5120;
#pragma unroll
    for (int t = 0; t < 5; ++t) bq[ks & 1][t] = lds_frag_tr(kv0 + o + 32 * t, kv1 + o + 32 * t);
  };
  SYNC(15); issue_kv(4);
  read2(0);
#pragma unroll
  for (int ks = 0; ks < 20; ++ks) {
    if (ks + 1 < 20) {
      if (((ks + 1) & 3) == 0) {
        const int j = (ks + 1) >> 2;
        if (j == 1) { SYNC(15); issue_q(0); issue_q(1); }
        if (j == 2) { SYNC(16); issue_q(2); }
        if (j == 3) { SYNC(14); issue_q(3); }
        if (j == 4) { SYNC(12); issue_q(4); }
      }
      read2(ks + 1);
    }
#pragma unroll
    for (int t = 0; t < 5; ++t)
#pragma unroll
      for (int mt = 0; mt < 2; ++mt) accK[t][mt] = mfma(xa[ks][mt], bq[ks & 1][t], accK[t][mt]);
  }

  {
    bf16x8 kf[5];
#pragma unroll
    for (int t = 0; t < 5; ++t) {
      unsigned u[4];
#pragma unroll
      for (int h2 = 0; h2 < 4; ++h2) {
        const float a = accK[t][h2 >> 1][(h2 & 1) * 2], b = accK[t][h2 >> 1][(h2 & 1) * 2 + 1];
        const unsigned raw = pk2(a, b);
        if (t == 2 || t == 3) u[h2] = raw;
        else {
          const unsigned ph = pk2(phi1(blo(raw)), phi1(bhi(raw)));
          u[h2] = (t < 2 || r < 8) ? ph : raw;
        }
      }
      kf[t] = frag4(u[0], u[1], u[2], u[3]);
    }
#pragma unroll
    for (int t = 0; t < 5; ++t) *(LAS bf16x8*)(lds + A3 + (w * 5 + t) * 1024 + lane * 16) = kf[t];
    LSYNC();
    const bf16x8 ones = frag4(0x3f803f80u, 0x3f803f80u, 0x3f803f80u, 0x3f803f80u);
    GAS u64* part = (GAS u64*)(ws + WS_PART) + (cg * 16 + rb) * 848;
    auto job = [&](int ta, int tb) {
      f32x4 d = {0, 0, 0, 0};
#pragma unroll
      for (int sw = 0; sw < 4; ++sw) {
        const bf16x8 a = lds_frag(lds + A3 + (sw * 5 + ta) * 1024 + lane * 16);
        bf16x8 b = lds_frag(lds + A3 + (sw * 5 + tb) * 1024 + lane * 16);
        if (tb == 4 && r < 8) b = ones;
        d = mfma(a, b, d);
      }
      return d;
    };
    if (w < 2) {
      const int h = w;
      const f32x4 d0 = job(h, 2 + h);
      const f32x4 d1 = job(4, 2 + h);
#pragma unroll
      for (int i = 0; i < 4; ++i) {
        gstore(part + 420 * h + 20 * (4 * g + i) + r, d0[i]);
        if (g == h) gstore(part + 420 * h + 20 * (16 + i) + r, d1[i]);
      }
    } else {
      const int h = w - 2;
      const f32x4 d0 = job(h, 4);
#pragma unroll
      for (int i = 0; i < 4; ++i) {
        if ((r >> 2) == 2 + h) gstore(part + 420 * h + 20 * (4 * g + i) + 16 + (r & 3), d0[i]);
        if (r == 0) gstore(part + 420 * h + 400 + 4 * g + i, d0[i]);
      }
      if (w == 2) {
        const f32x4 d1 = job(4, 4);
#pragma unroll
        for (int i = 0; i < 4; ++i) {
          if (g < 2 && (r >> 2) == 2 + g) gstore(part + 420 * g + 20 * (16 + i) + 16 + (r & 3), d1[i]);
          if (g < 2 && r == 0) gstore(part + 420 * g + 400 + 16 + i, d1[i]);
        }
      }
    }
  }

  f32x4 accQ[3][2];
#pragma unroll
  for (int t = 0; t < 3; ++t) { accQ[t][0] = f32x4{0, 0, 0, 0}; accQ[t][1] = f32x4{0, 0, 0, 0}; }
  LAS char* q0 = lds + (8 * g + q4) * 96 + 8 * pp + 384 * (g & 1);
  LAS char* q1 = lds + (8 * g + q4) * 96 + 8 * pp + 384 * (1 - (g & 1));
  auto read3 = [&](int ks) {
    const unsigned o = QSLOT[ks >> 2] + (ks & 3) * 3072;
#pragma unroll
    for (int t = 0; t < 3; ++t) bq[ks & 1][t] = lds_frag_tr(q0 + o + 32 * t, q1 + o + 32 * t);
  };
  SYNC(0);
  read3(0);
#pragma unroll
  for (int ks = 0; ks < 20; ++ks) {
    if (ks + 1 < 20) read3(ks + 1);
#pragma unroll
    for (int t = 0; t < 3; ++t)
#pragma unroll
      for (int mt = 0; mt < 2; ++mt) accQ[t][mt] = mfma(bq[ks & 1][t], xa[ks][mt], accQ[t][mt]);
  }

  float gv[2];
#pragma unroll
  for (int mt = 0; mt < 2; ++mt) {
    const float s = rbf(accQ[2][mt][0]);
    const float gg = rbf(1.f / (1.f + __expf(-s)));
    const int tl = 32 * w + 8 * (r >> 2) + 4 * mt + (r & 3);
    if (g == 2) {
      *(LAS float*)(lds + L_G + tl * 4) = gg;
      if (cg == 0) ((float*)(ws + WS_G))[128 * rb + tl] = gg;
    }
    gv[mt] = __shfl(gg, 32 + r);
  }
  bf16x8 qf[2][2];
#pragma unroll
  for (int mt = 0; mt < 2; ++mt) {
    unsigned tl[2];
#pragma unroll
    for (int h2 = 0; h2 < 2; ++h2) {
      const unsigned raw = pk2(accQ[2][mt][2 * h2], accQ[2][mt][2 * h2 + 1]);
      const unsigned ph = pk2(phi1(blo(raw)), phi1(bhi(raw)));
      tl[h2] = g < 2 ? ph : 0u;
    }
#pragma unroll
    for (int h = 0; h < 2; ++h) {
      unsigned mn[2];
#pragma unroll
      for (int h2 = 0; h2 < 2; ++h2) {
        const unsigned raw = pk2(accQ[h][mt][2 * h2], accQ[h][mt][2 * h2 + 1]);
        mn[h2] = pk2(phi1(blo(raw)), phi1(bhi(raw)));
      }
      qf[h][mt] = frag4(mn[0], mn[1], tl[0], tl[1]);
    }
  }

  if (w == 0) {
    const int i = 53 * rb + lane;
    const bool act = lane < 53 && i < 840;
    const GAS u64* src = (const GAS u64*)(ws + WS_PART) + cg * 16 * 848 + (act ? i : 0);
    float s = 0.f;
    unsigned spins = 0;
    for (;;) {
      u64 v[16];
#pragma unroll
      for (int j = 0; j < 16; ++j) v[j] = gload(src + j * 848);
      bool ok = true;
      s = 0.f;
#pragma unroll
      for (int j = 0; j < 16; ++j) { ok &= (unsigned)(v[j] >> 32) == TAG; s += __uint_as_float((unsigned)v[j]); }
      if (__all(ok || !act)) break;
      if (spin_fail(spins, tmo)) break;
    }
    if (act) gstore((GAS u64*)(ws + WS_FIN) + cg * 848 + i, rbf(s));
  } else if (w == 1) {
    const int ch = lane < 40 ? lane : 39;
    float cr = 0.f, ci = 0.f;
    if (rb > 0) {
      const GAS u64* src = (const GAS u64*)(ws + WS_AGG) + cg * 16 * 80 + 2 * ch;
      unsigned spins = 0;
      for (;;) {
        u64 vr[15], vi[15];
#pragma unroll
        for (int j = 0; j < 15; ++j) {
          const int jj = j < rb ? j : 0;
          vr[j] = gload(src + jj * 80); vi[j] = gload(src + jj * 80 + 1);
        }
        bool ok = true;
        cr = 0.f; ci = 0.f;
#pragma unroll
        for (int j = 0; j < 15; ++j) {
          if (j < rb) {
            ok &= (unsigned)(vr[j] >> 32) == TAG && (unsigned)(vi[j] >> 32) == TAG;
            const float nr = c_a128r * cr - c_a128i * ci + __uint_as_float((unsigned)vr[j]);
            ci = c_a128r * ci + c_a128i * cr + __uint_as_float((unsigned)vi[j]);
            cr = nr;
          }
        }
        if (__all(ok)) break;
        if (spin_fail(spins, tmo)) break;
      }
    }
    if (lane < 40) {
      f32x2 e[16];
#pragma unroll
      for (int s = 0; s < 16; ++s) e[s] = *(LAS f32x2*)(lds + L_E + (s * 40 + lane) * 8);
#pragma unroll
      for (int s = 0; s < 16; ++s) {
        *(LAS f32x2*)(lds + L_E + (s * 40 + lane) * 8) = f32x2{cr, ci};
        const float nr = c_a8r * cr - c_a8i * ci + e[s][0];
        ci = c_a8r * ci + c_a8i * cr + e[s][1];
        cr = nr;
      }
    }
  }
  LSYNC();

  {
    float g2[8];
    {
      const f32x4 ga = *(LAS f32x4*)(lds + L_G + (32 * w + 8 * g) * 4), gb = *(LAS f32x4*)(lds + L_G + (32 * w + 8 * g + 4) * 4);
#pragma unroll
      for (int t = 0; t < 4; ++t) { g2[t] = 2.f * ga[t]; g2[4 + t] = 2.f * gb[t]; }
    }
    LAS char* img = lds + A0 + w * 6400;
#pragma unroll
    for (int p = 0; p < 3; ++p) {
      const int chl = p < 2 ? 16 * p + r : 32 + (r & 7);
      const f32x2 cin = *(LAS f32x2*)(lds + L_E + ((4 * w + g) * 40 + chl) * 8);
      float hr = cin[0], hi = cin[1];
      float yr[8], yi[8];
#pragma unroll
      for (int t = 0; t < 8; ++t) {
        const float nr = par[p] * hr - pai[p] * hi + ur[p][t];
        hi = par[p] * hi + pai[p] * hr + ui[p][t];
        hr = nr;
        yr[t] = hr * g2[t]; yi[t] = -hi * g2[t];
      }
#pragma unroll
      for (int mt = 0; mt < 2; ++mt) {
        const u32x2 vr = {pk2(yr[4 * mt], yr[4 * mt + 1]), pk2(yr[4 * mt + 2], yr[4 * mt + 3])};
        const u32x2 vi = {pk2(yi[4 * mt], yi[4 * mt + 1]), pk2(yi[4 * mt + 2], yi[4 * mt + 3])};
        if (p < 2) {
          *(LAS u32x2*)(img + mt * 3200 + (16 * p + r) * 40 + 8 * g) = vr;
          *(LAS u32x2*)(img + mt * 3200 + (40 + 16 * p + r) * 40 + 8 * g) = vi;
        } else {
          const int row = r < 8 ? 32 + r : 72 + (r - 8);
          *(LAS u32x2*)(img + mt * 3200 + row * 40 + 8 * g) = r < 8 ? vr : vi;
        }
      }
    }
    asm volatile("s_waitcnt lgkmcnt(0)" ::: "memory");
#pragma unroll
    for (int f = 0; f < 5; ++f) {
      const int mt = f < 4 ? (f >> 1) : (g >> 1);
      const int o8 = f < 4 ? 4 * (f & 1) + g : 8 + (g & 1);
      LAS char* p0 = img + mt * 3200 + (8 * o8 + q4) * 40 + 8 * pp;
      const bf16x8 v = lds_frag_tr(p0, p0 + 160);
      const int part = o8 >= 5, o = 5 * cg + (part ? o8 - 5 : o8);
      const int q = 16 * (o >> 3) + 8 * part + (o & 7);
      const int rt = 8 * rb + 2 * w + mt;
      *(bf16x8*)(ws + WS_A + (rt * 240 + q) * 256 + r * 16) = v;
    }
  }

  {
    const GAS u64* src = (const GAS u64*)(ws + WS_FIN) + cg * 848;
    unsigned spins = 0;
    u64 v[4];
    for (;;) {
      bool ok = true;
#pragma unroll
      for (int it = 0; it < 4; ++it) {
        const int i = tid + 256 * it;
        v[it] = gload(src + (i < 840 ? i : 0));
      }
#pragma unroll
      for (int it = 0; it < 4; ++it) ok &= (unsigned)(v[it] >> 32) == TAG;
      if (__all(ok)) break;
      if (spin_fail(spins, tmo)) break;
    }
#pragma unroll
    for (int it = 0; it < 4; ++it) {
      const int i = tid + 256 * it;
      if (i < 840) {
        const int h = i >= 420, ii = i - 420 * h;
        const int e = ii < 400 ? ii % 20 : 20, d = ii < 400 ? ii / 20 : ii - 400;
        *(LAS u16*)(lds + L_KVT + (h * 21 + e) * 40 + d * 2) = (u16)((unsigned)v[it] >> 16);
      }
    }
    LSYNC();
    f32x4 num[2][2], nt[2], den[2][2];
#pragma unroll
    for (int mt = 0; mt < 2; ++mt) nt[mt] = f32x4{0, 0, 0, 0};
#pragma unroll
    for (int h = 0; h < 2; ++h) {
      LAS char* kb = lds + L_KVT + h * 840;
      const u32x2 z2 = {0u, 0u};
      const u32x2 ml = *(LAS u32x2*)(kb + r * 40 + 8 * g), mh = *(LAS u32x2*)(kb + r * 40 + 32);
      const u32x2 tl = *(LAS u32x2*)(kb + (16 + (r & 3)) * 40 + 8 * g), th = *(LAS u32x2*)(kb + (16 + (r & 3)) * 40 + 32);
      const u32x2 dl = *(LAS u32x2*)(kb + 800 + 8 * g), dh = *(LAS u32x2*)(kb + 800 + 32);
      const bool gh = g == h, rv = (r >> 2) == h;
      const u32x2 mh2 = gh ? mh : z2, dh2 = gh ? dh : z2, tl2 = rv ? tl : z2, th2 = (rv && gh) ? th : z2;
      const bf16x8 am = frag4(ml[0], ml[1], mh2[0], mh2[1]);
      const bf16x8 at = frag4(tl2[0], tl2[1], th2[0], th2[1]);
      const bf16x8 ad = frag4(dl[0], dl[1], dh2[0], dh2[1]);
#pragma unroll
      for (int mt = 0; mt < 2; ++mt) {
        num[h][mt] = mfma(am, qf[h][mt], f32x4{0, 0, 0, 0});
        nt[mt] = mfma(at, qf[h][mt], nt[mt]);
        den[h][mt] = mfma(ad, qf[h][mt], f32x4{0, 0, 0, 0});
      }
    }
#pragma unroll
    for (int mt = 0; mt < 2; ++mt) {
      const float og = rbf(1.f - gv[mt]);
      float sc[2];
#pragma unroll
      for (int h = 0; h < 2; ++h) sc[h] = 1.f / rbf(rbf(den[h][mt][0]) + 1e-6f);
      unsigned o[4];
#pragma unroll
      for (int h = 0; h < 2; ++h)
#pragma unroll
        for (int h2 = 0; h2 < 2; ++h2) {
          const unsigned nb = pk2(num[h][mt][2 * h2], num[h][mt][2 * h2 + 1]);
          const unsigned ob = pk2(blo(nb) * sc[h], bhi(nb) * sc[h]);
          o[2 * h + h2] = pk2(blo(ob) * og, bhi(ob) * og);
        }
      const int rt = 8 * rb + 2 * w + mt;
      const u32x4 ov = {o[0], o[1], o[2], o[3]};
      *(u32x4*)(ws + WS_A + (rt * 240 + 160 + 5 * cg + g) * 256 + r * 16) = ov;
      const float st = g == 0 ? sc[0] : sc[1];
      u32x2 tv;
#pragma unroll
      for (int h2 = 0; h2 < 2; ++h2) {
        const unsigned nb = pk2(nt[mt][2 * h2], nt[mt][2 * h2 + 1]);
        const unsigned ob = pk2(blo(nb) * st, bhi(nb) * st);
        tv[h2] = pk2(blo(ob) * og, bhi(ob) * og);
      }
      if (g < 2) *(u32x2*)(ws + WS_A + (rt * 240 + 160 + 5 * cg + 4) * 256 + r * 16 + 8 * g) = tv;
    }
  }
}

__global__ __launch_bounds__(256, 1) void k2(const u16* __restrict__ x, const u16* __restrict__ cre, const u16* __restrict__ cim,
                                             const u16* __restrict__ wo, const u16* __restrict__ dskip,
                                             const char* __restrict__ ws, float* __restrict__ out) {
  extern __shared__ __attribute__((aligned(16))) char lds_[];
  LAS char* lds = (LAS char*)lds_;
  const unsigned ldsb = (unsigned)(size_t)lds;
  const int tid = threadIdx.x, lane = tid & 63;
  const int w = __builtin_amdgcn_readfirstlane(tid >> 6);
  const int xcd = blockIdx.x & 7, idx = blockIdx.x >> 3;
  const int T = 4 * xcd + (idx & 3), jt = idx >> 2;
  const int r = lane & 15, g = lane >> 4;
  constexpr unsigned SLOT = 20480;
  constexpr int NS = 7;

  unsigned coff[5];
  const u16* cbase[5];
#pragma unroll
  for (int i = 0; i < 5; ++i) {
    const int n = 5 * w + i, p = 8 * n + (lane >> 3);
    const int part = n >= 10, dd = p - 80 * part;
    cbase[i] = part ? cim : cre;
    coff[i] = (80 * jt + dd) * 1280 + (((lane & 7) ^ ((dd >> 1) & 7)) << 4);
  }
  auto issue = [&](int sg) {
    const unsigned dst = ldsb + (sg % NS) * SLOT + 5 * w * 1024;
    if (sg < 10) {
#pragma unroll
      for (int i = 0; i < 5; ++i) glds<0>(cbase[i] + sg * 64, coff[i], dst + i * 1024);
    } else {
#pragma unroll
      for (int i = 0; i < 5; ++i) {
        const int c = (5 * w + i) * 64 + lane, rho = c / 10, ch = c - 10 * rho;
        const int oc = rho >> 3, j = (rho & 7) ^ ((oc & 1) << 2);
        const int qq = 16 * (sg - 10) + oc, cgq = qq / 5, ii = qq - 5 * cgq;
        const int row = 40 * cgq + 20 * (j >> 2) + 4 * ii + (j & 3);
        glds<0>(wo, row * 1280 + jt * 160 + ch * 16, dst + i * 1024);
      }
    }
  };
  const char* abase = ws + WS_A + (size_t)(4 * T) * 240 * 256 + w * 1024 + lane * 16;
  auto loadA = [&](int sg, bf16x8 (&a)[4]) {
#pragma unroll
    for (int mt = 0; mt < 4; ++mt) a[mt] = *(const bf16x8*)(abase + mt * 240 * 256 + sg * 4096);
  };
  const int q4 = r >> 2, pp = r & 3, oc = 4 * w + g;
  LAS char* crd = lds + ((w >> 1) * 80 + r) * 128 + (((4 * (w & 1) + g) ^ ((r >> 1) & 7)) << 4);
  LAS char* wr0 = lds + (8 * oc + q4 + 4 * (g & 1)) * 160 + 8 * pp;
  LAS char* wr1 = lds + (8 * oc + q4 + 4 * (1 - (g & 1))) * 160 + 8 * pp;

  f32x4 acc[4][5];
#pragma unroll
  for (int mt = 0; mt < 4; ++mt)
#pragma unroll
    for (int nt = 0; nt < 5; ++nt) acc[mt][nt] = f32x4{0, 0, 0, 0};

  bf16x8 a[3][4];
  loadA(0, a[0]); loadA(1, a[1]);
#pragma unroll
  for (int s = 0; s < NS - 1; ++s) issue(s);
#pragma unroll
  for (int sg = 0; sg < 15; ++sg) {
    constexpr int dummy = 0; (void)dummy;
    if (sg <= 9) SYNC(25);
    else if (sg == 10) SYNC(20);
    else if (sg == 11) SYNC(15);
    else if (sg == 12) SYNC(10);
    else if (sg == 13) SYNC(5);
    else SYNC(0);
    if (sg + NS - 1 < 15) issue(sg + NS - 1);
    if (sg + 2 < 15) loadA(sg + 2, a[(sg + 2) % 3]);
    bf16x8 b[5];
    const unsigned so = (sg % NS) * SLOT;
#pragma unroll
    for (int nt = 0; nt < 5; ++nt) {
      if (sg < 10) b[nt] = lds_frag(crd + so + nt * 2048);
      else b[nt] = lds_frag_tr(wr0 + so + 32 * nt, wr1 + so + 32 * nt);
    }
#pragma unroll
    for (int nt = 0; nt < 5; ++nt)
#pragma unroll
      for (int mt = 0; mt < 4; ++mt) acc[mt][nt] = mfma(b[nt], a[sg % 3][mt], acc[mt][nt]);
  }
  LSYNC();
#pragma unroll
  for (int mt = 0; mt < 4; ++mt)
#pragma unroll
    for (int nt = 0; nt < 5; ++nt)
      *(LAS f32x4*)(lds + ((w * 64 + 16 * mt + r) * 84 + 16 * nt + 4 * g) * 4) = acc[mt][nt];
  LSYNC();
  const float* gg = (const float*)(ws + WS_G);
#pragma unroll
  for (int it = 0; it < 5; ++it) {
    const int item = tid + 256 * it, tp = item / 20, dq = item - 20 * tp;
    f32x4 s = *(LAS f32x4*)(lds + (tp * 84 + 4 * dq) * 4);
#pragma unroll
    for (int v = 1; v < 4; ++v) s += *(LAS f32x4*)(lds + ((v * 64 + tp) * 84 + 4 * dq) * 4);
    const int mt = tp >> 4, rr = tp & 15;
    const int t = 64 * T + 32 * (mt >> 1) + 8 * (rr >> 2) + 4 * (mt & 1) + (rr & 3);
    const int d = 80 * jt + 4 * dq;
    const u32x2 xv = *(const u32x2*)(x + t * 640 + d), dv = *(const u32x2*)(dskip + d);
    const float gt = gg[t];
    const unsigned s0 = pk2(blo(xv[0]) * blo(dv[0]), bhi(xv[0]) * bhi(dv[0]));
    const unsigned s1 = pk2(blo(xv[1]) * blo(dv[1]), bhi(xv[1]) * bhi(dv[1]));
    s[0] += gt * blo(s0); s[1] += gt * bhi(s0); s[2] += gt * blo(s1); s[3] += gt * bhi(s1);
    *(f32x4*)(out + t * 640 + d) = s;
  }
}

extern "C" void kernel_launch(void* const* d_in, const int* in_sizes, int n_in, void* d_out, int out_size, void* d_ws,
                              size_t ws_size, hipStream_t stream) {
  const u16* x = (const u16*)d_in[0];
  const u16* lre = (const u16*)d_in[1];
  const u16* lim = (const u16*)d_in[2];
  const u16* bre = (const u16*)d_in[3];
  const u16* bim = (const u16*)d_in[4];
  const u16* cre = (const u16*)d_in[5];
  const u16* cim = (const u16*)d_in[6];
  const u16* ldt = (const u16*)d_in[7];
  const u16* dskip = (const u16*)d_in[8];
  const u16* wqkv = (const u16*)d_in[10];
  const u16* wo = (const u16*)d_in[11];
  const u16* gw = (const u16*)d_in[12];
  constexpr int LDS1 = 7 * 20480 + 8192, LDS2 = 7 * 20480;
  hipFuncSetAttribute((const void*)k1, hipFuncAttributeMaxDynamicSharedMemorySize, LDS1);
  hipFuncSetAttribute((const void*)k2, hipFuncAttributeMaxDynamicSharedMemorySize, LDS2);
  hipMemsetAsync(d_ws, 0, WS_ZEND, stream);
  k1<<<256, 256, LDS1, stream>>>(x, lre, lim, ldt, bre, bim, wqkv, gw, (char*)d_ws);
  k2<<<256, 256, LDS2, stream>>>(x, cre, cim, wo, dskip, (const char*)d_ws, (float*)d_out);
}
```

```cpp
#include <hip/hip_runtime.h>
#include <stdint.h>

typedef unsigned short u16;
typedef unsigned long long u64;
typedef short bf16x8 __attribute__((ext_vector_type(8)));
typedef short bf16x4 __attribute__((ext_vector_type(4)));
typedef float f32x4 __attribute__((ext_vector_type(4)));
typedef float f32x2 __attribute__((ext_vector_type(2)));
typedef unsigned u32x4 __attribute__((ext_vector_type(4)));
typedef unsigned u32x2 __attribute__((ext_vector_type(2)));
typedef __bf16 bf16v2 __attribute__((ext_vector_type(2)));
#define LAS __attribute__((address_space(3)))
#define GAS __attribute__((address_space(1)))
#define DI __device__ __forceinline__

constexpr unsigned WS_CFLAG = 0;
constexpr unsigned WS_AFLAG = 1024;
constexpr unsigned WS_TMO   = 2048;
constexpr unsigned WS_EPOCH = 2304;
constexpr unsigned WS_AGG   = 4096;
constexpr unsigned WS_PART  = WS_AGG + 16 * 16 * 80 * 8;
constexpr unsigned WS_FIN   = WS_PART + 16 * 16 * 848 * 8;
constexpr unsigned WS_ZEND  = WS_FIN + 16 * 848 * 8;
constexpr unsigned WS_WKV   = (WS_ZEND + 4095) / 4096 * 4096;
constexpr unsigned WS_WQ    = WS_WKV + 16 * 102400;
constexpr unsigned WS_A     = WS_WQ + 16 * 61440;
constexpr unsigned WS_G     = WS_A + 128 * 240 * 256;
constexpr unsigned WS_STAMP = (WS_G + 8192 + 4095) / 4096 * 4096;
constexpr unsigned SPIN_CAP = 1u << 15;

DI unsigned pk2(float a, float b) { f32x2 p = {a, b}; return __builtin_bit_cast(unsigned, __builtin_convertvector(p, bf16v2)); }
DI float blo(unsigned u) { return __uint_as_float(u << 16); }
DI float bhi(unsigned u) { return __uint_as_float(u & 0xffff0000u); }
DI float rbf(float a) { return blo(pk2(a, a)); }
DI float bf2f(unsigned v) { return __uint_as_float(v << 16); }
DI bf16x8 frag4(unsigned a, unsigned b, unsigned c, unsigned d) { u32x4 v = {a, b, c, d}; return __builtin_bit_cast(bf16x8, v); }
DI f32x4 mfma(bf16x8 a, bf16x8 b, f32x4 c) { return __builtin_amdgcn_mfma_f32_16x16x32_bf16(a, b, c, 0, 0, 0); }
DI bf16x8 lds_frag(LAS char* p) { return *(LAS bf16x8*)p; }
DI bf16x8 lds_frag_tr(LAS char* p0, LAS char* p1) {
  bf16x4 a = __builtin_amdgcn_ds_read_tr16_b64_v4i16((LAS bf16x4*)p0);
  bf16x4 b = __builtin_amdgcn_ds_read_tr16_b64_v4i16((LAS bf16x4*)p1);
  return __builtin_shufflevector(a, b, 0, 1, 2, 3, 4, 5, 6, 7);
}
template <int SC1, unsigned IMM, int GOFF = 0>
DI void glds(const void* sbase, unsigned voff, unsigned ldsbase) {
  if constexpr (SC1)
    asm volatile("s_add_u32 m0, %2, %3\n\ts_nop 0\n\tglobal_load_lds_dwordx4 %0, %1 offset:%4 sc1" ::"v"(voff), "s"(sbase), "s"(ldsbase), "i"(IMM - GOFF), "i"(GOFF) : "memory", "scc");
  else
    asm volatile("s_add_u32 m0, %2, %3\n\ts_nop 0\n\tglobal_load_lds_dwordx4 %0, %1 offset:%4" ::"v"(voff), "s"(sbase), "s"(ldsbase), "i"(IMM - GOFF), "i"(GOFF) : "memory", "scc");
}
template <int V> struct IC { static constexpr int value = V; constexpr operator int() const { return V; } };
template <int I, int N, class F>
DI void sfor(F&& f) { if constexpr (I < N) { f(IC<I>{}); sfor<I + 1, N>(f); } }
DI void store16_sc1(void* sbase, unsigned voff, u32x4 v) {
  asm volatile("global_store_dwordx4 %0, %1, %2 sc1\n\ts_nop 1" ::"v"(voff), "v"(v), "s"(sbase) : "memory");
}
#define SYNC(N) do { asm volatile("s_waitcnt vmcnt(" #N ") lgkmcnt(0)\n\ts_barrier" ::: "memory"); __builtin_amdgcn_sched_barrier(0); } while (0)
#define LSYNC() do { asm volatile("s_waitcnt lgkmcnt(0)\n\ts_barrier" ::: "memory"); __builtin_amdgcn_sched_barrier(0); } while (0)
DI u64 gload(const GAS u64* p) { return __hip_atomic_load(p, __ATOMIC_RELAXED, __HIP_MEMORY_SCOPE_AGENT); }
template <int OFF>
DI void gstore(const void* sbase, unsigned voff, float v, unsigned tag) {
  const u32x2 d = {__float_as_uint(v), tag};
  asm volatile("global_store_dwordx2 %0, %1, %2 offset:%3 sc1" ::"v"(voff), "v"(d), "s"(sbase), "i"(OFF) : "memory");
}
DI u32x2 ld8(const void* sbase, unsigned voff) { u32x2 t; asm volatile("global_load_dwordx2 %0, %1, %2" : "=v"(t) : "v"(voff), "s"(sbase) : "memory"); return t; }
DI unsigned ld4_sc1(const void* sbase, unsigned voff) { unsigned t; asm volatile("global_load_dword %0, %1, %2 sc1" : "=v"(t) : "v"(voff), "s"(sbase) : "memory"); return t; }
DI bf16x8 ld16_sc1(const void* sbase, unsigned voff) { bf16x8 t; asm volatile("global_load_dwordx4 %0, %1, %2 sc1" : "=v"(t) : "v"(voff), "s"(sbase) : "memory"); return t; }
DI void store8_sc1(void* sbase, unsigned voff, u32x2 v) {
  asm volatile("global_store_dwordx2 %0, %1, %2 sc1" ::"v"(voff), "v"(v), "s"(sbase) : "memory");
}
DI void store4_sc1(void* sbase, unsigned voff, unsigned v) {
  asm volatile("global_store_dword %0, %1, %2 sc1" ::"v"(voff), "v"(v), "s"(sbase) : "memory");
}
DI bool spin_fail(unsigned& spins, GAS unsigned* tmo, unsigned tag) {
  __builtin_amdgcn_s_sleep(1);
  if (++spins > SPIN_CAP) { __hip_atomic_store(tmo, tag, __ATOMIC_RELAXED, __HIP_MEMORY_SCOPE_AGENT); return true; }
  if ((spins & 63u) == 0u)
    return (unsigned)__builtin_amdgcn_readfirstlane(__hip_atomic_load(tmo, __ATOMIC_RELAXED, __HIP_MEMORY_SCOPE_AGENT)) == tag;
  return false;
}
DI float phi1(float t) {
  return (__builtin_amdgcn_exp2f(fminf(t, 0.f) * 1.4426950408889634f) + fmaxf(t, 0.f)) + 1e-6f;
}
DI f32x2 cneg(f32x2 a) { return f32x2{-a.y, a.y}; }
DI f32x2 cfma(f32x2 a, f32x2 n, f32x2 h, f32x2 u) {
  return __builtin_elementwise_fma(n, h.yx, __builtin_elementwise_fma(a.xx, h, u));
}
DI f32x2 cmul(f32x2 a, f32x2 n, f32x2 h) { return __builtin_elementwise_fma(n, h.yx, a.xx * h); }
DI f32x2 csq(f32x2 a) { return cmul(a, cneg(a), a); }
DI unsigned phi2(float a, float b) {
  const f32x2 t = {a, b};
  const f32x2 l = t * 1.4426950408889634f;
  f32x2 e, m;
  e.x = __builtin_amdgcn_fmed3f(__builtin_amdgcn_exp2f(l.x), 0.f, 1.f);
  e.y = __builtin_amdgcn_fmed3f(__builtin_amdgcn_exp2f(l.y), 0.f, 1.f);
  m.x = fmaxf(a, 0.f); m.y = fmaxf(b, 0.f);
  const f32x2 p = (e + m) + 1e-6f;
  return pk2(p.x, p.y);
}
#define SB() __builtin_amdgcn_sched_barrier(0)
#define VWAIT(N) do { asm volatile("s_waitcnt vmcnt(" #N ")" ::: "memory"); SB(); } while (0)

#define STAMP_DECL
#define STAMP(i)
#define CSTAMP(i)
#define STAMP_OUT(ws, k)
__global__ __launch_bounds__(256, 1) void k_fused(const u16* __restrict__ x, const u16* __restrict__ lre, const u16* __restrict__ lim,
                                                  const u16* __restrict__ ldt, const u16* __restrict__ bre, const u16* __restrict__ bim,
                                                  const u16* __restrict__ wqkv, const u16* __restrict__ gw, const u16* __restrict__ cre,
                                                  const u16* __restrict__ cim, const u16* __restrict__ wo, const u16* __restrict__ dskip,
                                                  char* __restrict__ ws, float* __restrict__ out) {
  extern __shared__ __attribute__((aligned(16))) char lds_[];
  LAS char* lds = (LAS char*)lds_;
  const unsigned ldsb = (unsigned)(size_t)lds;
  asm volatile("" ::"s"(x), "s"(lre), "s"(lim), "s"(ldt), "s"(bre), "s"(bim), "s"(wqkv), "s"(gw), "s"(ws));
  const int tid = threadIdx.x, lane = tid & 63;
  const int w = __builtin_amdgcn_readfirstlane(tid >> 6);
  const int xcd = blockIdx.x & 7, idx = blockIdx.x >> 3;
  const int rb = 8 * (xcd & 1) + (idx & 7), cg = 4 * (xcd >> 1) + (idx >> 3);
  const int r = lane & 15, g = lane >> 4;
  GAS unsigned* tmo = (GAS unsigned*)(ws + WS_TMO);
  STAMP_DECL
  STAMP(0);
  CSTAMP(30);

  constexpr unsigned SLOT = 20480;
  constexpr unsigned MISC = 0, A0 = 8192, A1 = A0 + SLOT, A2 = A0 + 2 * SLOT, A3 = A0 + 3 * SLOT, B0 = A0 + 4 * SLOT, B1 = A0 + 5 * SLOT,
                     B2 = A0 + 6 * SLOT;
  constexpr unsigned L_E = MISC;
  constexpr unsigned L_G = MISC + 5120;
  constexpr unsigned L_KVT = MISC + 5632;
  constexpr unsigned ASLOT[4] = {A0, A1, A2, A3};
  constexpr unsigned BSLOT[3] = {B0, B1, B2};
  constexpr unsigned KVSLOT[5] = {B2, A2, B0, A3, A0};
  constexpr unsigned QSLOT[5] = {A1, B1, B2, A2, B0};

  unsigned xoff[4];
#pragma unroll
  for (int n = 0; n < 4; ++n) {
    const int mt = n >> 1, rr = 8 * (n & 1) + (lane >> 3);
    const int row = 128 * rb + 32 * w + 8 * (rr >> 2) + 4 * mt + (rr & 3);
    xoff[n] = row * 1280 + (((lane & 7) ^ ((rr >> 1) & 7)) << 4);
  }
  unsigned boff[5];
  const u16* bbase[5];
#pragma unroll
  for (int i = 0; i < 5; ++i) {
    const int n = 5 * w + i;
    const int tt = n >> 2, rr = 4 * (n & 3) + g;
    const int isim = tt < 4 ? (tt & 1) : (n >= 18);
    const int cbase = tt < 4 ? 16 * (tt >> 1) : 32, cmask = tt < 4 ? 15 : 7;
    const int chl = cbase + (rr & cmask);
    bbase[i] = isim ? bim : bre;
    boff[i] = (40 * cg + chl) * 1280 + ((r ^ rr) << 4);
  }
  const unsigned xdst = ldsb + 32 * w * 128, bdst = ldsb + 5 * w * 1024, qdst = ldsb + 3 * w * 1024;
  auto gx = [&](auto S, auto N) { constexpr int s = S, n = N; glds<0, ASLOT[s & 3] + n * 1024, s * 128>(x, xoff[n], xdst); };
  auto gb = [&](auto BE, auto I) { constexpr int be = BE, i = I; glds<0, BSLOT[be % 3] + i * 1024, be * 256>(bbase[i], boff[i], bdst); };
  const unsigned lin = lane * 16;
  const char* kvsrc = ws + WS_WKV + cg * 102400 + w * 5120 + 2048;
  const char* qsrc = ws + WS_WQ + cg * 61440 + w * 3072 + 1024;
  auto gkv = [&](auto J, auto I) { constexpr int j = J, i = I; glds<1, KVSLOT[j] + i * 1024, i * 1024 - 2048>(kvsrc + j * 20480, lin, bdst); };
  auto gq = [&](auto M, auto I) { constexpr int m = M, i = I; glds<1, QSLOT[m] + i * 1024, i * 1024 - 1024>(qsrc + m * 12288, lin, qdst); };

  unsigned p_lre, p_lim, p_ldt, p_ep;
  {
    asm volatile("global_load_dword %0, %1, %2 sc1" : "=v"(p_ep) : "v"(WS_EPOCH), "s"(ws) : "memory");
    const int ch = 40 * cg + (lane < 40 ? lane : 39);
    asm volatile("global_load_ushort %0, %1, %2" : "=v"(p_lre) : "v"(2 * ch), "s"(lre) : "memory");
    asm volatile("global_load_ushort %0, %1, %2" : "=v"(p_lim) : "v"(2 * ch), "s"(lim) : "memory");
    asm volatile("global_load_ushort %0, %1, %2" : "=v"(p_ldt) : "v"(2 * ch), "s"(ldt) : "memory");
  }
  sfor<0, 4>([&](auto N) { gx(IC<0>{}, N); });
  sfor<0, 5>([&](auto I) { gb(IC<0>{}, I); });
  SB();
  u32x2 cq[3][2];
  unsigned cdst[3];
  unsigned cgate[3];
#pragma unroll
  for (int it = 0; it < 3; ++it) {
    const int c = it < 2 ? tid + 256 * it : 512 + (tid & 127);
    const int row = c >> 4, j = c & 15, k = 40 * rb + row;
    const unsigned sec = (0x0009aa55u >> (2 * j)) & 3u;
    const unsigned off4 = (unsigned)(0x0475204475207520ull >> (4 * j)) & 15u;
    const unsigned tail = (0x4300u >> j) & 1u;
    const unsigned col0 = sec * 640 + 40 * cg + 4 * off4;
    const unsigned col1 = col0 + 4 + 16 * tail;
    const bool gate = j == 15;
    const unsigned o0 = gate ? (k & ~3) : k * 1920 + col0, o1 = gate ? o0 : k * 1920 + col1;
    const u16* pb = gate ? gw : wqkv;
    asm volatile("global_load_dwordx2 %0, %1, off" : "=v"(cq[it][0]) : "v"(pb + o0) : "memory");
    asm volatile("global_load_dwordx2 %0, %1, off" : "=v"(cq[it][1]) : "v"(pb + o1) : "memory");
    const int pk = k ^ (((k >> 3) & 1) << 2);
    const unsigned dq = WS_WQ + cg * 61440 + pk * 96 + (j - 10) * 16, dk = WS_WKV + cg * 102400 + pk * 160 + j * 16;
    cdst[it] = j >= 10 ? dq : dk;
    cgate[it] = gate ? 4u | (k & 3) : 0u;
  }
  SB();
  sfor<0, 4>([&](auto N) { gx(IC<1>{}, N); });
  STAMP(1);
  auto cstore = [&](auto IT) {
    constexpr int it = IT;
    u32x4 v = {cq[it][0][0], cq[it][0][1], cq[it][1][0], cq[it][1][1]};
    const unsigned d = (cgate[it] & 2) ? cq[it][0][1] : cq[it][0][0];
    const unsigned gwv = (cgate[it] & 1) ? (d >> 16) : (d & 0xffffu);
    const bool gate = cgate[it] != 0;
    v[0] = gate ? gwv : v[0]; v[1] = gate ? 0u : v[1]; v[2] = gate ? 0u : v[2]; v[3] = gate ? 0u : v[3];
    store16_sc1(ws, cdst[it], v);
  };

  f32x2 c_a, c_a8, c_a128;
  f32x2 pa[3], pn[3], pc[3];
  asm volatile("s_waitcnt vmcnt(19)" : "+v"(p_lre), "+v"(p_lim), "+v"(p_ldt), "+v"(p_ep)::"memory");
  const unsigned tag = __builtin_amdgcn_readfirstlane(p_ep) + 1u;
  {
    const float lr = bf2f(p_lre), li = bf2f(p_lim);
    const float dt = rbf(__expf(bf2f(p_ldt)));
    const float er = __expf(dt * lr);
    const float rev = __builtin_amdgcn_fractf(dt * li * 0.15915494309189535f);
    c_a = f32x2{er * __builtin_amdgcn_cosf(rev), er * __builtin_amdgcn_sinf(rev)};
    const float nr = c_a.x - 1.f, ni = c_a.y, iden = 1.f / (lr * lr + li * li);
    const float cr = (nr * lr + ni * li) * iden, ci = (ni * lr - nr * li) * iden;
    c_a8 = csq(csq(csq(c_a)));
    c_a128 = csq(csq(csq(csq(c_a8))));
#pragma unroll
    for (int p = 0; p < 3; ++p) {
      const int src = p < 2 ? 16 * p + r : 32 + (r & 7);
      pa[p] = f32x2{__shfl(c_a.x, src), __shfl(c_a.y, src)};
      pc[p] = f32x2{__shfl(cr, src), __shfl(ci, src)};
      pn[p] = cneg(pa[p]);
    }
  }
  STAMP(2);

  bf16x8 xa[20][2];
  f32x4 accS[5][2];
#pragma unroll
  for (int t = 0; t < 5; ++t) { accS[t][0] = f32x4{0, 0, 0, 0}; accS[t][1] = f32x4{0, 0, 0, 0}; }
  bf16x8 bq[2][5];
  LAS char* xrd[2];
  LAS char* brd[4];
#pragma unroll
  for (int kk = 0; kk < 2; ++kk) xrd[kk] = lds + A0 + (32 * w + r) * 128 + (((4 * kk + g) ^ ((r >> 1) & 7)) << 4);
#pragma unroll
  for (int kq = 0; kq < 4; ++kq) brd[kq] = lds + B0 + r * 256 + (((4 * kq + g) ^ r) << 4);
  auto rd1 = [&](auto KS, auto I) {
    constexpr int ks = KS, i = I;
    if constexpr (i < 2) xa[ks][i] = lds_frag(xrd[ks & 1] + (ASLOT[(ks >> 1) & 3] - A0) + i * 2048);
    else bq[ks & 1][i - 2] = lds_frag(brd[ks & 3] + (BSLOT[(ks >> 2) % 3] - B0) + (i - 2) * 4096);
  };
  unsigned cf = 0;
  const GAS unsigned* cfp = (const GAS unsigned*)(ws + WS_CFLAG) + cg * 16 + r;

  auto dma1 = [&](auto S, auto J) {
    constexpr int s = S, j = J;
    if constexpr (s == 0) {
      if constexpr (j < 4) gx(IC<2>{}, J); else if constexpr (j < 9) gb(IC<1>{}, IC<j - 4>{});
    } else if constexpr (s == 1) {
      if constexpr (j < 3) cstore(J);
      else if constexpr (j < 7) gx(IC<3>{}, IC<j - 3>{});
      else if constexpr (j < 11) gx(IC<4>{}, IC<j - 7>{});
      else if constexpr (j < 16) gb(IC<2>{}, IC<j - 11>{});
    } else if constexpr (s <= 6) {
      if constexpr (j < 4) gx(IC<s + 3>{}, J);
      else if constexpr (s <= 5) {
        constexpr int be = 2 + (s >> 1), i = (s & 1) ? j - 1 : j - 4;
        if constexpr ((s & 1) ? j < 6 : j < 7) gb(IC<be>{}, IC<i>{});
      } else if constexpr (j == 4) {
        cf = ld4_sc1(ws, WS_CFLAG + (cg * 16 + r) * 4);
      }
    } else if constexpr (s == 7) {
      const u32x2 z = {0u, tag - 1u};
      if constexpr (j < 4) store8_sc1(ws, WS_PART + ((cg * 16 + rb) * 848 + min(tid + 256 * j, 847)) * 8, z);
      else if constexpr (j == 4) store8_sc1(ws, WS_AGG + ((cg * 16 + rb) * 80 + min(tid, 79)) * 8, z);
      else if constexpr (j == 5) store8_sc1(ws, WS_FIN + (cg * 848 + min(53 * rb + min(tid, 52), 847)) * 8, z);
    } else if constexpr (s == 8) { if constexpr (j < 5) gkv(IC<0>{}, J); }
    else if constexpr (s == 9) { if constexpr (j < 5) gkv(IC<1>{}, J); }
  };

  SYNC(10);
  STAMP(3);
  sfor<0, 7>([&](auto I) { rd1(IC<0>{}, I); });
  SB();
  sfor<0, 20>([&](auto KS) {
    constexpr int ks = KS, s = (ks + 1) >> 1, first = ks & 1;
    if constexpr (first && ks + 1 < 20) {
      if constexpr (s == 1) {
        asm volatile("s_waitcnt vmcnt(9)"
                     : "+v"(cq[0][0]), "+v"(cq[0][1]), "+v"(cq[1][0]), "+v"(cq[1][1]), "+v"(cq[2][0]), "+v"(cq[2][1])::"memory");
        SB();
      }
      if constexpr (s == 2) { SYNC(16); STAMP(4); }
      if constexpr (s == 3) VWAIT(16);
      if constexpr (s == 4) {
        SYNC(13);
        if (tid == 0) __hip_atomic_store((GAS unsigned*)(ws + WS_CFLAG) + cg * 16 + rb, tag, __ATOMIC_RELAXED, __HIP_MEMORY_SCOPE_AGENT);
      }
      if constexpr (s == 5) { VWAIT(16); STAMP(5); }
      if constexpr (s == 6) SYNC(13);
      if constexpr (s == 7) VWAIT(14);
      if constexpr (s == 8) {
        asm volatile("s_waitcnt vmcnt(0)" : "+v"(cf)::"memory");
        SYNC(0);
        if (!__all(cf == tag)) {
          unsigned spins = 0;
          for (;;) {
            cf = __hip_atomic_load(cfp, __ATOMIC_RELAXED, __HIP_MEMORY_SCOPE_AGENT);
            if (__all(cf == tag)) break;
            if (spin_fail(spins, tmo, tag)) break;
          }
        }
        STAMP(6);
      }
    }
    sfor<0, 10>([&](auto I) {
      constexpr int i = I;
      accS[i >> 1][i & 1] = mfma(xa[ks][i & 1], bq[ks & 1][i >> 1], accS[i >> 1][i & 1]);
      if constexpr (ks + 1 < 20 && i < 7) rd1(IC<ks + 1>{}, I);
      if constexpr (ks == 0) { if constexpr (i < 9) dma1(IC<0>{}, I); }
      else if constexpr (ks < 19) {
        constexpr int gi = first ? i : 10 + i;
        if constexpr (s == 1) { if constexpr (gi < 16) dma1(IC<1>{}, IC<gi>{}); }
        else if constexpr (s == 8 || s == 9) { if constexpr (gi % 4 == 1) dma1(IC<s>{}, IC<gi / 4>{}); }
        else if constexpr (gi % 3 == 1) dma1(IC<s>{}, IC<gi / 3>{});
      }
      SB();
    });
  });
  STAMP(7);

  f32x2 ub[3][8];
#pragma unroll
  for (int p = 0; p < 3; ++p) {
#pragma unroll
    for (int t = 0; t < 8; ++t) {
      if (p < 2) ub[p][t] = f32x2{accS[2 * p][t >> 2][t & 3], accS[2 * p + 1][t >> 2][t & 3]};
      else {
        const float v = accS[4][t >> 2][t & 3];
        const float o = __builtin_amdgcn_update_dpp(0.f, v, 0x128, 0xf, 0xf, false);
        ub[p][t] = r < 8 ? f32x2{v, o} : f32x2{o, v};
      }
    }
    f32x2 e = ub[p][0];
#pragma unroll
    for (int t = 1; t < 8; ++t) e = cfma(pa[p], pn[p], e, ub[p][t]);
    const int chl = p < 2 ? 16 * p + r : 32 + (r & 7);
    if (p < 2 || r < 8) *(LAS f32x2*)(lds + L_E + ((4 * w + g) * 40 + chl) * 8) = e;
  }
  STAMP(8);

  f32x4 accK[5][2];
#pragma unroll
  for (int t = 0; t < 5; ++t) { accK[t][0] = f32x4{0, 0, 0, 0}; accK[t][1] = f32x4{0, 0, 0, 0}; }
  const int q4 = r >> 2, pp = r & 3;
  LAS char* kv0 = lds + (8 * g + q4) * 160 + 8 * pp + 640 * (g & 1);
  LAS char* kv1 = lds + (8 * g + q4) * 160 + 8 * pp + 640 * (1 - (g & 1));
  bf16x4 bh[2][5][2];
  LAS char* kb[2][2];
  auto base2 = [&](auto J) {
    constexpr int j = J;
    kb[j & 1][0] = kv0 + KVSLOT[j]; kb[j & 1][1] = kv1 + KVSLOT[j];
    asm volatile("" : "+v"(kb[j & 1][0]), "+v"(kb[j & 1][1]));
  };
  auto rd2 = [&](auto KS, auto I) {
    constexpr int ks = KS, i = I;
    constexpr unsigned o = (ks & 3) * 5120 + 32 * (i >> 1);
    bh[ks & 1][i >> 1][i & 1] = __builtin_amdgcn_ds_read_tr16_b64_v4i16((LAS bf16x4*)(kb[(ks >> 2) & 1][i & 1] + o));
  };
  auto cat = [](bf16x4 a, bf16x4 b) { return __builtin_shufflevector(a, b, 0, 1, 2, 3, 4, 5, 6, 7); };
  auto dma2 = [&](auto J, auto N) {
    constexpr int j = J, n = N;
    if constexpr (j == 0) { if constexpr (n < 10) gkv(IC<2 + n / 5>{}, IC<n % 5>{}); }
    else if constexpr (j == 1) { if constexpr (n < 5) gkv(IC<4>{}, N); }
    else if constexpr (j == 2) { if constexpr (n < 6) gq(IC<n / 3>{}, IC<n % 3>{}); }
    else if constexpr (j == 3) { if constexpr (n < 6) gq(IC<2 + n / 3>{}, IC<n % 3>{}); }
    else { if constexpr (n < 3) gq(IC<4>{}, N); }
  };
  base2(IC<0>{});
  SYNC(5);
  STAMP(9);
  sfor<0, 10>([&](auto I) { rd2(IC<0>{}, I); });
  SB();
  sfor<0, 20>([&](auto KS) {
    constexpr int ks = KS, j = (ks + 1) >> 2, ph = (ks + 1) & 3;
    if constexpr (ph == 0 && ks + 1 < 20) {
      base2(IC<j>{});
      if constexpr (j == 1) SYNC(10);
      if constexpr (j == 2) SYNC(10);
      if constexpr (j == 3) SYNC(11);
      if constexpr (j == 4) SYNC(12);
    }
    sfor<0, 10>([&](auto I) {
      constexpr int i = I;
      accK[i >> 1][i & 1] = mfma(xa[ks][i & 1], cat(bh[ks & 1][i >> 1][0], bh[ks & 1][i >> 1][1]), accK[i >> 1][i & 1]);
      if constexpr (ks + 1 < 20) rd2(IC<ks + 1>{}, I);
      if constexpr (j == 0) { if constexpr (i % 3 == 1 || i == 9) dma2(IC<0>{}, IC<4 * ks + (i == 9 ? 3 : i / 3)>{}); }
      else if constexpr (ph < 2 && ks < 19 && (i == 1 || i == 4 || i == 7)) dma2(IC<j>{}, IC<3 * ph + (i - 1) / 3>{});
      SB();
    });
  });
  STAMP(10);

  {
    bf16x8 kf[5];
#pragma unroll
    for (int t = 0; t < 5; ++t) {
      unsigned u[4];
#pragma unroll
      for (int h2 = 0; h2 < 4; ++h2) {
        const float a = accK[t][h2 >> 1][(h2 & 1) * 2], b = accK[t][h2 >> 1][(h2 & 1) * 2 + 1];
        if (t == 2 || t == 3) u[h2] = pk2(a, b);
        else if (t < 2) u[h2] = phi2(a, b);
        else { const unsigned ph = phi2(a, b), raw = pk2(a, b); u[h2] = r < 8 ? ph : raw; }
      }
      kf[t] = frag4(u[0], u[1], u[2], u[3]);
      *(LAS bf16x8*)(lds + A3 + (w * 5 + t) * 1024 + lane * 16) = kf[t];
    }
    SYNC(0);
    const bf16x8 ones = frag4(0x3f803f80u, 0x3f803f80u, 0x3f803f80u, 0x3f803f80u);
    const char* part = ws + WS_PART + (cg * 16 + rb) * 848 * 8;
    LAS char* st = lds + A3 + lane * 16;
    auto job = [&](LAS char* pa_, LAS char* pb_, bool onesb) {
      f32x4 d = {0, 0, 0, 0};
#pragma unroll
      for (int sw = 0; sw < 4; ++sw) {
        const bf16x8 a = lds_frag(pa_ + sw * 5120);
        bf16x8 b = lds_frag(pb_ + sw * 5120);
        if (onesb && r < 8) b = ones;
        d = mfma(a, b, d);
      }
      return d;
    };
    if (w < 2) {
      const int h = w;
      const f32x4 d0 = job(st + h * 1024, st + (2 + h) * 1024, false);
      const f32x4 d1 = job(st + 4096, st + (2 + h) * 1024, false);
      const unsigned o0 = (420 * h + 80 * g + r) * 8, o1 = (420 * h + 320 + r) * 8;
      sfor<0, 4>([&](auto I) { constexpr int i = I; gstore<160 * i>(part, o0, d0[i], tag); });
      if (g == h) sfor<0, 4>([&](auto I) { constexpr int i = I; gstore<160 * i>(part, o1, d1[i], tag); });
    } else {
      const int h = w - 2;
      const f32x4 d0 = job(st + h * 1024, st + 4096, true);
      const bool kvp = (r >> 2) == 2 + h;
      const unsigned o0 = (420 * h + (kvp ? 80 * g + 16 + (r & 3) : 400 + 4 * g)) * 8, st0 = kvp ? 160 : 8;
      if (kvp || r == 0) sfor<0, 4>([&](auto I) { constexpr int i = I; gstore<0>(part, o0 + i * st0, d0[i], tag); });
      if (w == 2) {
        const f32x4 d1 = job(st + 4096, st + 4096, true);
        const bool kq = (r >> 2) == 2 + g;
        const unsigned o1 = (420 * g + (kq ? 336 + (r & 3) : 416)) * 8, st1 = kq ? 160 : 8;
        if (g < 2 && (kq || r == 0)) sfor<0, 4>([&](auto I) { constexpr int i = I; gstore<0>(part, o1 + i * st1, d1[i], tag); });
      } else if (rb < 15) {
        if (lane < 40) {
          f32x2 e[16];
#pragma unroll
          for (int s = 0; s < 16; ++s) e[s] = *(LAS f32x2*)(lds + L_E + (s * 40 + lane) * 8);
          const f32x2 n8 = cneg(c_a8);
          f32x2 run = e[0];
#pragma unroll
          for (int s = 1; s < 16; ++s) run = cfma(c_a8, n8, run, e[s]);
          const char* ag = ws + WS_AGG + (cg * 16 + rb) * 640;
          gstore<0>(ag, lane * 16, run.x, tag); gstore<8>(ag, lane * 16, run.y, tag);
        }
      }
    }
  }
  STAMP(11);

  f32x4 accQ[3][2];
#pragma unroll
  for (int t = 0; t < 3; ++t) { accQ[t][0] = f32x4{0, 0, 0, 0}; accQ[t][1] = f32x4{0, 0, 0, 0}; }
  LAS char* q0 = lds + (8 * g + q4) * 96 + 8 * pp + 384 * (g & 1);
  LAS char* q1 = lds + (8 * g + q4) * 96 + 8 * pp + 384 * (1 - (g & 1));
  auto base3 = [&](auto M) {
    constexpr int m = M;
    kb[m & 1][0] = q0 + QSLOT[m]; kb[m & 1][1] = q1 + QSLOT[m];
    asm volatile("" : "+v"(kb[m & 1][0]), "+v"(kb[m & 1][1]));
  };
  auto rd3 = [&](auto KS, auto I) {
    constexpr int ks = KS, i = I;
    constexpr unsigned o = (ks & 3) * 3072 + 32 * (i >> 1);
    bh[ks & 1][i >> 1][i & 1] = __builtin_amdgcn_ds_read_tr16_b64_v4i16((LAS bf16x4*)(kb[(ks >> 2) & 1][i & 1] + o));
  };
  base3(IC<0>{});
  SB();
  sfor<0, 6>([&](auto I) { rd3(IC<0>{}, I); });
  SB();
  sfor<0, 20>([&](auto KS) {
    constexpr int ks = KS;
    if constexpr (((ks + 1) & 3) == 0 && ks + 1 < 20) { base3(IC<(ks + 1) / 4>{}); SB(); }
    sfor<0, 6>([&](auto I) {
      constexpr int i = I;
      accQ[i >> 1][i & 1] = mfma(cat(bh[ks & 1][i >> 1][0], bh[ks & 1][i >> 1][1]), xa[ks][i & 1], accQ[i >> 1][i & 1]);
      if constexpr (ks + 1 < 20) rd3(IC<ks + 1>{}, I);
      SB();
    });
  });
  STAMP(12);

  float gv[2];
#pragma unroll
  for (int mt = 0; mt < 2; ++mt) {
    const float s = rbf(accQ[2][mt][0]);
    const float gg = rbf(__builtin_amdgcn_rcpf(1.f + __expf(-s)));
    const int tl = 32 * w + 8 * (r >> 2) + 4 * mt + (r & 3);
    if (g == 2) {
      *(LAS float*)(lds + L_G + tl * 4) = gg;
      if (cg == 0) store4_sc1(ws, WS_G + (128 * rb + tl) * 4, __float_as_uint(gg));
    }
    gv[mt] = __shfl(gg, 32 + r);
  }
  bf16x8 qf[2][2];
#pragma unroll
  for (int mt = 0; mt < 2; ++mt) {
    unsigned tl[2];
#pragma unroll
    for (int h2 = 0; h2 < 2; ++h2) {
      const unsigned ph = phi2(accQ[2][mt][2 * h2], accQ[2][mt][2 * h2 + 1]);
      tl[h2] = g < 2 ? ph : 0u;
    }
#pragma unroll
    for (int h = 0; h < 2; ++h)
      qf[h][mt] = frag4(phi2(accQ[h][mt][0], accQ[h][mt][1]), phi2(accQ[h][mt][2], accQ[h][mt][3]), tl[0], tl[1]);
  }
  STAMP(13);

  if (w == 0) {
    const int i = 53 * rb + lane;
    const bool act = lane < 53 && i < 840;
    const GAS u64* src = (const GAS u64*)(ws + WS_PART) + cg * 16 * 848 + (act ? i : 0);
    float s = 0.f;
    unsigned spins = 0;
    for (;;) {
      u64 v[16];
#pragma unroll
      for (int j = 0; j < 16; ++j) v[j] = gload(src + j * 848);
      bool ok = true;
      s = 0.f;
#pragma unroll
      for (int j = 0; j < 16; ++j) { ok &= (unsigned)(v[j] >> 32) == tag; s += __uint_as_float((unsigned)v[j]); }
      if (__all(ok || !act)) break;
      if (spin_fail(spins, tmo, tag)) break;
    }
    if (act) gstore<0>(ws + WS_FIN + cg * 848 * 8, i * 8, s, tag);
  } else if (w == 1) {
    const int ch = lane < 40 ? lane : 39;
    f32x2 c = {0.f, 0.f};
    if (rb > 0) {
      const GAS u64* src = (const GAS u64*)(ws + WS_AGG) + cg * 16 * 80 + 2 * ch;
      const f32x2 n128 = cneg(c_a128);
      unsigned spins = 0;
      for (;;) {
        u64 vr[15], vi[15];
#pragma unroll
        for (int j = 0; j < 15; ++j) {
          const int jj = j < rb ? j : 0;
          vr[j] = gload(src + jj * 80); vi[j] = gload(src + jj * 80 + 1);
        }
        bool ok = true;
        c = f32x2{0.f, 0.f};
#pragma unroll
        for (int j = 0; j < 15; ++j) {
          if (j < rb) {
            ok &= (unsigned)(vr[j] >> 32) == tag && (unsigned)(vi[j] >> 32) == tag;
            c = cfma(c_a128, n128, c, f32x2{__uint_as_float((unsigned)vr[j]), __uint_as_float((unsigned)vi[j])});
          }
        }
        if (__all(ok)) break;
        if (spin_fail(spins, tmo, tag)) break;
      }
    }
    if (lane < 40) {
      f32x2 e[16];
#pragma unroll
      for (int s = 0; s < 16; ++s) e[s] = *(LAS f32x2*)(lds + L_E + (s * 40 + lane) * 8);
      const f32x2 n8 = cneg(c_a8);
#pragma unroll
      for (int s = 0; s < 16; ++s) {
        *(LAS f32x2*)(lds + L_E + (s * 40 + lane) * 8) = c;
        c = cfma(c_a8, n8, c, e[s]);
      }
    }
  }
  STAMP(14);
  LSYNC();
  STAMP(15);

  const int T = 4 * xcd + (idx & 3), jt = idx >> 2;
  constexpr unsigned KSL = 5120, RING = 40960;
  const unsigned wbase = ldsb + RING + w * 6 * KSL;
  LAS char* wl = lds + RING + w * 6 * KSL;
  const u16* cb = ((w & 1) ? cim : cre) + (w >> 1) * 64;
  unsigned coff[10];
#pragma unroll
  for (int n = 0; n < 10; ++n) {
    const int dd = 8 * n + (lane >> 3);
    coff[n] = (80 * jt + dd) * 1280 + (((lane & 7) ^ ((dd >> 1) & 7)) << 4);
  }
  auto gc = [&](auto M, auto N) { constexpr int m = M, n = N; glds<0, ((2 * m) % 6) * KSL + n * 1024, m * 256>(cb, coff[n], wbase); };
  sfor<0, 10>([&](auto N) { gc(IC<0>{}, N); });
  SB();

  {
    f32x2 g2[8];
    {
      const f32x4 ga = *(LAS f32x4*)(lds + L_G + (32 * w + 8 * g) * 4), gb4 = *(LAS f32x4*)(lds + L_G + (32 * w + 8 * g + 4) * 4);
#pragma unroll
      for (int t = 0; t < 4; ++t) { g2[t] = f32x2{2.f * ga[t], -2.f * ga[t]}; g2[4 + t] = f32x2{2.f * gb4[t], -2.f * gb4[t]}; }
    }
    LAS char* img = lds + A0 + w * 6400;
#pragma unroll
    for (int p = 0; p < 3; ++p) {
      const int chl = p < 2 ? 16 * p + r : 32 + (r & 7);
      f32x2 h = *(LAS f32x2*)(lds + L_E + ((4 * w + g) * 40 + chl) * 8);
      const f32x2 nc = cneg(pc[p]);
      f32x2 y[8];
#pragma unroll
      for (int t = 0; t < 8; ++t) {
        h = cfma(pa[p], pn[p], h, ub[p][t]);
        y[t] = cmul(pc[p], nc, h) * g2[t];
      }
#pragma unroll
      for (int mt = 0; mt < 2; ++mt) {
        const u32x2 vr = {pk2(y[4 * mt].x, y[4 * mt + 1].x), pk2(y[4 * mt + 2].x, y[4 * mt + 3].x)};
        const u32x2 vi = {pk2(y[4 * mt].y, y[4 * mt + 1].y), pk2(y[4 * mt + 2].y, y[4 * mt + 3].y)};
        if (p < 2) {
          *(LAS u32x2*)(img + mt * 3200 + (16 * p + r) * 40 + 8 * g) = vr;
          *(LAS u32x2*)(img + mt * 3200 + (40 + 16 * p + r) * 40 + 8 * g) = vi;
        } else {
          const int row = r < 8 ? 32 + r : 72 + (r - 8);
          *(LAS u32x2*)(img + mt * 3200 + row * 40 + 8 * g) = r < 8 ? vr : vi;
        }
      }
    }
    asm volatile("s_waitcnt lgkmcnt(0)" ::: "memory");
#pragma unroll
    for (int f = 0; f < 5; ++f) {
      const int mt = f < 4 ? (f >> 1) : (g >> 1);
      const int o8 = f < 4 ? 4 * (f & 1) + g : 8 + (g & 1);
      LAS char* p0 = img + mt * 3200 + (8 * o8 + q4) * 40 + 8 * pp;
      const bf16x8 v = lds_frag_tr(p0, p0 + 160);
      const int part = o8 >= 5, o = 5 * cg + (part ? o8 - 5 : o8);
      const int q = 16 * (o >> 3) + 8 * part + (o & 7);
      const int rt = 8 * rb + 2 * w + mt;
      store16_sc1(ws, WS_A + (rt * 240 + q) * 256 + r * 16, __builtin_bit_cast(u32x4, v));
    }
  }
  STAMP(16);

  {
    const GAS u64* src = (const GAS u64*)(ws + WS_FIN) + cg * 848;
    unsigned kdst[4];
#pragma unroll
    for (int it = 0; it < 4; ++it) {
      const int i = tid + 256 * it, h = i >= 420, ii = i - 420 * h;
      const int dd = (ii * 3277) >> 16;
      const int e = ii < 400 ? ii - 20 * dd : 20, d = ii < 400 ? dd : ii - 400;
      kdst[it] = L_KVT + (h * 21 + e) * 40 + d * 2;
    }
    unsigned spins = 0;
    u64 v[4];
    for (;;) {
      bool ok = true;
#pragma unroll
      for (int it = 0; it < 4; ++it) {
        const int i = tid + 256 * it;
        v[it] = gload(src + (i < 840 ? i : 0));
      }
#pragma unroll
      for (int it = 0; it < 4; ++it) ok &= (unsigned)(v[it] >> 32) == tag;
      if (__all(ok)) break;
      if (spin_fail(spins, tmo, tag)) break;
    }
#pragma unroll
    for (int it = 0; it < 4; ++it) {
      const float f = __uint_as_float((unsigned)v[it]);
      if (it < 3 || tid < 72) *(LAS u16*)(lds + kdst[it]) = (u16)pk2(f, f);
    }
    STAMP(17);
    LSYNC();
    f32x4 num[2][2], nt[2], den[2][2];
#pragma unroll
    for (int mt = 0; mt < 2; ++mt) nt[mt] = f32x4{0, 0, 0, 0};
#pragma unroll
    for (int h = 0; h < 2; ++h) {
      LAS char* kb = lds + L_KVT + h * 840;
      const u32x2 z2 = {0u, 0u};
      const u32x2 ml = *(LAS u32x2*)(kb + r * 40 + 8 * g), mh = *(LAS u32x2*)(kb + r * 40 + 32);
      const u32x2 tl = *(LAS u32x2*)(kb + (16 + (r & 3)) * 40 + 8 * g), th = *(LAS u32x2*)(kb + (16 + (r & 3)) * 40 + 32);
      const u32x2 dl = *(LAS u32x2*)(kb + 800 + 8 * g), dh = *(LAS u32x2*)(kb + 800 + 32);
      const bool gh = g == h, rv = (r >> 2) == h;
      const u32x2 mh2 = gh ? mh : z2, dh2 = gh ? dh : z2, tl2 = rv ? tl : z2, th2 = (rv && gh) ? th : z2;
      const bf16x8 am = frag4(ml[0], ml[1], mh2[0], mh2[1]);
      const bf16x8 at = frag4(tl2[0], tl2[1], th2[0], th2[1]);
      const bf16x8 ad = frag4(dl[0], dl[1], dh2[0], dh2[1]);
#pragma unroll
      for (int mt = 0; mt < 2; ++mt) {
        num[h][mt] = mfma(am, qf[h][mt], f32x4{0, 0, 0, 0});
        nt[mt] = mfma(at, qf[h][mt], nt[mt]);
        den[h][mt] = mfma(ad, qf[h][mt], f32x4{0, 0, 0, 0});
      }
    }
#pragma unroll
    for (int mt = 0; mt < 2; ++mt) {
      const float og = rbf(1.f - gv[mt]);
      float sc[2];
#pragma unroll
      for (int h = 0; h < 2; ++h) sc[h] = og * __builtin_amdgcn_rcpf(den[h][mt][0] + 1e-6f);
      const int rt = 8 * rb + 2 * w + mt;
      const u32x4 ov = {pk2(num[0][mt][0] * sc[0], num[0][mt][1] * sc[0]), pk2(num[0][mt][2] * sc[0], num[0][mt][3] * sc[0]),
                        pk2(num[1][mt][0] * sc[1], num[1][mt][1] * sc[1]), pk2(num[1][mt][2] * sc[1], num[1][mt][3] * sc[1])};
      store16_sc1(ws, WS_A + (rt * 240 + 160 + 5 * cg + g) * 256 + r * 16, ov);
      const float st = g == 0 ? sc[0] : sc[1];
      const u32x2 tv = {pk2(nt[mt][0] * st, nt[mt][1] * st), pk2(nt[mt][2] * st, nt[mt][3] * st)};
      if (g < 2) store8_sc1(ws, WS_A + (rt * 240 + 160 + 5 * cg + 4) * 256 + r * 16 + 8 * g, tv);
    }
  }
  STAMP(18);
  asm volatile("s_waitcnt vmcnt(0) lgkmcnt(0)\n\ts_barrier" ::: "memory");
  if (tid == 0) __hip_atomic_store((GAS unsigned*)(ws + WS_AFLAG) + rb * 16 + cg, tag, __ATOMIC_RELAXED, __HIP_MEMORY_SCOPE_AGENT);
  STAMP(19);
  sfor<0, 10>([&](auto N) { gc(IC<1>{}, N); });
  sfor<0, 10>([&](auto N) { gc(IC<2>{}, N); });
  SB();
  {
    const GAS unsigned* afp = (const GAS unsigned*)(ws + WS_AFLAG) + (T >> 1) * 16 + r;
    unsigned spins = 0;
    for (;;) {
      const unsigned f = __hip_atomic_load(afp, __ATOMIC_RELAXED, __HIP_MEMORY_SCOPE_AGENT);
      if (__all(f == tag)) break;
      if (spin_fail(spins, tmo, tag)) break;
    }
    if (blockIdx.x == 0 && tid == 0) __hip_atomic_store((GAS unsigned*)(ws + WS_EPOCH), tag, __ATOMIC_RELAXED, __HIP_MEMORY_SCOPE_AGENT);
  }
  asm volatile("s_waitcnt vmcnt(0)" ::: "memory");
  STAMP(20);

  auto gw_ = [&](auto M, auto N) {
    constexpr int m = M, n = N;
    const int c = n * 64 + lane, rho = (c * 6554) >> 16, ch = c - 10 * rho;
    const int oc = rho >> 3, j = (rho & 7) ^ ((oc & 1) << 2);
    const int qq = 4 * (4 * m + w) + oc, cgq = (qq * 13108) >> 16, ii = qq - 5 * cgq;
    const int row = 40 * cgq + 20 * (j >> 2) + 4 * ii + (j & 3);
    glds<0, ((10 + m) % 6) * KSL + n * 1024>(wo, row * 1280 + jt * 160 + ch * 16, wbase);
  };
  const char* abase = ws + WS_A + (size_t)(4 * T) * 240 * 256;
  const unsigned alane = lane * 16;
  bf16x8 a[4][4];
  auto ga = [&](auto KP) {
    constexpr int kp = KP;
    const unsigned q = kp < 10 ? 32 * (kp >> 1) + 16 * (w >> 1) + 8 * (w & 1) + 4 * (kp & 1) : 160 + 16 * (kp - 10) + 4 * w;
    const char* p = abase + q * 256;
    sfor<0, 4>([&](auto MT) {
      constexpr int mt = MT;
      a[kp & 3][mt] = ld16_sc1(p + mt * 240 * 256, alane);
    });
  };
  ga(IC<0>{}); ga(IC<1>{}); ga(IC<2>{});
  LAS char* crd[2];
#pragma unroll
  for (int kk = 0; kk < 2; ++kk) crd[kk] = wl + r * 128 + (((4 * kk + g) ^ ((r >> 1) & 7)) << 4);
  LAS char* wr0 = wl + (8 * g + q4 + 4 * (g & 1)) * 160 + 8 * pp;
  LAS char* wr1 = wl + (8 * g + q4 + 4 * (1 - (g & 1))) * 160 + 8 * pp;
  asm volatile("" : "+v"(crd[0]), "+v"(crd[1]), "+v"(wr0), "+v"(wr1));
  bf16x8 bf[2][5];
  auto rd = [&](auto KP, auto I) {
    constexpr int kp = KP, i = I;
    if constexpr (kp < 10) {
      if constexpr (i < 5) bf[kp & 1][i] = lds_frag(crd[kp & 1] + ((kp & ~1) % 6) * KSL + i * 2048);
    } else {
      bh[kp & 1][i >> 1][i & 1] = __builtin_amdgcn_ds_read_tr16_b64_v4i16((LAS bf16x4*)(((i & 1) ? wr1 : wr0) + (kp % 6) * KSL + 32 * (i >> 1)));
    }
  };
  auto bfrag = [&](auto KP, auto NT) {
    constexpr int kp = KP, nt = NT;
    if constexpr (kp < 10) return bf[kp & 1][nt];
    else return (bf16x8)__builtin_shufflevector(bh[kp & 1][nt][0], bh[kp & 1][nt][1], 0, 1, 2, 3, 4, 5, 6, 7);
  };
  f32x4 acc[4][5];
#pragma unroll
  for (int mt = 0; mt < 4; ++mt)
#pragma unroll
    for (int nt = 0; nt < 5; ++nt) acc[mt][nt] = f32x4{0, 0, 0, 0};
  u32x2 exv[5], edv[5];
  unsigned egv[5], eoff[5], elds[5];
  auto gep = [&](auto IT) {
    constexpr int it = IT;
    const int item = tid + 256 * it, tp = (item * 3277) >> 16, dq = item - 20 * tp;
    const int mt = tp >> 4, rr = tp & 15;
    const int t = 64 * T + 32 * (mt >> 1) + 8 * (rr >> 2) + 4 * (mt & 1) + (rr & 3);
    const int d = 80 * jt + 4 * dq;
    eoff[it] = t * 640 + d; elds[it] = (tp * 84 + 4 * dq) * 4;
    exv[it] = ld8(x, eoff[it] * 2);
    edv[it] = ld8(dskip, d * 2);
    egv[it] = ld4_sc1(ws, WS_G + t * 4);
  };
#define AWAIT(N, S) do { asm volatile("s_waitcnt vmcnt(" #N ") lgkmcnt(0)" : "+v"(a[S][0]), "+v"(a[S][1]), "+v"(a[S][2]), "+v"(a[S][3])::"memory"); SB(); } while (0)
  sfor<0, 5>([&](auto I) { rd(IC<0>{}, I); });
  SB();
  sfor<0, 15>([&](auto KP) {
    constexpr int kp = KP, sl = kp & 3;
    if constexpr (kp == 0 || kp == 1) AWAIT(8, sl);
    if constexpr (kp == 2 || kp == 3 || kp == 10) AWAIT(18, sl);
    if constexpr (kp == 4 || kp == 6) AWAIT(28, sl);
    if constexpr (kp == 5 || kp == 7 || kp == 8 || kp == 9 || kp == 12) AWAIT(23, sl);
    if constexpr (kp == 11) AWAIT(13, sl);
    if constexpr (kp == 13) AWAIT(19, sl);
    if constexpr (kp == 14) AWAIT(15, sl);
    if constexpr (kp == 5) STAMP(21);
    if constexpr (kp == 10) STAMP(22);
    if constexpr (kp + 3 < 15) ga(IC<kp + 3>{});
    sfor<0, 20>([&](auto I) {
      constexpr int i = I, nt = i >> 2, mt = i & 3;
      acc[mt][nt] = mfma(bfrag(KP, IC<nt>{}), a[sl][mt], acc[mt][nt]);
      if constexpr (kp + 1 < 15 && i < 10) rd(IC<kp + 1>{}, I);
      if constexpr (kp == 1 && (i & 1) == 0) gc(IC<3>{}, IC<(i >> 1)>{});
      if constexpr (kp == 3 && (i & 1) == 0) gc(IC<4>{}, IC<(i >> 1)>{});
      if constexpr (kp >= 4 && kp <= 8 && (i & 3) == 1) gw_(IC<kp - 4>{}, IC<(i >> 2)>{});
      if constexpr (kp == 11 && (i & 3) == 2) gep(IC<(i >> 2)>{});
      SB();
    });
  });
  STAMP(23);
  LSYNC();
#pragma unroll
  for (int mt = 0; mt < 4; ++mt)
#pragma unroll
    for (int nt = 0; nt < 5; ++nt)
      *(LAS f32x4*)(lds + ((w * 64 + 16 * mt + r) * 84 + 16 * nt + 4 * g) * 4) = acc[mt][nt];
  asm volatile("s_waitcnt vmcnt(0) lgkmcnt(0)\n\ts_barrier"
               : "+v"(exv[0]), "+v"(exv[1]), "+v"(exv[2]), "+v"(exv[3]), "+v"(exv[4]), "+v"(edv[0]), "+v"(edv[1]), "+v"(edv[2]), "+v"(edv[3]),
                 "+v"(edv[4]), "+v"(egv[0]), "+v"(egv[1]), "+v"(egv[2]), "+v"(egv[3]), "+v"(egv[4])::"memory");
  STAMP(24);
#pragma unroll
  for (int it = 0; it < 5; ++it) {
    f32x4 s = *(LAS f32x4*)(lds + elds[it]);
#pragma unroll
    for (int v = 1; v < 4; ++v) s += *(LAS f32x4*)(lds + elds[it] + v * 64 * 84 * 4);
    const float gt = __uint_as_float(egv[it]);
    const unsigned s0 = pk2(blo(exv[it][0]) * blo(edv[it][0]), bhi(exv[it][0]) * bhi(edv[it][0]));
    const unsigned s1 = pk2(blo(exv[it][1]) * blo(edv[it][1]), bhi(exv[it][1]) * bhi(edv[it][1]));
    s[0] += gt * blo(s0); s[1] += gt * bhi(s0); s[2] += gt * blo(s1); s[3] += gt * bhi(s1);
    *(f32x4*)(out + eoff[it]) = s;
  }
  STAMP(25);
  CSTAMP(31);
  STAMP_OUT(ws, 0);
}

extern "C" void kernel_launch(void* const* d_in, const int* in_sizes, int n_in, void* d_out, int out_size, void* d_ws,
                              size_t ws_size, hipStream_t stream) {
  const u16* x = (const u16*)d_in[0];
  const u16* lre = (const u16*)d_in[1];
  const u16* lim = (const u16*)d_in[2];
  const u16* bre = (const u16*)d_in[3];
  const u16* bim = (const u16*)d_in[4];
  const u16* cre = (const u16*)d_in[5];
  const u16* cim = (const u16*)d_in[6];
  const u16* ldt = (const u16*)d_in[7];
  const u16* dskip = (const u16*)d_in[8];
  const u16* wqkv = (const u16*)d_in[10];
  const u16* wo = (const u16*)d_in[11];
  const u16* gw = (const u16*)d_in[12];
  constexpr int LDS = 163840;
  (void)hipFuncSetAttribute((const void*)k_fused, hipFuncAttributeMaxDynamicSharedMemorySize, LDS);
  k_fused<<<256, 256, LDS, stream>>>(x, lre, lim, ldt, bre, bim, wqkv, gw, cre, cim, wo, dskip, (char*)d_ws, (float*)d_out);
}
```

```cpp
#include <hip/hip_runtime.h>
#include <stdint.h>

typedef unsigned short u16;
typedef unsigned long long u64;
typedef short bf16x8 __attribute__((ext_vector_type(8)));
typedef short bf16x4 __attribute__((ext_vector_type(4)));
typedef float f32x4 __attribute__((ext_vector_type(4)));
typedef float f32x2 __attribute__((ext_vector_type(2)));
typedef unsigned u32x4 __attribute__((ext_vector_type(4)));
typedef unsigned u32x2 __attribute__((ext_vector_type(2)));
typedef __bf16 bf16v2 __attribute__((ext_vector_type(2)));
#define LAS __attribute__((address_space(3)))
#define GAS __attribute__((address_space(1)))
#define DI __device__ __forceinline__

constexpr unsigned WS_CFLAG = 0;
constexpr unsigned WS_HFLAG = 1024;
constexpr unsigned WS_OFLAG = 3072;
constexpr unsigned WS_TMO   = 2048;
constexpr unsigned WS_EPOCH = 2304;
constexpr unsigned WS_AGG   = 4096;
constexpr unsigned WS_PART  = WS_AGG + 16 * 16 * 80 * 8;
constexpr unsigned WS_FIN   = WS_PART + 16 * 16 * 848 * 8;
constexpr unsigned WS_ZEND  = WS_FIN + 16 * 848 * 8;
constexpr unsigned WS_WKV   = (WS_ZEND + 4095) / 4096 * 4096;
constexpr unsigned WS_WQ    = WS_WKV + 16 * 102400;
constexpr unsigned WS_WO    = WS_WQ + 16 * 61440;
constexpr unsigned WS_A     = WS_WO + 8 * 20 * 5120;
constexpr unsigned WS_G     = WS_A + 128 * 240 * 256;
constexpr unsigned WS_STAMP = (WS_G + 8192 + 4095) / 4096 * 4096;
constexpr unsigned SPIN_CAP = 1u << 15;

DI unsigned pk2(float a, float b) { f32x2 p = {a, b}; return __builtin_bit_cast(unsigned, __builtin_convertvector(p, bf16v2)); }
DI float blo(unsigned u) { return __uint_as_float(u << 16); }
DI float bhi(unsigned u) { return __uint_as_float(u & 0xffff0000u); }
DI float rbf(float a) { return blo(pk2(a, a)); }
DI float bf2f(unsigned v) { return __uint_as_float(v << 16); }
DI bf16x8 frag4(unsigned a, unsigned b, unsigned c, unsigned d) { u32x4 v = {a, b, c, d}; return __builtin_bit_cast(bf16x8, v); }
DI f32x4 mfma(bf16x8 a, bf16x8 b, f32x4 c) { return __builtin_amdgcn_mfma_f32_16x16x32_bf16(a, b, c, 0, 0, 0); }
DI bf16x8 lds_frag(LAS char* p) { return *(LAS bf16x8*)p; }
DI bf16x8 lds_frag_tr(LAS char* p0, LAS char* p1) {
  bf16x4 a = __builtin_amdgcn_ds_read_tr16_b64_v4i16((LAS bf16x4*)p0);
  bf16x4 b = __builtin_amdgcn_ds_read_tr16_b64_v4i16((LAS bf16x4*)p1);
  return __builtin_shufflevector(a, b, 0, 1, 2, 3, 4, 5, 6, 7);
}
template <int SC1, unsigned IMM, int GOFF = 0>
DI void glds(const void* sbase, unsigned voff, unsigned ldsbase) {
  if constexpr (SC1)
    asm volatile("s_add_u32 m0, %2, %3\n\ts_nop 0\n\tglobal_load_lds_dwordx4 %0, %1 offset:%4 sc1" ::"v"(voff), "s"(sbase), "s"(ldsbase), "i"(IMM - GOFF), "i"(GOFF) : "memory", "scc");
  else
    asm volatile("s_add_u32 m0, %2, %3\n\ts_nop 0\n\tglobal_load_lds_dwordx4 %0, %1 offset:%4" ::"v"(voff), "s"(sbase), "s"(ldsbase), "i"(IMM - GOFF), "i"(GOFF) : "memory", "scc");
}
template <int V> struct IC { static constexpr int value = V; constexpr operator int() const { return V; } };
template <int I, int N, class F>
DI void sfor(F&& f) { if constexpr (I < N) { f(IC<I>{}); sfor<I + 1, N>(f); } }
DI void touch(const void* p, unsigned ldsdummy) {
  asm volatile("s_mov_b32 m0, %1\n\ts_nop 0\n\tglobal_load_lds_dword %0, off" ::"v"(p), "s"(ldsdummy) : "memory");
}
DI void touch_s(const void* sbase, unsigned voff, unsigned ldsdummy) {
  asm volatile("s_mov_b32 m0, %2\n\ts_nop 0\n\tglobal_load_lds_dword %0, %1" ::"v"(voff), "s"(sbase), "s"(ldsdummy) : "memory");
}
DI void touch_s_sc1(const void* sbase, unsigned voff, unsigned ldsdummy) {
  asm volatile("s_mov_b32 m0, %2\n\ts_nop 0\n\tglobal_load_lds_dword %0, %1 sc1" ::"v"(voff), "s"(sbase), "s"(ldsdummy) : "memory");
}
DI void store16_sc1(void* sbase, unsigned voff, u32x4 v) {
  asm volatile("global_store_dwordx4 %0, %1, %2 sc1\n\ts_nop 1" ::"v"(voff), "v"(v), "s"(sbase) : "memory");
}
#define SYNC(N) do { asm volatile("s_waitcnt vmcnt(%0) lgkmcnt(0)\n\ts_barrier" ::"n"(N) : "memory"); __builtin_amdgcn_sched_barrier(0); } while (0)
#define LSYNC() do { asm volatile("s_waitcnt lgkmcnt(0)\n\ts_barrier" ::: "memory"); __builtin_amdgcn_sched_barrier(0); } while (0)
DI u64 gload(const GAS u64* p) { return __hip_atomic_load(p, __ATOMIC_RELAXED, __HIP_MEMORY_SCOPE_AGENT); }
template <int OFF>
DI void gstore(const void* sbase, unsigned voff, float v, unsigned tag) {
  const u32x2 d = {__float_as_uint(v), tag};
  asm volatile("global_store_dwordx2 %0, %1, %2 offset:%3 sc1" ::"v"(voff), "v"(d), "s"(sbase), "i"(OFF) : "memory");
}
DI u32x2 ld8(const void* sbase, unsigned voff) { u32x2 t; asm volatile("global_load_dwordx2 %0, %1, %2" : "=v"(t) : "v"(voff), "s"(sbase) : "memory"); return t; }
DI u32x4 ld16(const void* sbase, unsigned voff) { u32x4 t; asm volatile("global_load_dwordx4 %0, %1, %2" : "=v"(t) : "v"(voff), "s"(sbase) : "memory"); return t; }
DI unsigned ld4_sc1(const void* sbase, unsigned voff) { unsigned t; asm volatile("global_load_dword %0, %1, %2 sc1" : "=v"(t) : "v"(voff), "s"(sbase) : "memory"); return t; }
DI bf16x8 ld16_sc1(const void* sbase, unsigned voff) { bf16x8 t; asm volatile("global_load_dwordx4 %0, %1, %2 sc1" : "=v"(t) : "v"(voff), "s"(sbase) : "memory"); return t; }
DI u32x4 ld16u_sc1(const void* sbase, unsigned voff) { u32x4 t; asm volatile("global_load_dwordx4 %0, %1, %2 sc1" : "=v"(t) : "v"(voff), "s"(sbase) : "memory"); return t; }
DI u32x2 ld8_sc1(const void* sbase, unsigned voff) { u32x2 t; asm volatile("global_load_dwordx2 %0, %1, %2 sc1" : "=v"(t) : "v"(voff), "s"(sbase) : "memory"); return t; }
DI u32x2 ld8_wait(const void* sbase, unsigned voff) { u32x2 t; asm volatile("global_load_dwordx2 %0, %1, %2 sc1\n\ts_waitcnt vmcnt(0)" : "=&v"(t) : "v"(voff), "s"(sbase) : "memory"); return t; }
DI u32x4 ld16_wait(const void* sbase, unsigned voff) { u32x4 t; asm volatile("global_load_dwordx4 %0, %1, %2 sc1\n\ts_waitcnt vmcnt(0)" : "=&v"(t) : "v"(voff), "s"(sbase) : "memory"); return t; }
DI unsigned ld4_wait(const void* sbase, unsigned voff) { unsigned t; asm volatile("global_load_dword %0, %1, %2 sc1\n\ts_waitcnt vmcnt(0)" : "=&v"(t) : "v"(voff), "s"(sbase) : "memory"); return t; }
DI const char* sptr(const void* base, unsigned off) { return (const char*)base + (unsigned)__builtin_amdgcn_readfirstlane(off); }
DI void store8_sc1(void* sbase, unsigned voff, u32x2 v) {
  asm volatile("global_store_dwordx2 %0, %1, %2 sc1" ::"v"(voff), "v"(v), "s"(sbase) : "memory");
}
DI void store4_sc1(void* sbase, unsigned voff, unsigned v) {
  asm volatile("global_store_dword %0, %1, %2 sc1" ::"v"(voff), "v"(v), "s"(sbase) : "memory");
}
DI bool spin_fail(unsigned& spins, GAS unsigned* tmo, unsigned tag) {
  __builtin_amdgcn_s_sleep(1);
  if (++spins > SPIN_CAP) { __hip_atomic_store(tmo, tag, __ATOMIC_RELAXED, __HIP_MEMORY_SCOPE_AGENT); return true; }
  if ((spins & 63u) == 0u)
    return (unsigned)__builtin_amdgcn_readfirstlane(__hip_atomic_load(tmo, __ATOMIC_RELAXED, __HIP_MEMORY_SCOPE_AGENT)) == tag;
  return false;
}
DI float phi1(float t) {
  return (__builtin_amdgcn_exp2f(fminf(t, 0.f) * 1.4426950408889634f) + fmaxf(t, 0.f)) + 1e-6f;
}
DI f32x2 cfma(f32x2 a, f32x2 h, f32x2 u) {
  f32x2 t, o;
  asm("v_pk_fma_f32 %0, %1, %2, %3 op_sel_hi:[0,1,1]" : "=v"(t) : "v"(a), "v"(h), "v"(u));
  asm("v_pk_fma_f32 %0, %1, %2, %3 op_sel:[1,1,0] op_sel_hi:[1,0,1] neg_lo:[1,0,0]" : "=v"(o) : "v"(a), "v"(h), "v"(t));
  return o;
}
DI f32x2 cfma_1(f32x2 a, f32x2 h, f32x2 u) {
  f32x2 t;
  asm("v_pk_fma_f32 %0, %1, %2, %3 op_sel_hi:[0,1,1]" : "=v"(t) : "v"(a), "v"(h), "v"(u));
  return t;
}
DI f32x2 cfma_2(f32x2 a, f32x2 h, f32x2 t) {
  f32x2 o;
  asm("v_pk_fma_f32 %0, %1, %2, %3 op_sel:[1,1,0] op_sel_hi:[1,0,1] neg_lo:[1,0,0]" : "=v"(o) : "v"(a), "v"(h), "v"(t));
  return o;
}
DI f32x2 cco_1(f32x2 a, f32x2 h) {
  f32x2 t;
  asm("v_pk_mul_f32 %0, %1, %2 op_sel_hi:[0,1] neg_hi:[1,0]" : "=v"(t) : "v"(a), "v"(h));
  return t;
}
DI f32x2 cco_2(f32x2 a, f32x2 h, f32x2 t) {
  f32x2 o;
  asm("v_pk_fma_f32 %0, %1, %2, %3 op_sel:[1,1,0] op_sel_hi:[1,0,1] neg_lo:[1,0,0] neg_hi:[1,0,0]" : "=v"(o) : "v"(a), "v"(h), "v"(t));
  return o;
}
DI f32x2 cmul(f32x2 a, f32x2 h) {
  f32x2 t, o;
  asm("v_pk_mul_f32 %0, %1, %2 op_sel:[1,1] op_sel_hi:[1,0]" : "=v"(t) : "v"(a), "v"(h));
  asm("v_pk_fma_f32 %0, %1, %2, %3 op_sel_hi:[0,1,1] neg_lo:[0,0,1]" : "=v"(o) : "v"(a), "v"(h), "v"(t));
  return o;
}
DI f32x2 cmul_conj_out(f32x2 a, f32x2 h) {
  f32x2 t, o;
  asm("v_pk_mul_f32 %0, %1, %2 op_sel_hi:[0,1] neg_hi:[1,0]" : "=v"(t) : "v"(a), "v"(h));
  asm("v_pk_fma_f32 %0, %1, %2, %3 op_sel:[1,1,0] op_sel_hi:[1,0,1] neg_lo:[1,0,0] neg_hi:[1,0,0]" : "=v"(o) : "v"(a), "v"(h), "v"(t));
  return o;
}
DI f32x2 csq(f32x2 a) { return cmul(a, a); }
DI unsigned phi2(float a, float b) {
  const f32x2 t = {a, b};
  const f32x2 l = t * 1.4426950408889634f;
  f32x2 e, m;
  e.x = __builtin_amdgcn_fmed3f(__builtin_amdgcn_exp2f(l.x), 0.f, 1.f);
  e.y = __builtin_amdgcn_fmed3f(__builtin_amdgcn_exp2f(l.y), 0.f, 1.f);
  m.x = __builtin_amdgcn_fmed3f(a, 0.f, __builtin_inff()); m.y = __builtin_amdgcn_fmed3f(b, 0.f, __builtin_inff());
  const f32x2 p = (e + m) + 1e-6f;
  return pk2(p.x, p.y);
}
#define TOUCH1 1
#define TOUCH2 0
#define MAP48 1
#define TOUCHD 0
#define TOUCHA 0
constexpr int TAN = TOUCHA ? 1 : 0;
constexpr int TDN = TOUCHD ? 1 : 0, T1N = 0, T2N = TOUCH2 ? 2 : 0, TCN = TOUCH1 ? 1 : 0;
#define SB() __builtin_amdgcn_sched_barrier(0)
#define VWAIT(N) do { asm volatile("s_waitcnt vmcnt(%0)" ::"n"(N) : "memory"); SB(); } while (0)

#define STAMP_DECL
#define STAMP(i)
#define CSTAMP(i)
#define STAMP_OUT(ws, k)
__global__ __launch_bounds__(256, 1) void k_fused(const u16* __restrict__ x, const u16* __restrict__ lre, const u16* __restrict__ lim,
                                                  const u16* __restrict__ ldt, const u16* __restrict__ bre, const u16* __restrict__ bim,
                                                  const u16* __restrict__ wqkv, const u16* __restrict__ gw, const u16* __restrict__ cre,
                                                  const u16* __restrict__ cim, const u16* __restrict__ wo, const u16* __restrict__ dskip,
                                                  char* __restrict__ ws, float* __restrict__ out) {
  extern __shared__ __attribute__((aligned(16))) char lds_[];
  LAS char* lds = (LAS char*)lds_;
  const unsigned ldsb = (unsigned)(size_t)lds;
  asm volatile("" ::"s"(x), "s"(lre), "s"(lim), "s"(ldt), "s"(bre), "s"(bim), "s"(wqkv), "s"(gw), "s"(ws));
  const int tid = threadIdx.x, lane = tid & 63;
  const int w = __builtin_amdgcn_readfirstlane(tid >> 6);
  const int xcd = blockIdx.x & 7, idx = blockIdx.x >> 3;
  const int rb = 4 * (xcd & 3) + (idx & 3), cg = 8 * (xcd >> 2) + (idx >> 2);
  const int r = lane & 15, g = lane >> 4;
  GAS unsigned* tmo = (GAS unsigned*)(ws + WS_TMO);
  STAMP_DECL
  STAMP(0);
  CSTAMP(30);

  constexpr unsigned SLOT = 20480;
  constexpr unsigned MISC = 0, A0 = 10240, A1 = A0 + SLOT, A2 = A0 + 2 * SLOT, A3 = A0 + 3 * SLOT, B0 = A0 + 4 * SLOT, B1 = A0 + 5 * SLOT,
                     B2 = A0 + 6 * SLOT;
  constexpr unsigned L_E = MISC;
  constexpr unsigned L_G = MISC + 5120;
  constexpr unsigned L_KVT = MISC + 5632;
  constexpr unsigned L_DUMMY = MISC + 7424;
  constexpr unsigned L_H = MISC + 7680;
  constexpr unsigned L_C = MISC + 8704;
  constexpr unsigned ASLOT[4] = {A0, A1, A2, A3};
  constexpr unsigned BSLOT[3] = {B0, B1, B2};
  constexpr unsigned KVSLOT[5] = {B2, A2, B0, A3, A0};
  constexpr unsigned QSLOT[5] = {B1, B1 + 12288, B1 + 24576, B1 + 36864, B0};

  unsigned xoff[2];
  {
    const int l3 = lane >> 3;
    const unsigned row = 128 * rb + 32 * w + 8 * (l3 >> 2) + (l3 & 3), sw = ((lane & 7) ^ (l3 >> 1)) << 4;
    xoff[0] = row * 1280 + sw;
    xoff[1] = (row + 16) * 1280 + (sw ^ 64);
  }
  const char* const xb1 = (const char*)x + 4 * 1280;
  unsigned boff[5];
  const u16* bbase[5];
#pragma unroll
  for (int i = 0; i < 5; ++i) {
    const int n = 5 * w + i;
    const int tt = n >> 2, rr = 4 * (n & 3) + g;
    const int isim = tt < 4 ? (tt & 1) : (n >= 18);
    const int cbase = tt < 4 ? 16 * (tt >> 1) : 32, cmask = tt < 4 ? 15 : 7;
    const int chl = cbase + (rr & cmask);
    bbase[i] = isim ? bim : bre;
    boff[i] = (40 * cg + chl) * 1280 + ((r ^ rr) << 4);
  }
  const unsigned xdst = ldsb + 32 * w * 128, bdst = ldsb + 5 * w * 1024, qdst = ldsb + 3 * w * 1024;
  auto gx = [&](auto S, auto N) { constexpr int s = S, n = N; glds<0, ASLOT[s & 3] + n * 1024, s * 128>(n < 2 ? (const char*)x : xb1, xoff[n & 1], xdst); };
  auto gb = [&](auto BE, auto I) { constexpr int be = BE, i = I; glds<0, BSLOT[be % 3] + i * 1024, be * 256>(bbase[i], boff[i], bdst); };
  const unsigned lin = lane * 16;
  const char* kvsrc = ws + WS_WKV + cg * 102400 + w * 5120 + 2048;
  const char* qsrc = ws + WS_WQ + cg * 61440 + w * 3072 + 1024;
  auto gkv = [&](auto J, auto I) { constexpr int j = J, i = I; glds<1, KVSLOT[j] + i * 1024, i * 1024 - 2048>(kvsrc + j * 20480, lin, bdst); };
  auto gq = [&](auto M, auto I) { constexpr int m = M, i = I; glds<1, QSLOT[m] + i * 1024, i * 1024 - 1024>(qsrc + m * 12288, lin, qdst); };

  unsigned p_lre, p_lim, p_ldt, p_ep, p_gw;
  {
    asm volatile("global_load_dword %0, %1, %2 sc1" : "=v"(p_ep) : "v"(WS_EPOCH), "s"(ws) : "memory");
    const int l39 = lane < 40 ? lane : 39, ch = 40 * cg + l39;
    asm volatile("global_load_ushort %0, %1, %2" : "=v"(p_lre) : "v"(2 * ch), "s"(lre) : "memory");
    asm volatile("global_load_ushort %0, %1, %2" : "=v"(p_lim) : "v"(2 * ch), "s"(lim) : "memory");
    asm volatile("global_load_ushort %0, %1, %2" : "=v"(p_ldt) : "v"(2 * ch), "s"(ldt) : "memory");
    asm volatile("global_load_ushort %0, %1, %2" : "=v"(p_gw) : "v"(2 * (40 * rb + l39)), "s"(gw) : "memory");
  }
  sfor<0, 4>([&](auto N) { gx(IC<0>{}, N); });
  sfor<0, 5>([&](auto I) { gb(IC<0>{}, I); });
  SB();
  u32x4 cq[3];
  u32x2 ct[3];
  unsigned csrc[3], cdst[3];
  const int cj = tid & 15, crow0 = tid >> 4;
  const bool ctail = (0x4300u >> cj) & 1u, cgate = cj == 15;
  const int crow[3] = {crow0, crow0 + 16, 32 + (crow0 & 7)};
  sfor<0, 4>([&](auto N) { gx(IC<1>{}, N); });
  {
    const unsigned i = idx * 4 + w;
    u64 pc;
    unsigned clen;
    asm volatile("s_getpc_b64 %0\n.Lkf_pc:\n\ts_mov_b32 %1, .Lkf_end-.Lkf_pc" : "=s"(pc), "=s"(clen));
    const char* cp = (const char*)pc + (min(i * 384 + (lane & 3) * 128, clen - 4u) & ~3u);
    const char* tp = (lane & 1) ? (const char*)out + min(i, 79u) * 65536 : ws + WS_A + min(i, 119u) * 65536;
    touch(lane >= 4 && lane < 7 ? cp : tp, ldsb + L_DUMMY);
  }
  {
    const unsigned sec = (0x0009aa55u >> (2 * cj)) & 3u;
    const unsigned off4 = (unsigned)(0x0475204475207520ull >> (4 * cj)) & 15u;
    const unsigned colb = 2 * (sec * 640 + 40 * cg + 4 * off4) + 40 * rb * 3840;
    const unsigned rbytes = cj >= 10 ? 96 : 160;
    const unsigned dbase = cj >= 10 ? WS_WQ + cg * 61440 + (cj - 10) * 16 : WS_WKV + cg * 102400 + cj * 16;
#pragma unroll
    for (int it = 0; it < 3; ++it) {
      const unsigned k = 40 * rb + crow[it];
      csrc[it] = colb + crow[it] * 3840;
      cdst[it] = dbase + (k ^ (((k >> 3) & 1) << 2)) * rbytes;
    }
    asm volatile("" : "+v"(csrc[0]), "+v"(csrc[1]), "+v"(csrc[2]), "+v"(cdst[0]), "+v"(cdst[1]), "+v"(cdst[2]));
  }
  auto cload = [&](auto N) {
    constexpr int n = N, it = n >> 1;
    if constexpr (n & 1) ct[it] = ld8(wqkv, ctail ? csrc[it] + 40 : 0u);
    else cq[it] = ld16(wqkv, csrc[it]);
  };
  auto cstore = [&](auto IT) {
    constexpr int it = IT;
    u32x4 v = cq[it];
    const unsigned gwv = __shfl(p_gw, crow[it]);
    v[0] = cgate ? gwv : v[0]; v[1] = cgate ? 0u : v[1];
    v[2] = ctail ? ct[it][0] : (cgate ? 0u : v[2]); v[3] = ctail ? ct[it][1] : (cgate ? 0u : v[3]);
    store16_sc1(ws, cdst[it], v);
  };
  STAMP(1);

  f32x2 c_a, c_a8;
  float c_lmag, c_rev;
  f32x2 pa[3], pc[3], pw[3];
  f32x2 pj[4];
  asm volatile("s_waitcnt vmcnt(%5)" : "+v"(p_lre), "+v"(p_lim), "+v"(p_ldt), "+v"(p_ep), "+v"(p_gw) : "n"(13 + TCN) : "memory");
  const unsigned tag = __builtin_amdgcn_readfirstlane(p_ep) + 1u;
  {
    const float lr = bf2f(p_lre), li = bf2f(p_lim);
    const float dt = rbf(__expf(bf2f(p_ldt)));
    c_lmag = dt * lr * 1.4426950408889634f; c_rev = dt * li * 0.15915494309189535f;
    const float er = __builtin_amdgcn_exp2f(c_lmag), rev = __builtin_amdgcn_fractf(c_rev);
    c_a = f32x2{er * __builtin_amdgcn_cosf(rev), er * __builtin_amdgcn_sinf(rev)};
    const float nr = c_a.x - 1.f, ni = c_a.y, iden = 1.f / (lr * lr + li * li);
    const float cr = (nr * lr + ni * li) * iden, ci = (ni * lr - nr * li) * iden;
    c_a8 = csq(csq(csq(c_a)));
#pragma unroll
    for (int p = 0; p < 3; ++p) {
      const int src = p < 2 ? 16 * p + r : 32 + (r & 7);
      pa[p] = f32x2{__shfl(c_a.x, src), __shfl(c_a.y, src)};
      pc[p] = f32x2{2.f * __shfl(cr, src), 2.f * __shfl(ci, src)};
    }
    asm volatile("" : "+v"(pa[0]), "+v"(pa[1]), "+v"(pa[2]), "+v"(pc[0]), "+v"(pc[1]), "+v"(pc[2]), "+v"(c_a8),
                 "+v"(c_lmag), "+v"(c_rev));
  }
  STAMP(2);

  bf16x8 xa[20][2];
  f32x4 accS[5][2];
#pragma unroll
  for (int t = 0; t < 5; ++t) { accS[t][0] = f32x4{0, 0, 0, 0}; accS[t][1] = f32x4{0, 0, 0, 0}; }
  bf16x8 bq[2][5];
  LAS char* xrd[2];
  LAS char* brd[4];
#pragma unroll
  for (int kk = 0; kk < 2; ++kk) xrd[kk] = lds + A0 + (32 * w + r) * 128 + (((4 * kk + g) ^ ((r >> 1) & 7)) << 4);
#pragma unroll
  for (int kq = 0; kq < 4; ++kq) brd[kq] = lds + B0 + r * 256 + (((4 * kq + g) ^ r) << 4);
  auto rd1 = [&](auto KS, auto I) {
    constexpr int ks = KS, i = I;
    if constexpr (i < 2) xa[ks][i] = lds_frag(xrd[ks & 1] + (ASLOT[(ks >> 1) & 3] - A0) + i * 2048);
    else bq[ks & 1][i - 2] = lds_frag(brd[ks & 3] + (BSLOT[(ks >> 2) % 3] - B0) + (i - 2) * 4096);
  };
  unsigned cf = 0;
  const GAS unsigned* cfp = (const GAS unsigned*)(ws + WS_CFLAG) + cg * 16 + r;

  auto dma1 = [&](auto S, auto J) {
    constexpr int s = S, j = J;
    if constexpr (s == 0) {
      if constexpr (j < 4) gx(IC<2>{}, J); else if constexpr (j < 9) gb(IC<1>{}, IC<j - 4>{}); else if constexpr (j < 15) cload(IC<j - 9>{});
    } else if constexpr (s == 1) {
      if constexpr (j < 4) gx(IC<3>{}, J);
      else if constexpr (j < 8) gx(IC<4>{}, IC<j - 4>{});
      else if constexpr (j < 13) gb(IC<2>{}, IC<j - 8>{});
    } else if constexpr (s == 2 && j >= 100) {
      cstore(IC<j - 100>{});
    } else if constexpr (s <= 6) {
      if constexpr (j < 4) gx(IC<s + 3>{}, J);
      else if constexpr (s <= 5) {
        constexpr int be = 2 + (s >> 1), i = (s & 1) ? j - 1 : j - 4;
        if constexpr ((s & 1) ? j < 6 : j < 7) gb(IC<be>{}, IC<i>{});
      } else if constexpr (j == 4) {
        cf = ld4_sc1(ws, WS_CFLAG + (cg * 16 + r) * 4);
      }
    } else if constexpr (s == 7) {
      const u32x2 z = {0u, tag - 1u};
      if constexpr (j < 4) store8_sc1(ws, WS_PART + ((cg * 16 + rb) * 848 + min(tid + 256 * j, 847)) * 8, z);
      else if constexpr (j == 4) store8_sc1(ws, WS_AGG + ((cg * 16 + rb) * 80 + min(tid, 79)) * 8, z);
      else if constexpr (j == 5) store8_sc1(ws, WS_FIN + (cg * 848 + min(53 * rb + min(tid, 52), 847)) * 8, z);
    } else if constexpr (s == 8) { if constexpr (j < 5) gkv(IC<0>{}, J); }
    else if constexpr (s == 9) { if constexpr (j < 5) gkv(IC<1>{}, J); }
  };

  SYNC(4 + TCN);
  STAMP(3);
  sfor<0, 7>([&](auto I) { rd1(IC<0>{}, I); });
  SB();
  sfor<0, 20>([&](auto KS) {
    constexpr int ks = KS, s = (ks + 1) >> 1, first = ks & 1;
    if constexpr (first && ks + 1 < 20) {
      if constexpr (s == 1) VWAIT(15 + TCN);
      if constexpr (s == 2) {
        asm volatile("s_waitcnt vmcnt(13)" : "+v"(cq[0]), "+v"(cq[1]), "+v"(cq[2]), "+v"(ct[0]), "+v"(ct[1]), "+v"(ct[2])::"memory");
        SYNC(13);
        sfor<0, 3>([&](auto IT) { dma1(IC<2>{}, IC<100 + IT>{}); });
        STAMP(4);
      }
      if constexpr (s == 3) VWAIT(19);
      if constexpr (s == 4) {
        SYNC(13);
        if (tid == 0) __hip_atomic_store((GAS unsigned*)(ws + WS_CFLAG) + cg * 16 + rb, tag, __ATOMIC_RELAXED, __HIP_MEMORY_SCOPE_AGENT);
      }
      if constexpr (s == 5) { VWAIT(16); STAMP(5); }
      if constexpr (s == 6) SYNC(13);
      if constexpr (s == 7) VWAIT(14);
      if constexpr (s == 8) {
        asm volatile("s_waitcnt vmcnt(6)" : "+v"(cf)::"memory");
        SYNC(6);
        if (!__all(cf == tag)) {
          unsigned spins = 0;
          for (;;) {
            cf = __hip_atomic_load(cfp, __ATOMIC_RELAXED, __HIP_MEMORY_SCOPE_AGENT);
            if (__all(cf == tag)) break;
            if (spin_fail(spins, tmo, tag)) break;
          }
        }
        STAMP(6);
      }
    }
    sfor<0, 10>([&](auto I) {
      constexpr int i = I;
      accS[i >> 1][i & 1] = mfma(xa[ks][i & 1], bq[ks & 1][i >> 1], accS[i >> 1][i & 1]);
      if constexpr (ks + 1 < 20 && i < 7) rd1(IC<ks + 1>{}, I);
      if constexpr (ks == 0) { if constexpr (i < 9) dma1(IC<0>{}, I); else sfor<9, 15>([&](auto J) { dma1(IC<0>{}, J); }); }
      else if constexpr (ks < 19) {
        constexpr int gi = first ? i : 10 + i;
        if constexpr (s == 1) { if constexpr (gi < 13) dma1(IC<1>{}, IC<gi>{}); }
        else if constexpr (s == 8 || s == 9) { if constexpr (gi % 4 == 1) dma1(IC<s>{}, IC<gi / 4>{}); }
        else if constexpr (gi % 3 == 1) dma1(IC<s>{}, IC<gi / 3>{});
      }
      SB();
    });
  });
  STAMP(7);

  f32x2 ub[3][8];
  {
#pragma unroll
    for (int t = 0; t < 8; ++t) {
      ub[0][t] = f32x2{accS[0][t >> 2][t & 3], accS[1][t >> 2][t & 3]};
      ub[1][t] = f32x2{accS[2][t >> 2][t & 3], accS[3][t >> 2][t & 3]};
      const float v = accS[4][t >> 2][t & 3];
      const float o = __builtin_amdgcn_update_dpp(0.f, v, 0x128, 0xf, 0xf, false);
      ub[2][t] = r < 8 ? f32x2{v, o} : f32x2{o, v};
    }
    f32x2 e[3] = {ub[0][0], ub[1][0], ub[2][0]};
    SB();
#pragma unroll
    for (int t = 1; t < 8; ++t) {
      f32x2 t1[3];
#pragma unroll
      for (int p = 0; p < 3; ++p) t1[p] = cfma_1(pa[p], e[p], ub[p][t]);
      SB();
#pragma unroll
      for (int p = 0; p < 3; ++p) e[p] = cfma_2(pa[p], e[p], t1[p]);
      SB();
    }
    LAS char* ep = lds + L_E + ((4 * w + g) * 40 + r) * 8;
    *(LAS f32x2*)ep = e[0];
    *(LAS f32x2*)(ep + 128) = e[1];
    if (r < 8) *(LAS f32x2*)(ep + 256) = e[2];
  }
  STAMP(8);

  f32x4 accK[5][2];
#pragma unroll
  for (int t = 0; t < 5; ++t) { accK[t][0] = f32x4{0, 0, 0, 0}; accK[t][1] = f32x4{0, 0, 0, 0}; }
  const int q4 = r >> 2, pp = r & 3;
  LAS char* kv0 = lds + (8 * g + q4) * 160 + 8 * pp + 640 * (g & 1);
  LAS char* kv1 = lds + (8 * g + q4) * 160 + 8 * pp + 640 * (1 - (g & 1));
  bf16x4 bh[2][5][2];
  LAS char* kb[2][2];
  auto base2 = [&](auto J) {
    constexpr int j = J;
    kb[j & 1][0] = kv0 + KVSLOT[j]; kb[j & 1][1] = kv1 + KVSLOT[j];
    asm volatile("" : "+v"(kb[j & 1][0]), "+v"(kb[j & 1][1]));
  };
  auto rd2 = [&](auto KS, auto I) {
    constexpr int ks = KS, i = I;
    constexpr unsigned o = (ks & 3) * 5120 + 32 * (i >> 1);
    bh[ks & 1][i >> 1][i & 1] = __builtin_amdgcn_ds_read_tr16_b64_v4i16((LAS bf16x4*)(kb[(ks >> 2) & 1][i & 1] + o));
  };
  auto cat = [](bf16x4 a, bf16x4 b) { return __builtin_shufflevector(a, b, 0, 1, 2, 3, 4, 5, 6, 7); };
  const unsigned t2l = min(lane, 49), t2off = (20 * idx + 10 * (w >> 1) + ((t2l * 6554) >> 16)) * 1280 + (t2l - 10 * ((t2l * 6554) >> 16)) * 128;
  const char* t2p = (const char*)((w & 1) ? cim : cre);
  u32x4 cw;
  unsigned cwsrc, cwdst;
  {
    const unsigned sc = 200 * (16 * rb + cg) + min(tid, 199);
    cwsrc = sc * 16;
    const unsigned k = (sc * 52429u) >> 22, cc = sc - 80 * k, jw = (cc * 6554) >> 16, ch = cc - 10 * jw;
    const unsigned cgq = (k * 1639) >> 16, rem = k - 40 * cgq, hh = rem >= 20, e = rem - 20 * hh;
    const unsigned j = 4 * hh + (e & 3), qq = 5 * cgq + (e >> 2), oc = qq & 3;
    cwdst = WS_WO + ((jw * 20 + (qq >> 2)) * 32 + 8 * oc + (j ^ ((oc & 1) << 2))) * 160 + ch * 16;
  }
  auto dma2 = [&](auto J, auto N) {
    constexpr int j = J, n = N;
    if constexpr (j == 0) { if constexpr (n < 10) gkv(IC<2 + n / 5>{}, IC<n % 5>{}); else if constexpr (n == 10) cw = ld16(wo, cwsrc); }
    else if constexpr (j == 1) {
      if constexpr (n < 5) gkv(IC<4>{}, N);
      else if constexpr (n < 5 + T2N) touch_s(t2p + (n - 5) * 6400, t2off, ldsb + L_DUMMY);
    }
    else if constexpr (j == 2) { if constexpr (n < 6) gq(IC<n / 3>{}, IC<n % 3>{}); }
    else if constexpr (j == 3) { if constexpr (n < 6) gq(IC<2 + n / 3>{}, IC<n % 3>{}); else if constexpr (n == 6) store16_sc1(ws, cwdst, cw); }
    else { if constexpr (n < 3) gq(IC<4>{}, N); }
  };
  base2(IC<0>{});
  SYNC(5);
  STAMP(9);
  sfor<0, 10>([&](auto I) { rd2(IC<0>{}, I); });
  SB();
  sfor<0, 20>([&](auto KS) {
    constexpr int ks = KS, j = (ks + 1) >> 2, ph = (ks + 1) & 3;
    if constexpr (ph == 0 && ks + 1 < 20) {
      base2(IC<j>{});
      if constexpr (j == 1) SYNC(11);
      if constexpr (j == 2) SYNC(11 + T2N);
      if constexpr (j == 3) { asm volatile("s_waitcnt vmcnt(%1)" : "+v"(cw) : "n"(11 + T2N) : "memory"); SYNC(11 + T2N); }
      if constexpr (j == 4) SYNC(13 + T2N);
    }
    sfor<0, 10>([&](auto I) {
      constexpr int i = I;
      accK[i >> 1][i & 1] = mfma(xa[ks][i & 1], cat(bh[ks & 1][i >> 1][0], bh[ks & 1][i >> 1][1]), accK[i >> 1][i & 1]);
      if constexpr (ks + 1 < 20) rd2(IC<ks + 1>{}, I);
      if constexpr (j == 0) { if constexpr (i % 3 == 1 || i == 9) dma2(IC<0>{}, IC<4 * ks + (i == 9 ? 3 : i / 3)>{}); }
      else if constexpr (ph < 3 && ks < 19 && (i == 1 || i == 4 || i == 7)) dma2(IC<j>{}, IC<3 * ph + (i - 1) / 3>{});
      SB();
    });
  });
  STAMP(10);

  {
    bf16x8 kf[5];
#pragma unroll
    for (int t = 0; t < 5; ++t) {
      unsigned u[4];
#pragma unroll
      for (int h2 = 0; h2 < 4; ++h2) {
        const float a = accK[t][h2 >> 1][(h2 & 1) * 2], b = accK[t][h2 >> 1][(h2 & 1) * 2 + 1];
        if (t == 2 || t == 3) u[h2] = pk2(a, b);
        else if (t < 2) u[h2] = phi2(a, b);
        else { const unsigned ph = phi2(a, b), raw = pk2(a, b); u[h2] = r < 8 ? ph : raw; }
      }
      kf[t] = frag4(u[0], u[1], u[2], u[3]);
      *(LAS bf16x8*)(lds + A3 + (w * 5 + t) * 1024 + lane * 16) = kf[t];
    }
    SYNC(0);
    const bf16x8 ones = frag4(0x3f803f80u, 0x3f803f80u, 0x3f803f80u, 0x3f803f80u);
    const char* part = ws + WS_PART + (cg * 16 + rb) * 848 * 8;
    LAS char* st = lds + A3 + lane * 16;
    auto job = [&](LAS char* pa_, LAS char* pb_, bool onesb) {
      f32x4 d = {0, 0, 0, 0};
#pragma unroll
      for (int sw = 0; sw < 4; ++sw) {
        const bf16x8 a = lds_frag(pa_ + sw * 5120);
        bf16x8 b = lds_frag(pb_ + sw * 5120);
        if (onesb && r < 8) b = ones;
        d = mfma(a, b, d);
      }
      return d;
    };
    if (w < 2) {
      const int h = w;
      const f32x4 d0 = job(st + h * 1024, st + (2 + h) * 1024, false);
      const f32x4 d1 = job(st + 4096, st + (2 + h) * 1024, false);
      const unsigned o0 = (420 * h + 80 * g + r) * 8, o1 = (420 * h + 320 + r) * 8;
      sfor<0, 4>([&](auto I) { constexpr int i = I; gstore<160 * i>(part, o0, d0[i], tag); });
      if (g == h) sfor<0, 4>([&](auto I) { constexpr int i = I; gstore<160 * i>(part, o1, d1[i], tag); });
    } else if (w == 2) {
      const f32x4 d0 = job(st, st + 4096, true);
      const f32x4 d1 = job(st + 1024, st + 4096, true);
      const f32x4 d2 = job(st + 4096, st + 4096, true);
      const bool k0 = (r >> 2) == 2, k1 = (r >> 2) == 3, kq = (r >> 2) == 2 + g;
      const unsigned o0 = (k0 ? 80 * g + 16 + (r & 3) : 400 + 4 * g) * 8, st0 = k0 ? 160 : 8;
      const unsigned o1 = (420 + (k1 ? 80 * g + 16 + (r & 3) : 400 + 4 * g)) * 8, st1 = k1 ? 160 : 8;
      const unsigned o2 = (420 * g + (kq ? 336 + (r & 3) : 416)) * 8, st2 = kq ? 160 : 8;
      if (k0 || r == 0) sfor<0, 4>([&](auto I) { constexpr int i = I; gstore<0>(part, o0 + i * st0, d0[i], tag); });
      if (k1 || r == 0) sfor<0, 4>([&](auto I) { constexpr int i = I; gstore<0>(part, o1 + i * st1, d1[i], tag); });
      if (g < 2 && (kq || r == 0)) sfor<0, 4>([&](auto I) { constexpr int i = I; gstore<0>(part, o2 + i * st2, d2[i], tag); });
    } else {
      if (lane < 40) {
        f32x2 e[16];
#pragma unroll
        for (int s = 0; s < 16; ++s) e[s] = *(LAS f32x2*)(lds + L_E + (s * 40 + lane) * 8);
        f32x2 run = {0.f, 0.f};
#pragma unroll
        for (int s = 0; s < 16; ++s) {
          *(LAS f32x2*)(lds + L_E + (s * 40 + lane) * 8) = run;
          run = cfma(c_a8, run, e[s]);
        }
        const char* ag = ws + WS_AGG + (cg * 16 + rb) * 640;
        gstore<0>(ag, lane * 16, run.x, tag); gstore<8>(ag, lane * 16, run.y, tag);
      }
    }
  }
  STAMP(11);

  const int T = 4 * xcd + (idx & 3), jt = idx >> 2;
  constexpr unsigned UNIT = 10240, R1 = B0 + 25600;
  const unsigned dA = ldsb + A0 + w * 2 * UNIT, dB = ldsb + R1 + w * UNIT;
  const char* cb = sptr((w & 1) ? cim : cre, (w >> 1) * 128);
  unsigned coff[2];
  {
    const int l3 = lane >> 3;
    coff[0] = (80 * jt + l3) * 1280 + (((lane & 7) ^ (l3 >> 1)) << 4);
    coff[1] = coff[0] ^ 64;
  }
  auto gc = [&](auto M, auto N) {
    constexpr int m = M, n = N;
    if constexpr (m == 2) glds<0, n * 1024, m * 256>(cb + n * 10240, coff[n & 1], dB);
    else glds<0, (m == 1 || m == 4 ? UNIT : 0) + n * 1024, m * 256>(cb + n * 10240, coff[n & 1], dA);
  };

  f32x4 accQ[3][2];
#pragma unroll
  for (int t = 0; t < 3; ++t) { accQ[t][0] = f32x4{0, 0, 0, 0}; accQ[t][1] = f32x4{0, 0, 0, 0}; }
  LAS char* q0 = lds + (8 * g + q4) * 96 + 8 * pp + 384 * (g & 1);
  LAS char* q1 = lds + (8 * g + q4) * 96 + 8 * pp + 384 * (1 - (g & 1));
  auto base3 = [&](auto M) {
    constexpr int m = M;
    kb[m & 1][0] = q0 + QSLOT[m]; kb[m & 1][1] = q1 + QSLOT[m];
    asm volatile("" : "+v"(kb[m & 1][0]), "+v"(kb[m & 1][1]));
  };
  auto rd3 = [&](auto KS, auto I) {
    constexpr int ks = KS, i = I;
    constexpr unsigned o = (ks & 3) * 3072 + 32 * (i >> 1);
    bh[ks & 1][i >> 1][i & 1] = __builtin_amdgcn_ds_read_tr16_b64_v4i16((LAS bf16x4*)(kb[(ks >> 2) & 1][i & 1] + o));
  };
  const int i1 = 53 * rb + lane;
  const bool act1 = lane < 53 && i1 < 840;
  const unsigned x1off = (act1 ? i1 : 0) * 8, x2off = min(lane, 39) * 16;
  const char* x1src = sptr(ws, WS_PART + (cg * 16 + 4 * w) * 6784);
  const char* x2src = sptr(ws, WS_AGG + cg * 16 * 640);
  u32x2 xh[4];
  u32x4 xl[4];
  auto xload = [&](auto N) {
    constexpr int n = N;
    if constexpr (n < 4) xh[n] = ld8_sc1(x1src + n * 6784, x1off);
    else { const int j = w + 4 * (n - 4); xl[n - 4] = ld16u_sc1(x2src + (j < rb ? j : 0) * 640, x2off); }
  };
  base3(IC<0>{});
  SB();
  sfor<0, 6>([&](auto I) { rd3(IC<0>{}, I); });
  SB();
  sfor<0, 20>([&](auto KS) {
    constexpr int ks = KS;
    if constexpr (((ks + 1) & 3) == 0 && ks + 1 < 20) { base3(IC<(ks + 1) / 4>{}); SB(); }
    if constexpr (ks == 1) { asm volatile("s_barrier" ::: "memory"); SB(); }
    sfor<0, 6>([&](auto I) {
      constexpr int i = I;
      accQ[i >> 1][i & 1] = mfma(cat(bh[ks & 1][i >> 1][0], bh[ks & 1][i >> 1][1]), xa[ks][i & 1], accQ[i >> 1][i & 1]);
      if constexpr (ks + 1 < 20) rd3(IC<ks + 1>{}, I);
      if constexpr (ks >= 2 && ks < 12 && (i == 1 || i == 4)) gc(IC<(ks - 2) / 5>{}, IC<2 * ((ks - 2) % 5) + (i == 4)>{});
      if constexpr (ks >= 14 && ks < 18 && (i == 1 || i == 4)) xload(IC<2 * (ks - 14) + (i == 4)>{});
      SB();
    });
  });
  STAMP(12);

  float gv[2];
#pragma unroll
  for (int mt = 0; mt < 2; ++mt) {
    const float s = rbf(accQ[2][mt][0]);
    const float gg = rbf(__builtin_amdgcn_rcpf(1.f + __expf(-s)));
    const int tl = 32 * w + 8 * (r >> 2) + 4 * mt + (r & 3);
    if (g == 2) {
      *(LAS float*)(lds + L_G + tl * 4) = gg;
      if (cg == 0) store4_sc1(ws, WS_G + (128 * rb + tl) * 4, __float_as_uint(gg));
    }
    gv[mt] = __shfl(gg, 32 + r);
  }
  {
    auto cpow = [](float lmag, float rev, float n) {
      const float m = __builtin_amdgcn_exp2f(n * lmag), f = __builtin_amdgcn_fractf(n * rev);
      return f32x2{m * __builtin_amdgcn_cosf(f), m * __builtin_amdgcn_sinf(f)};
    };
    const float nseg = (float)(8 * (4 * w + g));
#pragma unroll
    for (int p = 0; p < 3; ++p) {
      const int src = p < 2 ? 16 * p + r : 32 + (r & 7);
      pw[p] = cpow(__shfl(c_lmag, src), __shfl(c_rev, src), nseg);
    }
#pragma unroll
    for (int i = 0; i < 4; ++i) pj[i] = cpow(c_lmag, c_rev, (float)(128 * max(rb - 1 - w - 4 * i, 0)));
    asm volatile("" : "+v"(pw[0]), "+v"(pw[1]), "+v"(pw[2]), "+v"(pj[0]), "+v"(pj[1]), "+v"(pj[2]), "+v"(pj[3]), "+v"(gv[0]), "+v"(gv[1]));
  }
  STAMP(13);

  asm volatile("s_waitcnt vmcnt(0)"
               : "+v"(xh[0]), "+v"(xh[1]), "+v"(xh[2]), "+v"(xh[3]), "+v"(xl[0]), "+v"(xl[1]), "+v"(xl[2]), "+v"(xl[3])::"memory");
  {
    unsigned spins = 0;
    for (;;) {
      bool ok = true;
#pragma unroll
      for (int n = 0; n < 4; ++n) {
        ok &= xh[n][1] == tag || !act1;
        ok &= (xl[n][1] == tag && xl[n][3] == tag) || w + 4 * n >= rb;
      }
      if (__all(ok)) break;
      if (spin_fail(spins, tmo, tag)) break;
#pragma unroll
      for (int n = 0; n < 4; ++n) {
        const int j = w + 4 * n;
        xh[n] = ld8_wait(x1src + n * 6784, x1off);
        xl[n] = ld16_wait(x2src + (j < rb ? j : 0) * 640, x2off);
      }
    }
    *(LAS float*)(lds + L_H + tid * 4) =
        (__uint_as_float(xh[0][0]) + __uint_as_float(xh[1][0])) + (__uint_as_float(xh[2][0]) + __uint_as_float(xh[3][0]));
    f32x2 c = {0.f, 0.f};
#pragma unroll
    for (int n = 0; n < 4; ++n)
      if (w + 4 * n < rb) c = cfma(pj[n], f32x2{__uint_as_float(xl[n][0]), __uint_as_float(xl[n][2])}, c);
    if (lane < 40) *(LAS f32x2*)(lds + L_C + (w * 40 + lane) * 8) = c;
  }
  LAS char* crA[2];
  LAS char* crB[2];
#pragma unroll
  for (int kk = 0; kk < 2; ++kk) {
    const int lp = r * 128 + (((4 * kk + g) ^ ((r >> 1) & 7)) << 4);
    crA[kk] = lds + A0 + w * 2 * UNIT + lp; crB[kk] = lds + R1 + w * UNIT + lp;
  }
  asm volatile("" : "+v"(crA[0]), "+v"(crA[1]), "+v"(crB[0]), "+v"(crB[1]));
  bf16x8 bu[4][5];
#pragma unroll
  for (int kp = 0; kp < 4; ++kp)
#pragma unroll
    for (int nt = 0; nt < 5; ++nt) bu[kp][nt] = lds_frag(crA[kp & 1] + (kp >> 1) * UNIT + nt * 2048);
  STAMP(14);
  LSYNC();
  STAMP(15);
  if (w == 0) {
    const float s = (*(LAS float*)(lds + L_H + lane * 4) + *(LAS float*)(lds + L_H + 256 + lane * 4)) +
                    (*(LAS float*)(lds + L_H + 512 + lane * 4) + *(LAS float*)(lds + L_H + 768 + lane * 4));
    if (act1) gstore<0>(sptr(ws, WS_FIN + cg * 848 * 8), i1 * 8, s, tag);
  }

  u32x2 fv[4];
  const unsigned foff[4] = {(unsigned)tid * 8, (unsigned)(tid + 256) * 8, (unsigned)(tid + 512) * 8, (unsigned)(tid < 72 ? tid + 768 : 0) * 8};
  const char* fsrc = sptr(ws, WS_FIN + cg * 848 * 8);
  {
    const f32x4 ga = *(LAS f32x4*)(lds + L_G + (32 * w + 8 * g) * 4), gb4 = *(LAS f32x4*)(lds + L_G + (32 * w + 8 * g + 4) * 4);
    LAS char* img = lds + B0 + w * 6400;
    f32x2 h[3];
#pragma unroll
    for (int p = 0; p < 3; ++p) {
      const int chl = p < 2 ? 16 * p + r : 32 + (r & 7);
      LAS char* cp = lds + L_C + chl * 8;
      const f32x2 cin = (*(LAS f32x2*)cp + *(LAS f32x2*)(cp + 320)) + (*(LAS f32x2*)(cp + 640) + *(LAS f32x2*)(cp + 960));
      h[p] = cfma(pw[p], cin, *(LAS f32x2*)(lds + L_E + ((4 * w + g) * 40 + chl) * 8));
    }
    SB();
    sfor<0, 8>([&](auto TT) {
      constexpr int t = TT;
      f32x2 t1[3];
#pragma unroll
      for (int p = 0; p < 3; ++p) t1[p] = cfma_1(pa[p], h[p], ub[p][t]);
      SB();
#pragma unroll
      for (int p = 0; p < 3; ++p) h[p] = cfma_2(pa[p], h[p], t1[p]);
      SB();
#pragma unroll
      for (int p = 0; p < 3; ++p) t1[p] = cco_1(pc[p], h[p]);
      SB();
#pragma unroll
      for (int p = 0; p < 3; ++p) t1[p] = cco_2(pc[p], h[p], t1[p]);
      SB();
#pragma unroll
      for (int p = 0; p < 3; ++p) ub[p][t] = t1[p] * (t < 4 ? ga[t & 3] : gb4[t & 3]);
      gc(IC<2>{}, TT);
      SB();
    });
#pragma unroll
    for (int p = 0; p < 3; ++p) {
#pragma unroll
      for (int mt = 0; mt < 2; ++mt) {
        const f32x2* y = &ub[p][4 * mt];
        const u32x2 vr = {pk2(y[0].x, y[1].x), pk2(y[2].x, y[3].x)};
        const u32x2 vi = {pk2(y[0].y, y[1].y), pk2(y[2].y, y[3].y)};
        if (p < 2) {
          *(LAS u32x2*)(img + mt * 3200 + (16 * p + r) * 40 + 8 * g) = vr;
          *(LAS u32x2*)(img + mt * 3200 + (40 + 16 * p + r) * 40 + 8 * g) = vi;
        } else {
          const int row = r < 8 ? 32 + r : 72 + (r - 8);
          *(LAS u32x2*)(img + mt * 3200 + row * 40 + 8 * g) = r < 8 ? vr : vi;
        }
      }
    }
    gc(IC<2>{}, IC<8>{}); gc(IC<2>{}, IC<9>{});
    asm volatile("s_waitcnt lgkmcnt(0)" ::: "memory");
#pragma unroll
    for (int f = 0; f < 5; ++f) {
      const int mt = f < 4 ? (f >> 1) : (g >> 1);
      const int o8 = f < 4 ? 4 * (f & 1) + g : 8 + (g & 1);
      LAS char* p0 = img + mt * 3200 + (8 * o8 + q4) * 40 + 8 * pp;
      const bf16x8 v = lds_frag_tr(p0, p0 + 160);
      const int part = o8 >= 5, o = 5 * cg + (part ? o8 - 5 : o8);
      const int q = 16 * (o >> 3) + 8 * part + (o & 7);
      const int rt = 8 * rb + 2 * w + mt;
      store16_sc1(ws, WS_A + (rt * 240 + q) * 256 + r * 16, __builtin_bit_cast(u32x4, v));
    }
  }
  STAMP(16);

#pragma unroll
  for (int it = 0; it < 4; ++it) fv[it] = ld8_sc1(fsrc, foff[it]);
  bf16x8 qf[2][2];
#pragma unroll
  for (int mt = 0; mt < 2; ++mt) {
    unsigned tl[2];
#pragma unroll
    for (int h2 = 0; h2 < 2; ++h2) {
      const unsigned ph = phi2(accQ[2][mt][2 * h2], accQ[2][mt][2 * h2 + 1]);
      tl[h2] = g < 2 ? ph : 0u;
    }
#pragma unroll
    for (int h = 0; h < 2; ++h)
      qf[h][mt] = frag4(phi2(accQ[h][mt][0], accQ[h][mt][1]), phi2(accQ[h][mt][2], accQ[h][mt][3]), tl[0], tl[1]);
  }
  asm volatile("" : "+v"(qf[0][0]), "+v"(qf[0][1]), "+v"(qf[1][0]), "+v"(qf[1][1]));
  unsigned kdst[4];
#pragma unroll
  for (int it = 0; it < 4; ++it) {
    const int i = tid + 256 * it, h = i >= 420, ii = i - 420 * h;
    const int dd = (ii * 3277) >> 16;
    const int e = ii < 400 ? ii - 20 * dd : 20, d = ii < 400 ? dd : ii - 400;
    kdst[it] = L_KVT + (h * 21 + e) * 40 + d * 2;
  }
  asm volatile("" : "+v"(kdst[0]), "+v"(kdst[1]), "+v"(kdst[2]), "+v"(kdst[3]));
  asm volatile("s_waitcnt vmcnt(0)\n\ts_barrier" : "+v"(fv[0]), "+v"(fv[1]), "+v"(fv[2]), "+v"(fv[3])::"memory");
  if (tid == 0) __hip_atomic_store((GAS unsigned*)(ws + WS_HFLAG) + rb * 16 + cg, tag, __ATOMIC_RELAXED, __HIP_MEMORY_SCOPE_AGENT);
  {
    unsigned spins = 0;
    for (;;) {
      const bool ok = fv[0][1] == tag && fv[1][1] == tag && fv[2][1] == tag && fv[3][1] == tag;
      if (__all(ok)) break;
      if (spin_fail(spins, tmo, tag)) break;
#pragma unroll
      for (int it = 0; it < 4; ++it) fv[it] = ld8_wait(fsrc, foff[it]);
    }
#pragma unroll
    for (int it = 0; it < 4; ++it) {
      const float f = __uint_as_float(fv[it][0]);
      if (it < 3 || tid < 72) *(LAS u16*)(lds + kdst[it]) = (u16)pk2(f, f);
    }
  }
  STAMP(17);
  LSYNC();

  {
    f32x4 num[2][2], nt[2], den[2][2];
#pragma unroll
    for (int mt = 0; mt < 2; ++mt) nt[mt] = f32x4{0, 0, 0, 0};
    sfor<0, 2>([&](auto H) {
      constexpr int h = H;
      LAS char* kbp = lds + L_KVT + h * 840;
      const u32x2 z2 = {0u, 0u};
      const u32x2 ml = *(LAS u32x2*)(kbp + r * 40 + 8 * g), mh = *(LAS u32x2*)(kbp + r * 40 + 32);
      const u32x2 tl = *(LAS u32x2*)(kbp + (16 + (r & 3)) * 40 + 8 * g), th = *(LAS u32x2*)(kbp + (16 + (r & 3)) * 40 + 32);
      const u32x2 dl = *(LAS u32x2*)(kbp + 800 + 8 * g), dh = *(LAS u32x2*)(kbp + 800 + 32);
      const bool gh = g == h, rv = (r >> 2) == h;
      const u32x2 mh2 = gh ? mh : z2, dh2 = gh ? dh : z2, tl2 = rv ? tl : z2, th2 = (rv && gh) ? th : z2;
      const bf16x8 am = frag4(ml[0], ml[1], mh2[0], mh2[1]);
      const bf16x8 at = frag4(tl2[0], tl2[1], th2[0], th2[1]);
      const bf16x8 ad = frag4(dl[0], dl[1], dh2[0], dh2[1]);
#pragma unroll
      for (int mt = 0; mt < 2; ++mt) {
        num[h][mt] = mfma(am, qf[h][mt], f32x4{0, 0, 0, 0});
        nt[mt] = mfma(at, qf[h][mt], nt[mt]);
        den[h][mt] = mfma(ad, qf[h][mt], f32x4{0, 0, 0, 0});
      }
    });
    sfor<0, 2>([&](auto MT) {
      constexpr int mt = MT;
      const float og = rbf(1.f - gv[mt]);
      float sc[2];
#pragma unroll
      for (int h = 0; h < 2; ++h) sc[h] = og * __builtin_amdgcn_rcpf(den[h][mt][0] + 1e-6f);
      const int rt = 8 * rb + 2 * w + mt;
      const u32x4 ov = {pk2(num[0][mt][0] * sc[0], num[0][mt][1] * sc[0]), pk2(num[0][mt][2] * sc[0], num[0][mt][3] * sc[0]),
                        pk2(num[1][mt][0] * sc[1], num[1][mt][1] * sc[1]), pk2(num[1][mt][2] * sc[1], num[1][mt][3] * sc[1])};
      store16_sc1(ws, WS_A + (rt * 240 + 160 + 5 * cg + g) * 256 + r * 16, ov);
      const float st = g == 0 ? sc[0] : sc[1];
      const u32x2 tv = {pk2(nt[mt][0] * st, nt[mt][1] * st), pk2(nt[mt][2] * st, nt[mt][3] * st)};
      if (g < 2) store8_sc1(ws, WS_A + (rt * 240 + 160 + 5 * cg + 4) * 256 + r * 16 + 8 * g, tv);
    });
  }
  STAMP(18);

  u32x2 exv[5], edv[5];
  unsigned egv[5], eoff[5], elds[5], goff[5];
#pragma unroll
  for (int it = 0; it < 5; ++it) {
    const int item = tid + 256 * it, tp = (item * 3277) >> 16, dq = item - 20 * tp;
    const int mt = tp >> 4, rr = tp & 15;
    const int t = 64 * T + 32 * (mt >> 1) + 8 * (rr >> 2) + 4 * (mt & 1) + (rr & 3);
    const int d = 80 * jt + 4 * dq;
    eoff[it] = (t * 640 + d) * 4; elds[it] = (tp * 84 + 4 * dq) * 4;
    exv[it] = ld8(x, eoff[it] >> 1);
    edv[it] = ld8(dskip, d * 2);
    goff[it] = WS_G + t * 4;
  }
  {
    const unsigned hoff = WS_HFLAG + ((T >> 1) * 16 + r) * 4;
    unsigned spins = 0;
    for (;;) {
      const unsigned f = ld4_wait(ws, hoff);
      if (__all(f == tag)) break;
      if (spin_fail(spins, tmo, tag)) break;
    }
  }
  STAMP(19);

  const char* abase = sptr(ws, WS_A + 4 * T * 240 * 256);
  const unsigned alane = lane * 16;
  bf16x8 a[4][4];
  auto ga = [&](auto KP) {
    constexpr int kp = KP;
    const unsigned q = kp < 10 ? 32 * (kp >> 1) + 16 * (w >> 1) + 8 * (w & 1) + 4 * (kp & 1) : 160 + 16 * (kp - 10) + 4 * w;
    const char* p = abase + q * 256;
    sfor<0, 4>([&](auto MT) {
      constexpr int mt = MT;
      a[kp & 3][mt] = ld16_sc1(p + mt * 240 * 256, alane);
    });
  };
  ga(IC<0>{}); ga(IC<1>{}); ga(IC<2>{});
#pragma unroll
  for (int it = 0; it < 5; ++it) egv[it] = ld4_sc1(ws, goff[it]);
  sfor<0, 10>([&](auto N) { gc(IC<3>{}, N); });
  if (TOUCHA) {
    const unsigned li = 40 * w + min(lane, 39);
    const unsigned rtl = li / 40, rem = li - 40 * rtl;
    touch_s_sc1(abase, rtl * 240 * 256 + (20 * jt) * 256 + rem * 128, ldsb + L_DUMMY);
  }
  asm volatile("s_barrier" ::: "memory");
  if (tid == 0) {
    __hip_atomic_store((GAS unsigned*)(ws + WS_OFLAG) + rb * 16 + cg, tag, __ATOMIC_RELAXED, __HIP_MEMORY_SCOPE_AGENT);
    if (blockIdx.x == 0) __hip_atomic_store((GAS unsigned*)(ws + WS_EPOCH), tag, __ATOMIC_RELAXED, __HIP_MEMORY_SCOPE_AGENT);
  }
  asm volatile("" : "+v"(exv[0]), "+v"(exv[1]), "+v"(exv[2]), "+v"(exv[3]), "+v"(exv[4]), "+v"(edv[0]), "+v"(edv[1]), "+v"(edv[2]),
               "+v"(edv[3]), "+v"(edv[4])::"memory");
  f32x4 skip[5];
#pragma unroll
  for (int it = 0; it < 5; ++it) {
    const unsigned s0 = pk2(blo(exv[it][0]) * blo(edv[it][0]), bhi(exv[it][0]) * bhi(edv[it][0]));
    const unsigned s1 = pk2(blo(exv[it][1]) * blo(edv[it][1]), bhi(exv[it][1]) * bhi(edv[it][1]));
    skip[it] = f32x4{blo(s0), bhi(s0), blo(s1), bhi(s1)};
  }
  sfor<0, 10>([&](auto N) { gc(IC<4>{}, N); });
  asm volatile("" : "+v"(skip[0]), "+v"(skip[1]), "+v"(skip[2]), "+v"(skip[3]), "+v"(skip[4]), "+v"(eoff[0]), "+v"(eoff[1]), "+v"(eoff[2]),
               "+v"(eoff[3]), "+v"(eoff[4]), "+v"(elds[0]), "+v"(elds[1]), "+v"(elds[2]), "+v"(elds[3]), "+v"(elds[4]));
  SB();
  STAMP(20);

  const char* wsrc = sptr(ws, WS_WO + (jt * 20 + w) * 5120 + 2048);
  auto gw_ = [&](auto M, auto N) {
    constexpr int m = M, n = N;
    if constexpr (m < 2) glds<1, m * 5120 + n * 1024, n * 1024 - 2048>(wsrc + m * 4 * 5120, lin, dB);
    else glds<1, (m - 2) * 5120 + n * 1024, n * 1024 - 2048>(wsrc + m * 4 * 5120, lin, dA);
  };
  const int wlp0 = (8 * g + q4 + 4 * (g & 1)) * 160 + 8 * pp, wlp1 = (8 * g + q4 + 4 * (1 - (g & 1))) * 160 + 8 * pp;
  LAS char* wrA[2] = {lds + A0 + w * 2 * UNIT + wlp0, lds + A0 + w * 2 * UNIT + wlp1};
  LAS char* wrB[2] = {lds + R1 + w * UNIT + wlp0, lds + R1 + w * UNIT + wlp1};
  asm volatile("" : "+v"(wrA[0]), "+v"(wrA[1]), "+v"(wrB[0]), "+v"(wrB[1]));
  bf16x8 bf[2][5];
  auto rd = [&](auto KP, auto I) {
    constexpr int kp = KP, i = I;
    if constexpr (kp < 4) {
    } else if constexpr (kp < 10) {
      if constexpr (i < 5) bf[kp & 1][i] = lds_frag(kp < 6 ? crB[kp & 1] + i * 2048 : crA[kp & 1] + (kp < 8 ? 0 : UNIT) + i * 2048);
    } else {
      constexpr int m = kp - 10;
      bh[kp & 1][i >> 1][i & 1] = __builtin_amdgcn_ds_read_tr16_b64_v4i16(
          (LAS bf16x4*)(m < 2 ? wrB[i & 1] + m * 5120 + 32 * (i >> 1) : wrA[i & 1] + (m - 2) * 5120 + 32 * (i >> 1)));
    }
  };
  auto bfrag = [&](auto KP, auto NT) {
    constexpr int kp = KP, nt = NT;
    if constexpr (kp < 4) return bu[kp][nt];
    else if constexpr (kp < 10) return bf[kp & 1][nt];
    else return (bf16x8)__builtin_shufflevector(bh[kp & 1][nt][0], bh[kp & 1][nt][1], 0, 1, 2, 3, 4, 5, 6, 7);
  };
  f32x4 acc[4][5];
#pragma unroll
  for (int mt = 0; mt < 4; ++mt)
#pragma unroll
    for (int nt = 0; nt < 5; ++nt) acc[mt][nt] = f32x4{0, 0, 0, 0};
  unsigned of = 0;
  const unsigned ooff = WS_OFLAG + ((T >> 1) * 16 + r) * 4;
#define AWAITN(N, S) do { asm volatile("s_waitcnt vmcnt(%4) lgkmcnt(0)" : "+v"(a[S][0]), "+v"(a[S][1]), "+v"(a[S][2]), "+v"(a[S][3]) : "n"(N) : "memory"); SB(); } while (0)
#define AWAIT(N, S) do { asm volatile("s_waitcnt vmcnt(" #N ") lgkmcnt(0)" : "+v"(a[S][0]), "+v"(a[S][1]), "+v"(a[S][2]), "+v"(a[S][3])::"memory"); SB(); } while (0)
  sfor<0, 15>([&](auto KP) {
    constexpr int kp = KP, sl = kp & 3;
    if constexpr (kp <= 2) AWAITN(33 + TAN, sl);
    if constexpr (kp == 3 || kp == 4) AWAIT(8, sl);
    if constexpr (kp == 3) asm volatile("" : "+v"(egv[0]), "+v"(egv[1]), "+v"(egv[2]), "+v"(egv[3]), "+v"(egv[4])::"memory");
    if constexpr (kp == 5) AWAIT(9, sl);
    if constexpr (kp == 6) AWAIT(19, sl);
    if constexpr (kp == 13) AWAIT(4, sl);
    if constexpr (kp == 7 || kp == 9) AWAIT(18, sl);
    if constexpr (kp == 7) asm volatile("" : "+v"(of)::"memory");
    if constexpr (kp == 8) AWAIT(28, sl);
    if constexpr (kp == 12) AWAIT(13, sl);
    if constexpr (kp == 10) AWAIT(23, sl);
    if constexpr (kp == 11) AWAIT(13, sl);
    if constexpr (kp == 14) AWAIT(0, sl);
    if constexpr (kp == 7) {
      if (!__all(of == tag)) {
        unsigned spins = 0;
        for (;;) {
          of = ld4_wait(ws, ooff);
          if (__all(of == tag)) break;
          if (spin_fail(spins, tmo, tag)) break;
        }
      }
      SB();
    }
    if constexpr (kp == 5) STAMP(21);
    if constexpr (kp == 10) STAMP(22);
    if constexpr (kp + 3 < 15) ga(IC<kp + 3>{});
    sfor<0, 20>([&](auto I) {
      constexpr int i = I, nt = i >> 2, mt = i & 3;
      acc[mt][nt] = mfma(bfrag(KP, IC<nt>{}), a[sl][mt], acc[mt][nt]);
      if constexpr (kp + 1 < 15 && i < 10) rd(IC<kp + 1>{}, I);
      if constexpr ((kp == 5 || kp == 7) && (i & 1) == 1) gw_(IC<kp - 5 + (i >= 10)>{}, IC<(i % 10) / 2>{});
      if constexpr (kp == 9 && (i & 3) == 1) gw_(IC<4>{}, IC<(i >> 2)>{});
      if constexpr (kp == 4 && i == 19) of = ld4_sc1(ws, ooff);
      SB();
    });
  });
  STAMP(23);
  LSYNC();
#pragma unroll
  for (int mt = 0; mt < 4; ++mt)
#pragma unroll
    for (int nt = 0; nt < 5; ++nt)
      *(LAS f32x4*)(lds + ((w * 64 + 16 * mt + r) * 84 + 16 * nt + 4 * g) * 4) = acc[mt][nt];
  LSYNC();
  STAMP(24);
#pragma unroll
  for (int it = 0; it < 5; ++it) {
    const f32x4 s0 = *(LAS f32x4*)(lds + elds[it]), s1 = *(LAS f32x4*)(lds + elds[it] + 64 * 84 * 4);
    const f32x4 s2 = *(LAS f32x4*)(lds + elds[it] + 2 * 64 * 84 * 4), s3 = *(LAS f32x4*)(lds + elds[it] + 3 * 64 * 84 * 4);
    __builtin_nontemporal_store(((s0 + s1) + (s2 + s3)) + skip[it] * __uint_as_float(egv[it]), (f32x4*)((char*)out + eoff[it]));
  }
  asm volatile(".Lkf_end:");
  STAMP(25);
  CSTAMP(31);
  STAMP_OUT(ws, 0);
}

extern "C" void kernel_launch(void* const* d_in, const int* in_sizes, int n_in, void* d_out, int out_size, void* d_ws,
                              size_t ws_size, hipStream_t stream) {
  const u16* x = (const u16*)d_in[0];
  const u16* lre = (const u16*)d_in[1];
  const u16* lim = (const u16*)d_in[2];
  const u16* bre = (const u16*)d_in[3];
  const u16* bim = (const u16*)d_in[4];
  const u16* cre = (const u16*)d_in[5];
  const u16* cim = (const u16*)d_in[6];
  const u16* ldt = (const u16*)d_in[7];
  const u16* dskip = (const u16*)d_in[8];
  const u16* wqkv = (const u16*)d_in[10];
  const u16* wo = (const u16*)d_in[11];
  const u16* gw = (const u16*)d_in[12];
  constexpr int LDS = 163840;
  (void)hipFuncSetAttribute((const void*)k_fused, hipFuncAttributeMaxDynamicSharedMemorySize, LDS);
  k_fused<<<256, 256, LDS, stream>>>(x, lre, lim, ldt, bre, bim, wqkv, gw, cre, cim, wo, dskip, (char*)d_ws, (float*)d_out);
}
```

```cpp
#define DIAGK 0
#define DIAGB 8
#include <hip/hip_runtime.h>
#include <stdint.h>

typedef unsigned short u16;
typedef unsigned long long u64;
typedef short bf16x8 __attribute__((ext_vector_type(8)));
typedef short bf16x4 __attribute__((ext_vector_type(4)));
typedef float f32x4 __attribute__((ext_vector_type(4)));
typedef float f32x2 __attribute__((ext_vector_type(2)));
typedef unsigned u32x4 __attribute__((ext_vector_type(4)));
typedef unsigned u32x2 __attribute__((ext_vector_type(2)));
typedef __bf16 bf16v2 __attribute__((ext_vector_type(2)));
#define LAS __attribute__((address_space(3)))
#define GAS __attribute__((address_space(1)))
#define DI __device__ __forceinline__

constexpr unsigned WS_CFLAG = 0;
constexpr unsigned WS_HFLAG = 1024;
constexpr unsigned WS_OFLAG = 3072;
constexpr unsigned WS_TMO   = 2048;
constexpr unsigned WS_EPOCH = 2304;
constexpr unsigned WS_AGG   = 4096;
constexpr unsigned WS_PART  = WS_AGG + 16 * 16 * 80 * 8;
constexpr unsigned WS_FIN   = WS_PART + 16 * 16 * 848 * 8;
constexpr unsigned WS_ZEND  = WS_FIN + 16 * 848 * 8;
constexpr unsigned WS_WKV   = (WS_ZEND + 4095) / 4096 * 4096;
constexpr unsigned WS_WQ    = WS_WKV + 16 * 102400;
constexpr unsigned WS_WO    = WS_WQ + 16 * 61440;
constexpr unsigned WS_A     = WS_WO + 8 * 20 * 5120;
constexpr unsigned WS_G     = WS_A + 128 * 240 * 256;
constexpr unsigned WS_STAMP = (WS_G + 8192 + 4095) / 4096 * 4096;
constexpr unsigned SPIN_CAP = 1u << 15;

DI unsigned pk2(float a, float b) { f32x2 p = {a, b}; return __builtin_bit_cast(unsigned, __builtin_convertvector(p, bf16v2)); }
DI float blo(unsigned u) { return __uint_as_float(u << 16); }
DI float bhi(unsigned u) { return __uint_as_float(u & 0xffff0000u); }
DI float rbf(float a) { return blo(pk2(a, a)); }
DI float bf2f(unsigned v) { return __uint_as_float(v << 16); }
DI bf16x8 frag4(unsigned a, unsigned b, unsigned c, unsigned d) { u32x4 v = {a, b, c, d}; return __builtin_bit_cast(bf16x8, v); }
DI f32x4 mfma(bf16x8 a, bf16x8 b, f32x4 c) { return __builtin_amdgcn_mfma_f32_16x16x32_bf16(a, b, c, 0, 0, 0); }
DI bf16x8 lds_frag(LAS char* p) { return *(LAS bf16x8*)p; }
DI bf16x8 lds_frag_tr(LAS char* p0, LAS char* p1) {
  bf16x4 a = __builtin_amdgcn_ds_read_tr16_b64_v4i16((LAS bf16x4*)p0);
  bf16x4 b = __builtin_amdgcn_ds_read_tr16_b64_v4i16((LAS bf16x4*)p1);
  return __builtin_shufflevector(a, b, 0, 1, 2, 3, 4, 5, 6, 7);
}
template <int SC1, unsigned IMM, int GOFF = 0>
DI void glds(const void* sbase, unsigned voff, unsigned ldsbase) {
  if constexpr (SC1)
    asm volatile("s_add_u32 m0, %2, %3\n\ts_nop 0\n\tglobal_load_lds_dwordx4 %0, %1 offset:%4 sc1" ::"v"(voff), "s"(sbase), "s"(ldsbase), "i"(IMM - GOFF), "i"(GOFF) : "memory", "scc");
  else
    asm volatile("s_add_u32 m0, %2, %3\n\ts_nop 0\n\tglobal_load_lds_dwordx4 %0, %1 offset:%4" ::"v"(voff), "s"(sbase), "s"(ldsbase), "i"(IMM - GOFF), "i"(GOFF) : "memory", "scc");
}
template <int V> struct IC { static constexpr int value = V; constexpr operator int() const { return V; } };
template <int I, int N, class F>
DI void sfor(F&& f) { if constexpr (I < N) { f(IC<I>{}); sfor<I + 1, N>(f); } }
DI void touch(const void* p, unsigned ldsdummy) {
  asm volatile("s_mov_b32 m0, %1\n\ts_nop 0\n\tglobal_load_lds_dword %0, off" ::"v"(p), "s"(ldsdummy) : "memory");
}
DI void touch_s(const void* sbase, unsigned voff, unsigned ldsdummy) {
  asm volatile("s_mov_b32 m0, %2\n\ts_nop 0\n\tglobal_load_lds_dword %0, %1" ::"v"(voff), "s"(sbase), "s"(ldsdummy) : "memory");
}
DI void touch_s_sc1(const void* sbase, unsigned voff, unsigned ldsdummy) {
  asm volatile("s_mov_b32 m0, %2\n\ts_nop 0\n\tglobal_load_lds_dword %0, %1 sc1" ::"v"(voff), "s"(sbase), "s"(ldsdummy) : "memory");
}
DI void store16_sc1(void* sbase, unsigned voff, u32x4 v) {
  asm volatile("global_store_dwordx4 %0, %1, %2 sc1\n\ts_nop 1" ::"v"(voff), "v"(v), "s"(sbase) : "memory");
}
#define SYNC(N) do { asm volatile("s_waitcnt vmcnt(%0) lgkmcnt(0)\n\ts_barrier" ::"n"(N) : "memory"); __builtin_amdgcn_sched_barrier(0); } while (0)
#define LSYNC() do { asm volatile("s_waitcnt lgkmcnt(0)\n\ts_barrier" ::: "memory"); __builtin_amdgcn_sched_barrier(0); } while (0)
DI u64 gload(const GAS u64* p) { return __hip_atomic_load(p, __ATOMIC_RELAXED, __HIP_MEMORY_SCOPE_AGENT); }
template <int OFF>
DI void gstore(const void* sbase, unsigned voff, float v, unsigned tag) {
  const u32x2 d = {__float_as_uint(v), tag};
  asm volatile("global_store_dwordx2 %0, %1, %2 offset:%3 sc1" ::"v"(voff), "v"(d), "s"(sbase), "i"(OFF) : "memory");
}
DI u32x2 ld8(const void* sbase, unsigned voff) { u32x2 t; asm volatile("global_load_dwordx2 %0, %1, %2" : "=v"(t) : "v"(voff), "s"(sbase) : "memory"); return t; }
DI u32x4 ld16(const void* sbase, unsigned voff) { u32x4 t; asm volatile("global_load_dwordx4 %0, %1, %2" : "=v"(t) : "v"(voff), "s"(sbase) : "memory"); return t; }
DI unsigned ld4_sc1(const void* sbase, unsigned voff) { unsigned t; asm volatile("global_load_dword %0, %1, %2 sc1" : "=v"(t) : "v"(voff), "s"(sbase) : "memory"); return t; }
DI bf16x8 ld16_sc1(const void* sbase, unsigned voff) { bf16x8 t; asm volatile("global_load_dwordx4 %0, %1, %2 sc1" : "=v"(t) : "v"(voff), "s"(sbase) : "memory"); return t; }
DI u32x4 ld16u_sc1(const void* sbase, unsigned voff) { u32x4 t; asm volatile("global_load_dwordx4 %0, %1, %2 sc1" : "=v"(t) : "v"(voff), "s"(sbase) : "memory"); return t; }
DI u32x2 ld8_sc1(const void* sbase, unsigned voff) { u32x2 t; asm volatile("global_load_dwordx2 %0, %1, %2 sc1" : "=v"(t) : "v"(voff), "s"(sbase) : "memory"); return t; }
DI u32x2 ld8_wait(const void* sbase, unsigned voff) { u32x2 t; asm volatile("global_load_dwordx2 %0, %1, %2 sc1\n\ts_waitcnt vmcnt(0)" : "=&v"(t) : "v"(voff), "s"(sbase) : "memory"); return t; }
DI u32x4 ld16_wait(const void* sbase, unsigned voff) { u32x4 t; asm volatile("global_load_dwordx4 %0, %1, %2 sc1\n\ts_waitcnt vmcnt(0)" : "=&v"(t) : "v"(voff), "s"(sbase) : "memory"); return t; }
DI unsigned ld4_wait(const void* sbase, unsigned voff) { unsigned t; asm volatile("global_load_dword %0, %1, %2 sc1\n\ts_waitcnt vmcnt(0)" : "=&v"(t) : "v"(voff), "s"(sbase) : "memory"); return t; }
DI const char* sptr(const void* base, unsigned off) { return (const char*)base + (unsigned)__builtin_amdgcn_readfirstlane(off); }
DI void store8_sc1(void* sbase, unsigned voff, u32x2 v) {
  asm volatile("global_store_dwordx2 %0, %1, %2 sc1" ::"v"(voff), "v"(v), "s"(sbase) : "memory");
}
DI void store4_sc1(void* sbase, unsigned voff, unsigned v) {
  asm volatile("global_store_dword %0, %1, %2 sc1" ::"v"(voff), "v"(v), "s"(sbase) : "memory");
}
DI bool spin_fail(unsigned& spins, GAS unsigned* tmo, unsigned tag) {
  __builtin_amdgcn_s_sleep(1);
  if (++spins > SPIN_CAP) { __hip_atomic_store(tmo, tag, __ATOMIC_RELAXED, __HIP_MEMORY_SCOPE_AGENT); return true; }
  if ((spins & 63u) == 0u)
    return (unsigned)__builtin_amdgcn_readfirstlane(__hip_atomic_load(tmo, __ATOMIC_RELAXED, __HIP_MEMORY_SCOPE_AGENT)) == tag;
  return false;
}
DI float phi1(float t) {
  return (__builtin_amdgcn_exp2f(fminf(t, 0.f) * 1.4426950408889634f) + fmaxf(t, 0.f)) + 1e-6f;
}
DI f32x2 cfma(f32x2 a, f32x2 h, f32x2 u) {
  f32x2 t, o;
  asm("v_pk_fma_f32 %0, %1, %2, %3 op_sel_hi:[0,1,1]" : "=v"(t) : "v"(a), "v"(h), "v"(u));
  asm("v_pk_fma_f32 %0, %1, %2, %3 op_sel:[1,1,0] op_sel_hi:[1,0,1] neg_lo:[1,0,0]" : "=v"(o) : "v"(a), "v"(h), "v"(t));
  return o;
}
DI f32x2 cfma_1(f32x2 a, f32x2 h, f32x2 u) {
  f32x2 t;
  asm("v_pk_fma_f32 %0, %1, %2, %3 op_sel_hi:[0,1,1]" : "=v"(t) : "v"(a), "v"(h), "v"(u));
  return t;
}
DI f32x2 cfma_2(f32x2 a, f32x2 h, f32x2 t) {
  f32x2 o;
  asm("v_pk_fma_f32 %0, %1, %2, %3 op_sel:[1,1,0] op_sel_hi:[1,0,1] neg_lo:[1,0,0]" : "=v"(o) : "v"(a), "v"(h), "v"(t));
  return o;
}
DI f32x2 cco_1(f32x2 a, f32x2 h) {
  f32x2 t;
  asm("v_pk_mul_f32 %0, %1, %2 op_sel_hi:[0,1] neg_hi:[1,0]" : "=v"(t) : "v"(a), "v"(h));
  return t;
}
DI f32x2 cco_2(f32x2 a, f32x2 h, f32x2 t) {
  f32x2 o;
  asm("v_pk_fma_f32 %0, %1, %2, %3 op_sel:[1,1,0] op_sel_hi:[1,0,1] neg_lo:[1,0,0] neg_hi:[1,0,0]" : "=v"(o) : "v"(a), "v"(h), "v"(t));
  return o;
}
DI f32x2 cmul(f32x2 a, f32x2 h) {
  f32x2 t, o;
  asm("v_pk_mul_f32 %0, %1, %2 op_sel:[1,1] op_sel_hi:[1,0]" : "=v"(t) : "v"(a), "v"(h));
  asm("v_pk_fma_f32 %0, %1, %2, %3 op_sel_hi:[0,1,1] neg_lo:[0,0,1]" : "=v"(o) : "v"(a), "v"(h), "v"(t));
  return o;
}
DI f32x2 cmul_conj_out(f32x2 a, f32x2 h) {
  f32x2 t, o;
  asm("v_pk_mul_f32 %0, %1, %2 op_sel_hi:[0,1] neg_hi:[1,0]" : "=v"(t) : "v"(a), "v"(h));
  asm("v_pk_fma_f32 %0, %1, %2, %3 op_sel:[1,1,0] op_sel_hi:[1,0,1] neg_lo:[1,0,0] neg_hi:[1,0,0]" : "=v"(o) : "v"(a), "v"(h), "v"(t));
  return o;
}
DI f32x2 csq(f32x2 a) { return cmul(a, a); }
DI unsigned phi2(float a, float b) {
  const f32x2 t = {a, b};
  const f32x2 l = t * 1.4426950408889634f;
  f32x2 e, m;
  e.x = __builtin_amdgcn_fmed3f(__builtin_amdgcn_exp2f(l.x), 0.f, 1.f);
  e.y = __builtin_amdgcn_fmed3f(__builtin_amdgcn_exp2f(l.y), 0.f, 1.f);
  m.x = __builtin_amdgcn_fmed3f(a, 0.f, __builtin_inff()); m.y = __builtin_amdgcn_fmed3f(b, 0.f, __builtin_inff());
  const f32x2 p = (e + m) + 1e-6f;
  return pk2(p.x, p.y);
}
#define TOUCH1 1
#define TOUCH2 0
#define MAP48 1
#define TOUCHD 0
#define TOUCHA 0
constexpr int TAN = TOUCHA ? 1 : 0;
constexpr int TDN = TOUCHD ? 1 : 0, T1N = 0, T2N = TOUCH2 ? 2 : 0, TCN = TOUCH1 ? 1 : 0;
#define SB() __builtin_amdgcn_sched_barrier(0)
#define VWAIT(N) do { asm volatile("s_waitcnt vmcnt(%0)" ::"n"(N) : "memory"); SB(); } while (0)

#define STAMP_DECL u64 d0_ = 0, dk_ = 0;
#define STAMP(i) do { if ((i) == 0) d0_ = __builtin_amdgcn_s_memrealtime(); if ((i) == DIAGK) dk_ = __builtin_amdgcn_s_memrealtime(); } while (0)
#define CSTAMP(i)
#define STAMP_OUT(ws, k) do { if (blockIdx.x == DIAGB && threadIdx.x == 0) { const u64 n_ = __builtin_amdgcn_s_memrealtime(), e_ = n_ + 4 * (dk_ - d0_); while (__builtin_amdgcn_s_memrealtime() < e_) __builtin_amdgcn_s_sleep(1); } } while (0)
__global__ __launch_bounds__(256, 1) void k_fused(const u16* __restrict__ x, const u16* __restrict__ lre, const u16* __restrict__ lim,
                                                  const u16* __restrict__ ldt, const u16* __restrict__ bre, const u16* __restrict__ bim,
                                                  const u16* __restrict__ wqkv, const u16* __restrict__ gw, const u16* __restrict__ cre,
                                                  const u16* __restrict__ cim, const u16* __restrict__ wo, const u16* __restrict__ dskip,
                                                  char* __restrict__ ws, float* __restrict__ out) {
  extern __shared__ __attribute__((aligned(16))) char lds_[];
  LAS char* lds = (LAS char*)lds_;
  const unsigned ldsb = (unsigned)(size_t)lds;
  asm volatile("" ::"s"(x), "s"(lre), "s"(lim), "s"(ldt), "s"(bre), "s"(bim), "s"(wqkv), "s"(gw), "s"(ws));
  const int tid = threadIdx.x, lane = tid & 63;
  const int w = __builtin_amdgcn_readfirstlane(tid >> 6);
  const int xcd = blockIdx.x & 7, idx = blockIdx.x >> 3;
  const int rb = 4 * (xcd & 3) + (idx & 3), cg = 8 * (xcd >> 2) + (idx >> 2);
  const int r = lane & 15, g = lane >> 4;
  GAS unsigned* tmo = (GAS unsigned*)(ws + WS_TMO);
  STAMP_DECL
  STAMP(0);
  CSTAMP(30);

  constexpr unsigned SLOT = 20480;
  constexpr unsigned MISC = 0, A0 = 10240, A1 = A0 + SLOT, A2 = A0 + 2 * SLOT, A3 = A0 + 3 * SLOT, B0 = A0 + 4 * SLOT, B1 = A0 + 5 * SLOT,
                     B2 = A0 + 6 * SLOT;
  constexpr unsigned L_E = MISC;
  constexpr unsigned L_G = MISC + 5120;
  constexpr unsigned L_KVT = MISC + 5632;
  constexpr unsigned L_DUMMY = MISC + 7424;
  constexpr unsigned L_H = MISC + 7680;
  constexpr unsigned L_C = MISC + 8704;
  constexpr unsigned ASLOT[4] = {A0, A1, A2, A3};
  constexpr unsigned BSLOT[3] = {B0, B1, B2};
  constexpr unsigned KVSLOT[5] = {B2, A2, B0, A3, A0};
  constexpr unsigned QSLOT[5] = {B1, B1 + 12288, B1 + 24576, B1 + 36864, B0};

  unsigned xoff[2];
  {
    const int l3 = lane >> 3;
    const unsigned row = 128 * rb + 32 * w + 8 * (l3 >> 2) + (l3 & 3), sw = ((lane & 7) ^ (l3 >> 1)) << 4;
    xoff[0] = row * 1280 + sw;
    xoff[1] = (row + 16) * 1280 + (sw ^ 64);
  }
  const char* const xb1 = (const char*)x + 4 * 1280;
  unsigned boff[5];
  const u16* bbase[5];
#pragma unroll
  for (int i = 0; i < 5; ++i) {
    const int n = 5 * w + i;
    const int tt = n >> 2, rr = 4 * (n & 3) + g;
    const int isim = tt < 4 ? (tt & 1) : (n >= 18);
    const int cbase = tt < 4 ? 16 * (tt >> 1) : 32, cmask = tt < 4 ? 15 : 7;
    const int chl = cbase + (rr & cmask);
    bbase[i] = isim ? bim : bre;
    boff[i] = (40 * cg + chl) * 1280 + ((r ^ rr) << 4);
  }
  const unsigned xdst = ldsb + 32 * w * 128, bdst = ldsb + 5 * w * 1024, qdst = ldsb + 3 * w * 1024;
  auto gx = [&](auto S, auto N) { constexpr int s = S, n = N; glds<0, ASLOT[s & 3] + n * 1024, s * 128>(n < 2 ? (const char*)x : xb1, xoff[n & 1], xdst); };
  auto gb = [&](auto BE, auto I) { constexpr int be = BE, i = I; glds<0, BSLOT[be % 3] + i * 1024, be * 256>(bbase[i], boff[i], bdst); };
  const unsigned lin = lane * 16;
  const char* kvsrc = ws + WS_WKV + cg * 102400 + w * 5120 + 2048;
  const char* qsrc = ws + WS_WQ + cg * 61440 + w * 3072 + 1024;
  auto gkv = [&](auto J, auto I) { constexpr int j = J, i = I; glds<1, KVSLOT[j] + i * 1024, i * 1024 - 2048>(kvsrc + j * 20480, lin, bdst); };
  auto gq = [&](auto M, auto I) { constexpr int m = M, i = I; glds<1, QSLOT[m] + i * 1024, i * 1024 - 1024>(qsrc + m * 12288, lin, qdst); };

  unsigned p_lre, p_lim, p_ldt, p_ep, p_gw;
  {
    asm volatile("global_load_dword %0, %1, %2 sc1" : "=v"(p_ep) : "v"(WS_EPOCH), "s"(ws) : "memory");
    const int l39 = lane < 40 ? lane : 39, ch = 40 * cg + l39;
    asm volatile("global_load_ushort %0, %1, %2" : "=v"(p_lre) : "v"(2 * ch), "s"(lre) : "memory");
    asm volatile("global_load_ushort %0, %1, %2" : "=v"(p_lim) : "v"(2 * ch), "s"(lim) : "memory");
    asm volatile("global_load_ushort %0, %1, %2" : "=v"(p_ldt) : "v"(2 * ch), "s"(ldt) : "memory");
    asm volatile("global_load_ushort %0, %1, %2" : "=v"(p_gw) : "v"(2 * (40 * rb + l39)), "s"(gw) : "memory");
  }
  sfor<0, 4>([&](auto N) { gx(IC<0>{}, N); });
  sfor<0, 5>([&](auto I) { gb(IC<0>{}, I); });
  SB();
  u32x4 cq[3];
  u32x2 ct[3];
  unsigned csrc[3], cdst[3];
  const int cj = tid & 15, crow0 = tid >> 4;
  const bool ctail = (0x4300u >> cj) & 1u, cgate = cj == 15;
  const int crow[3] = {crow0, crow0 + 16, 32 + (crow0 & 7)};
  sfor<0, 4>([&](auto N) { gx(IC<1>{}, N); });
  {
    const unsigned i = idx * 4 + w;
    u64 pc;
    unsigned clen;
    asm volatile("s_getpc_b64 %0\n.Lkf_pc:\n\ts_mov_b32 %1, .Lkf_end-.Lkf_pc" : "=s"(pc), "=s"(clen));
    const char* cp = (const char*)pc + (min(i * 384 + (lane & 3) * 128, clen - 4u) & ~3u);
    const char* tp = (lane & 1) ? (const char*)out + min(i, 79u) * 65536 : ws + WS_A + min(i, 119u) * 65536;
    touch(lane >= 4 && lane < 7 ? cp : tp, ldsb + L_DUMMY);
  }
  {
    const unsigned sec = (0x0009aa55u >> (2 * cj)) & 3u;
    const unsigned off4 = (unsigned)(0x0475204475207520ull >> (4 * cj)) & 15u;
    const unsigned colb = 2 * (sec * 640 + 40 * cg + 4 * off4) + 40 * rb * 3840;
    const unsigned rbytes = cj >= 10 ? 96 : 160;
    const unsigned dbase = cj >= 10 ? WS_WQ + cg * 61440 + (cj - 10) * 16 : WS_WKV + cg * 102400 + cj * 16;
#pragma unroll
    for (int it = 0; it < 3; ++it) {
      const unsigned k = 40 * rb + crow[it];
      csrc[it] = colb + crow[it] * 3840;
      cdst[it] = dbase + (k ^ (((k >> 3) & 1) << 2)) * rbytes;
    }
    asm volatile("" : "+v"(csrc[0]), "+v"(csrc[1]), "+v"(csrc[2]), "+v"(cdst[0]), "+v"(cdst[1]), "+v"(cdst[2]));
  }
  auto cload = [&](auto N) {
    constexpr int n = N, it = n >> 1;
    if constexpr (n & 1) ct[it] = ld8(wqkv, ctail ? csrc[it] + 40 : 0u);
    else cq[it] = ld16(wqkv, csrc[it]);
  };
  auto cstore = [&](auto IT) {
    constexpr int it = IT;
    u32x4 v = cq[it];
    const unsigned gwv = __shfl(p_gw, crow[it]);
    v[0] = cgate ? gwv : v[0]; v[1] = cgate ? 0u : v[1];
    v[2] = ctail ? ct[it][0] : (cgate ? 0u : v[2]); v[3] = ctail ? ct[it][1] : (cgate ? 0u : v[3]);
    store16_sc1(ws, cdst[it], v);
  };
  STAMP(1);

  f32x2 c_a, c_a8;
  float c_lmag, c_rev;
  f32x2 pa[3], pc[3], pw[3];
  f32x2 pj[4];
  asm volatile("s_waitcnt vmcnt(%5)" : "+v"(p_lre), "+v"(p_lim), "+v"(p_ldt), "+v"(p_ep), "+v"(p_gw) : "n"(13 + TCN) : "memory");
  const unsigned tag = __builtin_amdgcn_readfirstlane(p_ep) + 1u;
  {
    const float lr = bf2f(p_lre), li = bf2f(p_lim);
    const float dt = rbf(__expf(bf2f(p_ldt)));
    c_lmag = dt * lr * 1.4426950408889634f; c_rev = dt * li * 0.15915494309189535f;
    const float er = __builtin_amdgcn_exp2f(c_lmag), rev = __builtin_amdgcn_fractf(c_rev);
    c_a = f32x2{er * __builtin_amdgcn_cosf(rev), er * __builtin_amdgcn_sinf(rev)};
    const float nr = c_a.x - 1.f, ni = c_a.y, iden = 1.f / (lr * lr + li * li);
    const float cr = (nr * lr + ni * li) * iden, ci = (ni * lr - nr * li) * iden;
    c_a8 = csq(csq(csq(c_a)));
#pragma unroll
    for (int p = 0; p < 3; ++p) {
      const int src = p < 2 ? 16 * p + r : 32 + (r & 7);
      pa[p] = f32x2{__shfl(c_a.x, src), __shfl(c_a.y, src)};
      pc[p] = f32x2{2.f * __shfl(cr, src), 2.f * __shfl(ci, src)};
    }
    asm volatile("" : "+v"(pa[0]), "+v"(pa[1]), "+v"(pa[2]), "+v"(pc[0]), "+v"(pc[1]), "+v"(pc[2]), "+v"(c_a8),
                 "+v"(c_lmag), "+v"(c_rev));
  }
  STAMP(2);

  bf16x8 xa[20][2];
  f32x4 accS[5][2];
#pragma unroll
  for (int t = 0; t < 5; ++t) { accS[t][0] = f32x4{0, 0, 0, 0}; accS[t][1] = f32x4{0, 0, 0, 0}; }
  bf16x8 bq[2][5];
  LAS char* xrd[2];
  LAS char* brd[4];
#pragma unroll
  for (int kk = 0; kk < 2; ++kk) xrd[kk] = lds + A0 + (32 * w + r) * 128 + (((4 * kk + g) ^ ((r >> 1) & 7)) << 4);
#pragma unroll
  for (int kq = 0; kq < 4; ++kq) brd[kq] = lds + B0 + r * 256 + (((4 * kq + g) ^ r) << 4);
  auto rd1 = [&](auto KS, auto I) {
    constexpr int ks = KS, i = I;
    if constexpr (i < 2) xa[ks][i] = lds_frag(xrd[ks & 1] + (ASLOT[(ks >> 1) & 3] - A0) + i * 2048);
    else bq[ks & 1][i - 2] = lds_frag(brd[ks & 3] + (BSLOT[(ks >> 2) % 3] - B0) + (i - 2) * 4096);
  };
  unsigned cf = 0;
  const GAS unsigned* cfp = (const GAS unsigned*)(ws + WS_CFLAG) + cg * 16 + r;

  auto dma1 = [&](auto S, auto J) {
    constexpr int s = S, j = J;
    if constexpr (s == 0) {
      if constexpr (j < 4) gx(IC<2>{}, J); else if constexpr (j < 9) gb(IC<1>{}, IC<j - 4>{}); else if constexpr (j < 15) cload(IC<j - 9>{});
    } else if constexpr (s == 1) {
      if constexpr (j < 4) gx(IC<3>{}, J);
      else if constexpr (j < 8) gx(IC<4>{}, IC<j - 4>{});
      else if constexpr (j < 13) gb(IC<2>{}, IC<j - 8>{});
    } else if constexpr (s == 2 && j >= 100) {
      cstore(IC<j - 100>{});
    } else if constexpr (s <= 6) {
      if constexpr (j < 4) gx(IC<s + 3>{}, J);
      else if constexpr (s <= 5) {
        constexpr int be = 2 + (s >> 1), i = (s & 1) ? j - 1 : j - 4;
        if constexpr ((s & 1) ? j < 6 : j < 7) gb(IC<be>{}, IC<i>{});
      } else if constexpr (j == 4) {
        cf = ld4_sc1(ws, WS_CFLAG + (cg * 16 + r) * 4);
      }
    } else if constexpr (s == 7) {
      const u32x2 z = {0u, tag - 1u};
      if constexpr (j < 4) store8_sc1(ws, WS_PART + ((cg * 16 + rb) * 848 + min(tid + 256 * j, 847)) * 8, z);
      else if constexpr (j == 4) store8_sc1(ws, WS_AGG + ((cg * 16 + rb) * 80 + min(tid, 79)) * 8, z);
      else if constexpr (j == 5) store8_sc1(ws, WS_FIN + (cg * 848 + min(53 * rb + min(tid, 52), 847)) * 8, z);
    } else if constexpr (s == 8) { if constexpr (j < 5) gkv(IC<0>{}, J); }
    else if constexpr (s == 9) { if constexpr (j < 5) gkv(IC<1>{}, J); }
  };

  SYNC(4 + TCN);
  STAMP(3);
  sfor<0, 7>([&](auto I) { rd1(IC<0>{}, I); });
  SB();
  sfor<0, 20>([&](auto KS) {
    constexpr int ks = KS, s = (ks + 1) >> 1, first = ks & 1;
    if constexpr (first && ks + 1 < 20) {
      if constexpr (s == 1) VWAIT(15 + TCN);
      if constexpr (s == 2) {
        asm volatile("s_waitcnt vmcnt(13)" : "+v"(cq[0]), "+v"(cq[1]), "+v"(cq[2]), "+v"(ct[0]), "+v"(ct[1]), "+v"(ct[2])::"memory");
        SYNC(13);
        sfor<0, 3>([&](auto IT) { dma1(IC<2>{}, IC<100 + IT>{}); });
        STAMP(4);
      }
      if constexpr (s == 3) VWAIT(19);
      if constexpr (s == 4) {
        SYNC(13);
        if (tid == 0) __hip_atomic_store((GAS unsigned*)(ws + WS_CFLAG) + cg * 16 + rb, tag, __ATOMIC_RELAXED, __HIP_MEMORY_SCOPE_AGENT);
      }
      if constexpr (s == 5) { VWAIT(16); STAMP(5); }
      if constexpr (s == 6) SYNC(13);
      if constexpr (s == 7) VWAIT(14);
      if constexpr (s == 8) {
        asm volatile("s_waitcnt vmcnt(6)" : "+v"(cf)::"memory");
        SYNC(6);
        if (!__all(cf == tag)) {
          unsigned spins = 0;
          for (;;) {
            cf = __hip_atomic_load(cfp, __ATOMIC_RELAXED, __HIP_MEMORY_SCOPE_AGENT);
            if (__all(cf == tag)) break;
            if (spin_fail(spins, tmo, tag)) break;
          }
        }
        STAMP(6);
      }
    }
    sfor<0, 10>([&](auto I) {
      constexpr int i = I;
      accS[i >> 1][i & 1] = mfma(xa[ks][i & 1], bq[ks & 1][i >> 1], accS[i >> 1][i & 1]);
      if constexpr (ks + 1 < 20 && i < 7) rd1(IC<ks + 1>{}, I);
      if constexpr (ks == 0) { if constexpr (i < 9) dma1(IC<0>{}, I); else sfor<9, 15>([&](auto J) { dma1(IC<0>{}, J); }); }
      else if constexpr (ks < 19) {
        constexpr int gi = first ? i : 10 + i;
        if constexpr (s == 1) { if constexpr (gi < 13) dma1(IC<1>{}, IC<gi>{}); }
        else if constexpr (s == 8 || s == 9) { if constexpr (gi % 4 == 1) dma1(IC<s>{}, IC<gi / 4>{}); }
        else if constexpr (gi % 3 == 1) dma1(IC<s>{}, IC<gi / 3>{});
      }
      SB();
    });
  });
  STAMP(7);

  f32x2 ub[3][8];
  {
#pragma unroll
    for (int t = 0; t < 8; ++t) {
      ub[0][t] = f32x2{accS[0][t >> 2][t & 3], accS[1][t >> 2][t & 3]};
      ub[1][t] = f32x2{accS[2][t >> 2][t & 3], accS[3][t >> 2][t & 3]};
      const float v = accS[4][t >> 2][t & 3];
      const float o = __builtin_amdgcn_update_dpp(0.f, v, 0x128, 0xf, 0xf, false);
      ub[2][t] = r < 8 ? f32x2{v, o} : f32x2{o, v};
    }
    f32x2 e[3] = {ub[0][0], ub[1][0], ub[2][0]};
    SB();
#pragma unroll
    for (int t = 1; t < 8; ++t) {
      f32x2 t1[3];
#pragma unroll
      for (int p = 0; p < 3; ++p) t1[p] = cfma_1(pa[p], e[p], ub[p][t]);
      SB();
#pragma unroll
      for (int p = 0; p < 3; ++p) e[p] = cfma_2(pa[p], e[p], t1[p]);
      SB();
    }
    LAS char* ep = lds + L_E + ((4 * w + g) * 40 + r) * 8;
    *(LAS f32x2*)ep = e[0];
    *(LAS f32x2*)(ep + 128) = e[1];
    if (r < 8) *(LAS f32x2*)(ep + 256) = e[2];
  }
  STAMP(8);

  f32x4 accK[5][2];
#pragma unroll
  for (int t = 0; t < 5; ++t) { accK[t][0] = f32x4{0, 0, 0, 0}; accK[t][1] = f32x4{0, 0, 0, 0}; }
  const int q4 = r >> 2, pp = r & 3;
  LAS char* kv0 = lds + (8 * g + q4) * 160 + 8 * pp + 640 * (g & 1);
  LAS char* kv1 = lds + (8 * g + q4) * 160 + 8 * pp + 640 * (1 - (g & 1));
  bf16x4 bh[2][5][2];
  LAS char* kb[2][2];
  auto base2 = [&](auto J) {
    constexpr int j = J;
    kb[j & 1][0] = kv0 + KVSLOT[j]; kb[j & 1][1] = kv1 + KVSLOT[j];
    asm volatile("" : "+v"(kb[j & 1][0]), "+v"(kb[j & 1][1]));
  };
  auto rd2 = [&](auto KS, auto I) {
    constexpr int ks = KS, i = I;
    constexpr unsigned o = (ks & 3) * 5120 + 32 * (i >> 1);
    bh[ks & 1][i >> 1][i & 1] = __builtin_amdgcn_ds_read_tr16_b64_v4i16((LAS bf16x4*)(kb[(ks >> 2) & 1][i & 1] + o));
  };
  auto cat = [](bf16x4 a, bf16x4 b) { return __builtin_shufflevector(a, b, 0, 1, 2, 3, 4, 5, 6, 7); };
  const unsigned t2l = min(lane, 49), t2off = (20 * idx + 10 * (w >> 1) + ((t2l * 6554) >> 16)) * 1280 + (t2l - 10 * ((t2l * 6554) >> 16)) * 128;
  const char* t2p = (const char*)((w & 1) ? cim : cre);
  u32x4 cw;
  unsigned cwsrc, cwdst;
  {
    const unsigned sc = 200 * (16 * rb + cg) + min(tid, 199);
    cwsrc = sc * 16;
    const unsigned k = (sc * 52429u) >> 22, cc = sc - 80 * k, jw = (cc * 6554) >> 16, ch = cc - 10 * jw;
    const unsigned cgq = (k * 1639) >> 16, rem = k - 40 * cgq, hh = rem >= 20, e = rem - 20 * hh;
    const unsigned j = 4 * hh + (e & 3), qq = 5 * cgq + (e >> 2), oc = qq & 3;
    cwdst = WS_WO + ((jw * 20 + (qq >> 2)) * 32 + 8 * oc + (j ^ ((oc & 1) << 2))) * 160 + ch * 16;
  }
  auto dma2 = [&](auto J, auto N) {
    constexpr int j = J, n = N;
    if constexpr (j == 0) { if constexpr (n < 10) gkv(IC<2 + n / 5>{}, IC<n % 5>{}); else if constexpr (n == 10) cw = ld16(wo, cwsrc); }
    else if constexpr (j == 1) {
      if constexpr (n < 5) gkv(IC<4>{}, N);
      else if constexpr (n < 5 + T2N) touch_s(t2p + (n - 5) * 6400, t2off, ldsb + L_DUMMY);
    }
    else if constexpr (j == 2) { if constexpr (n < 6) gq(IC<n / 3>{}, IC<n % 3>{}); }
    else if constexpr (j == 3) { if constexpr (n < 6) gq(IC<2 + n / 3>{}, IC<n % 3>{}); else if constexpr (n == 6) store16_sc1(ws, cwdst, cw); }
    else { if constexpr (n < 3) gq(IC<4>{}, N); }
  };
  base2(IC<0>{});
  SYNC(5);
  STAMP(9);
  sfor<0, 10>([&](auto I) { rd2(IC<0>{}, I); });
  SB();
  sfor<0, 20>([&](auto KS) {
    constexpr int ks = KS, j = (ks + 1) >> 2, ph = (ks + 1) & 3;
    if constexpr (ph == 0 && ks + 1 < 20) {
      base2(IC<j>{});
      if constexpr (j == 1) SYNC(11);
      if constexpr (j == 2) SYNC(11 + T2N);
      if constexpr (j == 3) { asm volatile("s_waitcnt vmcnt(%1)" : "+v"(cw) : "n"(11 + T2N) : "memory"); SYNC(11 + T2N); }
      if constexpr (j == 4) SYNC(13 + T2N);
    }
    sfor<0, 10>([&](auto I) {
      constexpr int i = I;
      accK[i >> 1][i & 1] = mfma(xa[ks][i & 1], cat(bh[ks & 1][i >> 1][0], bh[ks & 1][i >> 1][1]), accK[i >> 1][i & 1]);
      if constexpr (ks + 1 < 20) rd2(IC<ks + 1>{}, I);
      if constexpr (j == 0) { if constexpr (i % 3 == 1 || i == 9) dma2(IC<0>{}, IC<4 * ks + (i == 9 ? 3 : i / 3)>{}); }
      else if constexpr (ph < 3 && ks < 19 && (i == 1 || i == 4 || i == 7)) dma2(IC<j>{}, IC<3 * ph + (i - 1) / 3>{});
      SB();
    });
  });
  STAMP(10);

  {
    bf16x8 kf[5];
#pragma unroll
    for (int t = 0; t < 5; ++t) {
      unsigned u[4];
#pragma unroll
      for (int h2 = 0; h2 < 4; ++h2) {
        const float a = accK[t][h2 >> 1][(h2 & 1) * 2], b = accK[t][h2 >> 1][(h2 & 1) * 2 + 1];
        if (t == 2 || t == 3) u[h2] = pk2(a, b);
        else if (t < 2) u[h2] = phi2(a, b);
        else { const unsigned ph = phi2(a, b), raw = pk2(a, b); u[h2] = r < 8 ? ph : raw; }
      }
      kf[t] = frag4(u[0], u[1], u[2], u[3]);
      *(LAS bf16x8*)(lds + A3 + (w * 5 + t) * 1024 + lane * 16) = kf[t];
    }
    SYNC(0);
    const bf16x8 ones = frag4(0x3f803f80u, 0x3f803f80u, 0x3f803f80u, 0x3f803f80u);
    const char* part = ws + WS_PART + (cg * 16 + rb) * 848 * 8;
    LAS char* st = lds + A3 + lane * 16;
    auto job = [&](LAS char* pa_, LAS char* pb_, bool onesb) {
      f32x4 d = {0, 0, 0, 0};
#pragma unroll
      for (int sw = 0; sw < 4; ++sw) {
        const bf16x8 a = lds_frag(pa_ + sw * 5120);
        bf16x8 b = lds_frag(pb_ + sw * 5120);
        if (onesb && r < 8) b = ones;
        d = mfma(a, b, d);
      }
      return d;
    };
    if (w < 2) {
      const int h = w;
      const f32x4 d0 = job(st + h * 1024, st + (2 + h) * 1024, false);
      const f32x4 d1 = job(st + 4096, st + (2 + h) * 1024, false);
      const unsigned o0 = (420 * h + 80 * g + r) * 8, o1 = (420 * h + 320 + r) * 8;
      sfor<0, 4>([&](auto I) { constexpr int i = I; gstore<160 * i>(part, o0, d0[i], tag); });
      if (g == h) sfor<0, 4>([&](auto I) { constexpr int i = I; gstore<160 * i>(part, o1, d1[i], tag); });
    } else if (w == 2) {
      const f32x4 d0 = job(st, st + 4096, true);
      const f32x4 d1 = job(st + 1024, st + 4096, true);
      const f32x4 d2 = job(st + 4096, st + 4096, true);
      const bool k0 = (r >> 2) == 2, k1 = (r >> 2) == 3, kq = (r >> 2) == 2 + g;
      const unsigned o0 = (k0 ? 80 * g + 16 + (r & 3) : 400 + 4 * g) * 8, st0 = k0 ? 160 : 8;
      const unsigned o1 = (420 + (k1 ? 80 * g + 16 + (r & 3) : 400 + 4 * g)) * 8, st1 = k1 ? 160 : 8;
      const unsigned o2 = (420 * g + (kq ? 336 + (r & 3) : 416)) * 8, st2 = kq ? 160 : 8;
      if (k0 || r == 0) sfor<0, 4>([&](auto I) { constexpr int i = I; gstore<0>(part, o0 + i * st0, d0[i], tag); });
      if (k1 || r == 0) sfor<0, 4>([&](auto I) { constexpr int i = I; gstore<0>(part, o1 + i * st1, d1[i], tag); });
      if (g < 2 && (kq || r == 0)) sfor<0, 4>([&](auto I) { constexpr int i = I; gstore<0>(part, o2 + i * st2, d2[i], tag); });
    } else {
      if (lane < 40) {
        f32x2 e[16];
#pragma unroll
        for (int s = 0; s < 16; ++s) e[s] = *(LAS f32x2*)(lds + L_E + (s * 40 + lane) * 8);
        f32x2 run = {0.f, 0.f};
#pragma unroll
        for (int s = 0; s < 16; ++s) {
          *(LAS f32x2*)(lds + L_E + (s * 40 + lane) * 8) = run;
          run = cfma(c_a8, run, e[s]);
        }
        const char* ag = ws + WS_AGG + (cg * 16 + rb) * 640;
        gstore<0>(ag, lane * 16, run.x, tag); gstore<8>(ag, lane * 16, run.y, tag);
      }
    }
  }
  STAMP(11);

  const int T = 4 * xcd + (idx & 3), jt = idx >> 2;
  constexpr unsigned UNIT = 10240, R1 = B0 + 25600;
  const unsigned dA = ldsb + A0 + w * 2 * UNIT, dB = ldsb + R1 + w * UNIT;
  const char* cb = sptr((w & 1) ? cim : cre, (w >> 1) * 128);
  unsigned coff[2];
  {
    const int l3 = lane >> 3;
    coff[0] = (80 * jt + l3) * 1280 + (((lane & 7) ^ (l3 >> 1)) << 4);
    coff[1] = coff[0] ^ 64;
  }
  auto gc = [&](auto M, auto N) {
    constexpr int m = M, n = N;
    if constexpr (m == 2) glds<0, n * 1024, m * 256>(cb + n * 10240, coff[n & 1], dB);
    else glds<0, (m == 1 || m == 4 ? UNIT : 0) + n * 1024, m * 256>(cb + n * 10240, coff[n & 1], dA);
  };

  f32x4 accQ[3][2];
#pragma unroll
  for (int t = 0; t < 3; ++t) { accQ[t][0] = f32x4{0, 0, 0, 0}; accQ[t][1] = f32x4{0, 0, 0, 0}; }
  LAS char* q0 = lds + (8 * g + q4) * 96 + 8 * pp + 384 * (g & 1);
  LAS char* q1 = lds + (8 * g + q4) * 96 + 8 * pp + 384 * (1 - (g & 1));
  auto base3 = [&](auto M) {
    constexpr int m = M;
    kb[m & 1][0] = q0 + QSLOT[m]; kb[m & 1][1] = q1 + QSLOT[m];
    asm volatile("" : "+v"(kb[m & 1][0]), "+v"(kb[m & 1][1]));
  };
  auto rd3 = [&](auto KS, auto I) {
    constexpr int ks = KS, i = I;
    constexpr unsigned o = (ks & 3) * 3072 + 32 * (i >> 1);
    bh[ks & 1][i >> 1][i & 1] = __builtin_amdgcn_ds_read_tr16_b64_v4i16((LAS bf16x4*)(kb[(ks >> 2) & 1][i & 1] + o));
  };
  const int i1 = 53 * rb + lane;
  const bool act1 = lane < 53 && i1 < 840;
  const unsigned x1off = (act1 ? i1 : 0) * 8, x2off = min(lane, 39) * 16;
  const char* x1src = sptr(ws, WS_PART + (cg * 16 + 4 * w) * 6784);
  const char* x2src = sptr(ws, WS_AGG + cg * 16 * 640);
  u32x2 xh[4];
  u32x4 xl[4];
  auto xload = [&](auto N) {
    constexpr int n = N;
    if constexpr (n < 4) xh[n] = ld8_sc1(x1src + n * 6784, x1off);
    else { const int j = w + 4 * (n - 4); xl[n - 4] = ld16u_sc1(x2src + (j < rb ? j : 0) * 640, x2off); }
  };
  base3(IC<0>{});
  SB();
  sfor<0, 6>([&](auto I) { rd3(IC<0>{}, I); });
  SB();
  sfor<0, 20>([&](auto KS) {
    constexpr int ks = KS;
    if constexpr (((ks + 1) & 3) == 0 && ks + 1 < 20) { base3(IC<(ks + 1) / 4>{}); SB(); }
    if constexpr (ks == 1) { asm volatile("s_barrier" ::: "memory"); SB(); }
    sfor<0, 6>([&](auto I) {
      constexpr int i = I;
      accQ[i >> 1][i & 1] = mfma(cat(bh[ks & 1][i >> 1][0], bh[ks & 1][i >> 1][1]), xa[ks][i & 1], accQ[i >> 1][i & 1]);
      if constexpr (ks + 1 < 20) rd3(IC<ks + 1>{}, I);
      if constexpr (ks >= 2 && ks < 12 && (i == 1 || i == 4)) gc(IC<(ks - 2) / 5>{}, IC<2 * ((ks - 2) % 5) + (i == 4)>{});
      if constexpr (ks >= 14 && ks < 18 && (i == 1 || i == 4)) xload(IC<2 * (ks - 14) + (i == 4)>{});
      SB();
    });
  });
  STAMP(12);

  float gv[2];
#pragma unroll
  for (int mt = 0; mt < 2; ++mt) {
    const float s = rbf(accQ[2][mt][0]);
    const float gg = rbf(__builtin_amdgcn_rcpf(1.f + __expf(-s)));
    const int tl = 32 * w + 8 * (r >> 2) + 4 * mt + (r & 3);
    if (g == 2) {
      *(LAS float*)(lds + L_G + tl * 4) = gg;
      if (cg == 0) store4_sc1(ws, WS_G + (128 * rb + tl) * 4, __float_as_uint(gg));
    }
    gv[mt] = __shfl(gg, 32 + r);
  }
  {
    auto cpow = [](float lmag, float rev, float n) {
      const float m = __builtin_amdgcn_exp2f(n * lmag), f = __builtin_amdgcn_fractf(n * rev);
      return f32x2{m * __builtin_amdgcn_cosf(f), m * __builtin_amdgcn_sinf(f)};
    };
    const float nseg = (float)(8 * (4 * w + g));
#pragma unroll
    for (int p = 0; p < 3; ++p) {
      const int src = p < 2 ? 16 * p + r : 32 + (r & 7);
      pw[p] = cpow(__shfl(c_lmag, src), __shfl(c_rev, src), nseg);
    }
#pragma unroll
    for (int i = 0; i < 4; ++i) pj[i] = cpow(c_lmag, c_rev, (float)(128 * max(rb - 1 - w - 4 * i, 0)));
    asm volatile("" : "+v"(pw[0]), "+v"(pw[1]), "+v"(pw[2]), "+v"(pj[0]), "+v"(pj[1]), "+v"(pj[2]), "+v"(pj[3]), "+v"(gv[0]), "+v"(gv[1]));
  }
  STAMP(13);

  asm volatile("s_waitcnt vmcnt(0)"
               : "+v"(xh[0]), "+v"(xh[1]), "+v"(xh[2]), "+v"(xh[3]), "+v"(xl[0]), "+v"(xl[1]), "+v"(xl[2]), "+v"(xl[3])::"memory");
  {
    unsigned spins = 0;
    for (;;) {
      bool ok = true;
#pragma unroll
      for (int n = 0; n < 4; ++n) {
        ok &= xh[n][1] == tag || !act1;
        ok &= (xl[n][1] == tag && xl[n][3] == tag) || w + 4 * n >= rb;
      }
      if (__all(ok)) break;
      if (spin_fail(spins, tmo, tag)) break;
#pragma unroll
      for (int n = 0; n < 4; ++n) {
        const int j = w + 4 * n;
        xh[n] = ld8_wait(x1src + n * 6784, x1off);
        xl[n] = ld16_wait(x2src + (j < rb ? j : 0) * 640, x2off);
      }
    }
    *(LAS float*)(lds + L_H + tid * 4) =
        (__uint_as_float(xh[0][0]) + __uint_as_float(xh[1][0])) + (__uint_as_float(xh[2][0]) + __uint_as_float(xh[3][0]));
    f32x2 c = {0.f, 0.f};
#pragma unroll
    for (int n = 0; n < 4; ++n)
      if (w + 4 * n < rb) c = cfma(pj[n], f32x2{__uint_as_float(xl[n][0]), __uint_as_float(xl[n][2])}, c);
    if (lane < 40) *(LAS f32x2*)(lds + L_C + (w * 40 + lane) * 8) = c;
  }
  LAS char* crA[2];
  LAS char* crB[2];
#pragma unroll
  for (int kk = 0; kk < 2; ++kk) {
    const int lp = r * 128 + (((4 * kk + g) ^ ((r >> 1) & 7)) << 4);
    crA[kk] = lds + A0 + w * 2 * UNIT + lp; crB[kk] = lds + R1 + w * UNIT + lp;
  }
  asm volatile("" : "+v"(crA[0]), "+v"(crA[1]), "+v"(crB[0]), "+v"(crB[1]));
  bf16x8 bu[4][5];
#pragma unroll
  for (int kp = 0; kp < 4; ++kp)
#pragma unroll
    for (int nt = 0; nt < 5; ++nt) bu[kp][nt] = lds_frag(crA[kp & 1] + (kp >> 1) * UNIT + nt * 2048);
  STAMP(14);
  LSYNC();
  STAMP(15);
  if (w == 0) {
    const float s = (*(LAS float*)(lds + L_H + lane * 4) + *(LAS float*)(lds + L_H + 256 + lane * 4)) +
                    (*(LAS float*)(lds + L_H + 512 + lane * 4) + *(LAS float*)(lds + L_H + 768 + lane * 4));
    if (act1) gstore<0>(sptr(ws, WS_FIN + cg * 848 * 8), i1 * 8, s, tag);
  }

  u32x2 fv[4];
  const unsigned foff[4] = {(unsigned)tid * 8, (unsigned)(tid + 256) * 8, (unsigned)(tid + 512) * 8, (unsigned)(tid < 72 ? tid + 768 : 0) * 8};
  const char* fsrc = sptr(ws, WS_FIN + cg * 848 * 8);
  {
    const f32x4 ga = *(LAS f32x4*)(lds + L_G + (32 * w + 8 * g) * 4), gb4 = *(LAS f32x4*)(lds + L_G + (32 * w + 8 * g + 4) * 4);
    LAS char* img = lds + B0 + w * 6400;
    f32x2 h[3];
#pragma unroll
    for (int p = 0; p < 3; ++p) {
      const int chl = p < 2 ? 16 * p + r : 32 + (r & 7);
      LAS char* cp = lds + L_C + chl * 8;
      const f32x2 cin = (*(LAS f32x2*)cp + *(LAS f32x2*)(cp + 320)) + (*(LAS f32x2*)(cp + 640) + *(LAS f32x2*)(cp + 960));
      h[p] = cfma(pw[p], cin, *(LAS f32x2*)(lds + L_E + ((4 * w + g) * 40 + chl) * 8));
    }
    SB();
    sfor<0, 8>([&](auto TT) {
      constexpr int t = TT;
      f32x2 t1[3];
#pragma unroll
      for (int p = 0; p < 3; ++p) t1[p] = cfma_1(pa[p], h[p], ub[p][t]);
      SB();
#pragma unroll
      for (int p = 0; p < 3; ++p) h[p] = cfma_2(pa[p], h[p], t1[p]);
      SB();
#pragma unroll
      for (int p = 0; p < 3; ++p) t1[p] = cco_1(pc[p], h[p]);
      SB();
#pragma unroll
      for (int p = 0; p < 3; ++p) t1[p] = cco_2(pc[p], h[p], t1[p]);
      SB();
#pragma unroll
      for (int p = 0; p < 3; ++p) ub[p][t] = t1[p] * (t < 4 ? ga[t & 3] : gb4[t & 3]);
      gc(IC<2>{}, TT);
      SB();
    });
#pragma unroll
    for (int p = 0; p < 3; ++p) {
#pragma unroll
      for (int mt = 0; mt < 2; ++mt) {
        const f32x2* y = &ub[p][4 * mt];
        const u32x2 vr = {pk2(y[0].x, y[1].x), pk2(y[2].x, y[3].x)};
        const u32x2 vi = {pk2(y[0].y, y[1].y), pk2(y[2].y, y[3].y)};
        if (p < 2) {
          *(LAS u32x2*)(img + mt * 3200 + (16 * p + r) * 40 + 8 * g) = vr;
          *(LAS u32x2*)(img + mt * 3200 + (40 + 16 * p + r) * 40 + 8 * g) = vi;
        } else {
          const int row = r < 8 ? 32 + r : 72 + (r - 8);
          *(LAS u32x2*)(img + mt * 3200 + row * 40 + 8 * g) = r < 8 ? vr : vi;
        }
      }
    }
    gc(IC<2>{}, IC<8>{}); gc(IC<2>{}, IC<9>{});
    asm volatile("s_waitcnt lgkmcnt(0)" ::: "memory");
#pragma unroll
    for (int f = 0; f < 5; ++f) {
      const int mt = f < 4 ? (f >> 1) : (g >> 1);
      const int o8 = f < 4 ? 4 * (f & 1) + g : 8 + (g & 1);
      LAS char* p0 = img + mt * 3200 + (8 * o8 + q4) * 40 + 8 * pp;
      const bf16x8 v = lds_frag_tr(p0, p0 + 160);
      const int part = o8 >= 5, o = 5 * cg + (part ? o8 - 5 : o8);
      const int q = 16 * (o >> 3) + 8 * part + (o & 7);
      const int rt = 8 * rb + 2 * w + mt;
      store16_sc1(ws, WS_A + (rt * 240 + q) * 256 + r * 16, __builtin_bit_cast(u32x4, v));
    }
  }
  STAMP(16);

#pragma unroll
  for (int it = 0; it < 4; ++it) fv[it] = ld8_sc1(fsrc, foff[it]);
  bf16x8 qf[2][2];
#pragma unroll
  for (int mt = 0; mt < 2; ++mt) {
    unsigned tl[2];
#pragma unroll
    for (int h2 = 0; h2 < 2; ++h2) {
      const unsigned ph = phi2(accQ[2][mt][2 * h2], accQ[2][mt][2 * h2 + 1]);
      tl[h2] = g < 2 ? ph : 0u;
    }
#pragma unroll
    for (int h = 0; h < 2; ++h)
      qf[h][mt] = frag4(phi2(accQ[h][mt][0], accQ[h][mt][1]), phi2(accQ[h][mt][2], accQ[h][mt][3]), tl[0], tl[1]);
  }
  asm volatile("" : "+v"(qf[0][0]), "+v"(qf[0][1]), "+v"(qf[1][0]), "+v"(qf[1][1]));
  unsigned kdst[4];
#pragma unroll
  for (int it = 0; it < 4; ++it) {
    const int i = tid + 256 * it, h = i >= 420, ii = i - 420 * h;
    const int dd = (ii * 3277) >> 16;
    const int e = ii < 400 ? ii - 20 * dd : 20, d = ii < 400 ? dd : ii - 400;
    kdst[it] = L_KVT + (h * 21 + e) * 40 + d * 2;
  }
  asm volatile("" : "+v"(kdst[0]), "+v"(kdst[1]), "+v"(kdst[2]), "+v"(kdst[3]));
  asm volatile("s_waitcnt vmcnt(0)\n\ts_barrier" : "+v"(fv[0]), "+v"(fv[1]), "+v"(fv[2]), "+v"(fv[3])::"memory");
  if (tid == 0) __hip_atomic_store((GAS unsigned*)(ws + WS_HFLAG) + rb * 16 + cg, tag, __ATOMIC_RELAXED, __HIP_MEMORY_SCOPE_AGENT);
  {
    unsigned spins = 0;
    for (;;) {
      const bool ok = fv[0][1] == tag && fv[1][1] == tag && fv[2][1] == tag && fv[3][1] == tag;
      if (__all(ok)) break;
      if (spin_fail(spins, tmo, tag)) break;
#pragma unroll
      for (int it = 0; it < 4; ++it) fv[it] = ld8_wait(fsrc, foff[it]);
    }
#pragma unroll
    for (int it = 0; it < 4; ++it) {
      const float f = __uint_as_float(fv[it][0]);
      if (it < 3 || tid < 72) *(LAS u16*)(lds + kdst[it]) = (u16)pk2(f, f);
    }
  }
  STAMP(17);
  LSYNC();

  {
    f32x4 num[2][2], nt[2], den[2][2];
#pragma unroll
    for (int mt = 0; mt < 2; ++mt) nt[mt] = f32x4{0, 0, 0, 0};
    sfor<0, 2>([&](auto H) {
      constexpr int h = H;
      LAS char* kbp = lds + L_KVT + h * 840;
      const u32x2 z2 = {0u, 0u};
      const u32x2 ml = *(LAS u32x2*)(kbp + r * 40 + 8 * g), mh = *(LAS u32x2*)(kbp + r * 40 + 32);
      const u32x2 tl = *(LAS u32x2*)(kbp + (16 + (r & 3)) * 40 + 8 * g), th = *(LAS u32x2*)(kbp + (16 + (r & 3)) * 40 + 32);
      const u32x2 dl = *(LAS u32x2*)(kbp + 800 + 8 * g), dh = *(LAS u32x2*)(kbp + 800 + 32);
      const bool gh = g == h, rv = (r >> 2) == h;
      const u32x2 mh2 = gh ? mh : z2, dh2 = gh ? dh : z2, tl2 = rv ? tl : z2, th2 = (rv && gh) ? th : z2;
      const bf16x8 am = frag4(ml[0], ml[1], mh2[0], mh2[1]);
      const bf16x8 at = frag4(tl2[0], tl2[1], th2[0], th2[1]);
      const bf16x8 ad = frag4(dl[0], dl[1], dh2[0], dh2[1]);
#pragma unroll
      for (int mt = 0; mt < 2; ++mt) {
        num[h][mt] = mfma(am, qf[h][mt], f32x4{0, 0, 0, 0});
        nt[mt] = mfma(at, qf[h][mt], nt[mt]);
        den[h][mt] = mfma(ad, qf[h][mt], f32x4{0, 0, 0, 0});
      }
    });
    sfor<0, 2>([&](auto MT) {
      constexpr int mt = MT;
      const float og = rbf(1.f - gv[mt]);
      float sc[2];
#pragma unroll
      for (int h = 0; h < 2; ++h) sc[h] = og * __builtin_amdgcn_rcpf(den[h][mt][0] + 1e-6f);
      const int rt = 8 * rb + 2 * w + mt;
      const u32x4 ov = {pk2(num[0][mt][0] * sc[0], num[0][mt][1] * sc[0]), pk2(num[0][mt][2] * sc[0], num[0][mt][3] * sc[0]),
                        pk2(num[1][mt][0] * sc[1], num[1][mt][1] * sc[1]), pk2(num[1][mt][2] * sc[1], num[1][mt][3] * sc[1])};
      store16_sc1(ws, WS_A + (rt * 240 + 160 + 5 * cg + g) * 256 + r * 16, ov);
      const float st = g == 0 ? sc[0] : sc[1];
      const u32x2 tv = {pk2(nt[mt][0] * st, nt[mt][1] * st), pk2(nt[mt][2] * st, nt[mt][3] * st)};
      if (g < 2) store8_sc1(ws, WS_A + (rt * 240 + 160 + 5 * cg + 4) * 256 + r * 16 + 8 * g, tv);
    });
  }
  STAMP(18);

  u32x2 exv[5], edv[5];
  unsigned egv[5], eoff[5], elds[5], goff[5];
#pragma unroll
  for (int it = 0; it < 5; ++it) {
    const int item = tid + 256 * it, tp = (item * 3277) >> 16, dq = item - 20 * tp;
    const int mt = tp >> 4, rr = tp & 15;
    const int t = 64 * T + 32 * (mt >> 1) + 8 * (rr >> 2) + 4 * (mt & 1) + (rr & 3);
    const int d = 80 * jt + 4 * dq;
    eoff[it] = (t * 640 + d) * 4; elds[it] = (tp * 84 + 4 * dq) * 4;
    exv[it] = ld8(x, eoff[it] >> 1);
    edv[it] = ld8(dskip, d * 2);
    goff[it] = WS_G + t * 4;
  }
  {
    const unsigned hoff = WS_HFLAG + ((T >> 1) * 16 + r) * 4;
    unsigned spins = 0;
    for (;;) {
      const unsigned f = ld4_wait(ws, hoff);
      if (__all(f == tag)) break;
      if (spin_fail(spins, tmo, tag)) break;
    }
  }
  STAMP(19);

  const char* abase = sptr(ws, WS_A + 4 * T * 240 * 256);
  const unsigned alane = lane * 16;
  bf16x8 a[4][4];
  auto ga = [&](auto KP) {
    constexpr int kp = KP;
    const unsigned q = kp < 10 ? 32 * (kp >> 1) + 16 * (w >> 1) + 8 * (w & 1) + 4 * (kp & 1) : 160 + 16 * (kp - 10) + 4 * w;
    const char* p = abase + q * 256;
    sfor<0, 4>([&](auto MT) {
      constexpr int mt = MT;
      a[kp & 3][mt] = ld16_sc1(p + mt * 240 * 256, alane);
    });
  };
  ga(IC<0>{}); ga(IC<1>{}); ga(IC<2>{});
#pragma unroll
  for (int it = 0; it < 5; ++it) egv[it] = ld4_sc1(ws, goff[it]);
  sfor<0, 10>([&](auto N) { gc(IC<3>{}, N); });
  if (TOUCHA) {
    const unsigned li = 40 * w + min(lane, 39);
    const unsigned rtl = li / 40, rem = li - 40 * rtl;
    touch_s_sc1(abase, rtl * 240 * 256 + (20 * jt) * 256 + rem * 128, ldsb + L_DUMMY);
  }
  asm volatile("s_barrier" ::: "memory");
  if (tid == 0) {
    __hip_atomic_store((GAS unsigned*)(ws + WS_OFLAG) + rb * 16 + cg, tag, __ATOMIC_RELAXED, __HIP_MEMORY_SCOPE_AGENT);
    if (blockIdx.x == 0) __hip_atomic_store((GAS unsigned*)(ws + WS_EPOCH), tag, __ATOMIC_RELAXED, __HIP_MEMORY_SCOPE_AGENT);
  }
  asm volatile("" : "+v"(exv[0]), "+v"(exv[1]), "+v"(exv[2]), "+v"(exv[3]), "+v"(exv[4]), "+v"(edv[0]), "+v"(edv[1]), "+v"(edv[2]),
               "+v"(edv[3]), "+v"(edv[4])::"memory");
  f32x4 skip[5];
#pragma unroll
  for (int it = 0; it < 5; ++it) {
    const unsigned s0 = pk2(blo(exv[it][0]) * blo(edv[it][0]), bhi(exv[it][0]) * bhi(edv[it][0]));
    const unsigned s1 = pk2(blo(exv[it][1]) * blo(edv[it][1]), bhi(exv[it][1]) * bhi(edv[it][1]));
    skip[it] = f32x4{blo(s0), bhi(s0), blo(s1), bhi(s1)};
  }
  sfor<0, 10>([&](auto N) { gc(IC<4>{}, N); });
  asm volatile("" : "+v"(skip[0]), "+v"(skip[1]), "+v"(skip[2]), "+v"(skip[3]), "+v"(skip[4]), "+v"(eoff[0]), "+v"(eoff[1]), "+v"(eoff[2]),
               "+v"(eoff[3]), "+v"(eoff[4]), "+v"(elds[0]), "+v"(elds[1]), "+v"(elds[2]), "+v"(elds[3]), "+v"(elds[4]));
  SB();
  STAMP(20);

  const char* wsrc = sptr(ws, WS_WO + (jt * 20 + w) * 5120 + 2048);
  auto gw_ = [&](auto M, auto N) {
    constexpr int m = M, n = N;
    if constexpr (m < 2) glds<1, m * 5120 + n * 1024, n * 1024 - 2048>(wsrc + m * 4 * 5120, lin, dB);
    else glds<1, (m - 2) * 5120 + n * 1024, n * 1024 - 2048>(wsrc + m * 4 * 5120, lin, dA);
  };
  const int wlp0 = (8 * g + q4 + 4 * (g & 1)) * 160 + 8 * pp, wlp1 = (8 * g + q4 + 4 * (1 - (g & 1))) * 160 + 8 * pp;
  LAS char* wrA[2] = {lds + A0 + w * 2 * UNIT + wlp0, lds + A0 + w * 2 * UNIT + wlp1};
  LAS char* wrB[2] = {lds + R1 + w * UNIT + wlp0, lds + R1 + w * UNIT + wlp1};
  asm volatile("" : "+v"(wrA[0]), "+v"(wrA[1]), "+v"(wrB[0]), "+v"(wrB[1]));
  bf16x8 bf[2][5];
  auto rd = [&](auto KP, auto I) {
    constexpr int kp = KP, i = I;
    if constexpr (kp < 4) {
    } else if constexpr (kp < 10) {
      if constexpr (i < 5) bf[kp & 1][i] = lds_frag(kp < 6 ? crB[kp & 1] + i * 2048 : crA[kp & 1] + (kp < 8 ? 0 : UNIT) + i * 2048);
    } else {
      constexpr int m = kp - 10;
      bh[kp & 1][i >> 1][i & 1] = __builtin_amdgcn_ds_read_tr16_b64_v4i16(
          (LAS bf16x4*)(m < 2 ? wrB[i & 1] + m * 5120 + 32 * (i >> 1) : wrA[i & 1] + (m - 2) * 5120 + 32 * (i >> 1)));
    }
  };
  auto bfrag = [&](auto KP, auto NT) {
    constexpr int kp = KP, nt = NT;
    if constexpr (kp < 4) return bu[kp][nt];
    else if constexpr (kp < 10) return bf[kp & 1][nt];
    else return (bf16x8)__builtin_shufflevector(bh[kp & 1][nt][0], bh[kp & 1][nt][1], 0, 1, 2, 3, 4, 5, 6, 7);
  };
  f32x4 acc[4][5];
#pragma unroll
  for (int mt = 0; mt < 4; ++mt)
#pragma unroll
    for (int nt = 0; nt < 5; ++nt) acc[mt][nt] = f32x4{0, 0, 0, 0};
  unsigned of = 0;
  const unsigned ooff = WS_OFLAG + ((T >> 1) * 16 + r) * 4;
#define AWAITN(N, S) do { asm volatile("s_waitcnt vmcnt(%4) lgkmcnt(0)" : "+v"(a[S][0]), "+v"(a[S][1]), "+v"(a[S][2]), "+v"(a[S][3]) : "n"(N) : "memory"); SB(); } while (0)
#define AWAIT(N, S) do { asm volatile("s_waitcnt vmcnt(" #N ") lgkmcnt(0)" : "+v"(a[S][0]), "+v"(a[S][1]), "+v"(a[S][2]), "+v"(a[S][3])::"memory"); SB(); } while (0)
  sfor<0, 15>([&](auto KP) {
    constexpr int kp = KP, sl = kp & 3;
    if constexpr (kp <= 2) AWAITN(33 + TAN, sl);
    if constexpr (kp == 3 || kp == 4) AWAIT(8, sl);
    if constexpr (kp == 3) asm volatile("" : "+v"(egv[0]), "+v"(egv[1]), "+v"(egv[2]), "+v"(egv[3]), "+v"(egv[4])::"memory");
    if constexpr (kp == 5) AWAIT(9, sl);
    if constexpr (kp == 6) AWAIT(19, sl);
    if constexpr (kp == 13) AWAIT(4, sl);
    if constexpr (kp == 7 || kp == 9) AWAIT(18, sl);
    if constexpr (kp == 7) asm volatile("" : "+v"(of)::"memory");
    if constexpr (kp == 8) AWAIT(28, sl);
    if constexpr (kp == 12) AWAIT(13, sl);
    if constexpr (kp == 10) AWAIT(23, sl);
    if constexpr (kp == 11) AWAIT(13, sl);
    if constexpr (kp == 14) AWAIT(0, sl);
    if constexpr (kp == 7) {
      if (!__all(of == tag)) {
        unsigned spins = 0;
        for (;;) {
          of = ld4_wait(ws, ooff);
          if (__all(of == tag)) break;
          if (spin_fail(spins, tmo, tag)) break;
        }
      }
      SB();
    }
    if constexpr (kp == 5) STAMP(21);
    if constexpr (kp == 10) STAMP(22);
    if constexpr (kp + 3 < 15) ga(IC<kp + 3>{});
    sfor<0, 20>([&](auto I) {
      constexpr int i = I, nt = i >> 2, mt = i & 3;
      acc[mt][nt] = mfma(bfrag(KP, IC<nt>{}), a[sl][mt], acc[mt][nt]);
      if constexpr (kp + 1 < 15 && i < 10) rd(IC<kp + 1>{}, I);
      if constexpr ((kp == 5 || kp == 7) && (i & 1) == 1) gw_(IC<kp - 5 + (i >= 10)>{}, IC<(i % 10) / 2>{});
      if constexpr (kp == 9 && (i & 3) == 1) gw_(IC<4>{}, IC<(i >> 2)>{});
      if constexpr (kp == 4 && i == 19) of = ld4_sc1(ws, ooff);
      SB();
    });
  });
  STAMP(23);
  LSYNC();
#pragma unroll
  for (int mt = 0; mt < 4; ++mt)
#pragma unroll
    for (int nt = 0; nt < 5; ++nt)
      *(LAS f32x4*)(lds + ((w * 64 + 16 * mt + r) * 84 + 16 * nt + 4 * g) * 4) = acc[mt][nt];
  LSYNC();
  STAMP(24);
#pragma unroll
  for (int it = 0; it < 5; ++it) {
    const f32x4 s0 = *(LAS f32x4*)(lds + elds[it]), s1 = *(LAS f32x4*)(lds + elds[it] + 64 * 84 * 4);
    const f32x4 s2 = *(LAS f32x4*)(lds + elds[it] + 2 * 64 * 84 * 4), s3 = *(LAS f32x4*)(lds + elds[it] + 3 * 64 * 84 * 4);
    *(f32x4*)((char*)out + eoff[it]) = ((s0 + s1) + (s2 + s3)) + skip[it] * __uint_as_float(egv[it]);
  }
  asm volatile(".Lkf_end:");
  STAMP(25);
  CSTAMP(31);
  STAMP_OUT(ws, 0);
}

extern "C" void kernel_launch(void* const* d_in, const int* in_sizes, int n_in, void* d_out, int out_size, void* d_ws,
                              size_t ws_size, hipStream_t stream) {
  const u16* x = (const u16*)d_in[0];
  const u16* lre = (const u16*)d_in[1];
  const u16* lim = (const u16*)d_in[2];
  const u16* bre = (const u16*)d_in[3];
  const u16* bim = (const u16*)d_in[4];
  const u16* cre = (const u16*)d_in[5];
  const u16* cim = (const u16*)d_in[6];
  const u16* ldt = (const u16*)d_in[7];
  const u16* dskip = (const u16*)d_in[8];
  const u16* wqkv = (const u16*)d_in[10];
  const u16* wo = (const u16*)d_in[11];
  const u16* gw = (const u16*)d_in[12];
  constexpr int LDS = 163840;
  (void)hipFuncSetAttribute((const void*)k_fused, hipFuncAttributeMaxDynamicSharedMemorySize, LDS);
  k_fused<<<256, 256, LDS, stream>>>(x, lre, lim, ldt, bre, bim, wqkv, gw, cre, cim, wo, dskip, (char*)d_ws, (float*)d_out);
}
```

```cpp
#include <hip/hip_runtime.h>
#include <stdint.h>

typedef unsigned short u16;
typedef unsigned long long u64;
typedef short bf16x8 __attribute__((ext_vector_type(8)));
typedef short bf16x4 __attribute__((ext_vector_type(4)));
typedef float f32x4 __attribute__((ext_vector_type(4)));
typedef float f32x2 __attribute__((ext_vector_type(2)));
typedef unsigned u32x4 __attribute__((ext_vector_type(4)));
typedef unsigned u32x2 __attribute__((ext_vector_type(2)));
typedef __bf16 bf16v2 __attribute__((ext_vector_type(2)));
#define LAS __attribute__((address_space(3)))
#define GAS __attribute__((address_space(1)))
#define DI __device__ __forceinline__

constexpr unsigned WS_CFLAG = 0;
constexpr unsigned WS_HFLAG = 1024;
constexpr unsigned WS_OFLAG = 3072;
constexpr unsigned WS_TMO   = 2048;
constexpr unsigned WS_EPOCH = 2304;
constexpr unsigned WS_AGG   = 4096;
constexpr unsigned WS_PART  = WS_AGG + 16 * 16 * 80 * 8;
constexpr unsigned WS_FIN   = WS_PART + 16 * 16 * 848 * 8;
constexpr unsigned WS_ZEND  = WS_FIN + 16 * 848 * 8;
constexpr unsigned WS_WKV   = (WS_ZEND + 4095) / 4096 * 4096;
constexpr unsigned WS_WQ    = WS_WKV + 16 * 102400;
constexpr unsigned WS_WO    = WS_WQ + 16 * 61440;
constexpr unsigned WS_A     = WS_WO + 8 * 20 * 5120;
constexpr unsigned WS_G     = WS_A + 128 * 240 * 256;
constexpr unsigned WS_STAMP = (WS_G + 8192 + 4095) / 4096 * 4096;
constexpr unsigned SPIN_CAP = 1u << 15;

DI unsigned pk2(float a, float b) { f32x2 p = {a, b}; return __builtin_bit_cast(unsigned, __builtin_convertvector(p, bf16v2)); }
DI float blo(unsigned u) { return __uint_as_float(u << 16); }
DI float bhi(unsigned u) { return __uint_as_float(u & 0xffff0000u); }
DI float rbf(float a) { return blo(pk2(a, a)); }
DI float bf2f(unsigned v) { return __uint_as_float(v << 16); }
DI bf16x8 frag4(unsigned a, unsigned b, unsigned c, unsigned d) { u32x4 v = {a, b, c, d}; return __builtin_bit_cast(bf16x8, v); }
DI f32x4 mfma(bf16x8 a, bf16x8 b, f32x4 c) { return __builtin_amdgcn_mfma_f32_16x16x32_bf16(a, b, c, 0, 0, 0); }
DI bf16x8 lds_frag(LAS char* p) { return *(LAS bf16x8*)p; }
DI bf16x8 lds_frag_tr(LAS char* p0, LAS char* p1) {
  bf16x4 a = __builtin_amdgcn_ds_read_tr16_b64_v4i16((LAS bf16x4*)p0);
  bf16x4 b = __builtin_amdgcn_ds_read_tr16_b64_v4i16((LAS bf16x4*)p1);
  return __builtin_shufflevector(a, b, 0, 1, 2, 3, 4, 5, 6, 7);
}
template <int SC1, unsigned IMM, int GOFF = 0>
DI void glds(const void* sbase, unsigned voff, unsigned ldsbase) {
  if constexpr (SC1)
    asm volatile("s_add_u32 m0, %2, %3\n\ts_nop 0\n\tglobal_load_lds_dwordx4 %0, %1 offset:%4 sc1" ::"v"(voff), "s"(sbase), "s"(ldsbase), "i"(IMM - GOFF), "i"(GOFF) : "memory", "scc");
  else
    asm volatile("s_add_u32 m0, %2, %3\n\ts_nop 0\n\tglobal_load_lds_dwordx4 %0, %1 offset:%4" ::"v"(voff), "s"(sbase), "s"(ldsbase), "i"(IMM - GOFF), "i"(GOFF) : "memory", "scc");
}
template <int V> struct IC { static constexpr int value = V; constexpr operator int() const { return V; } };
template <int I, int N, class F>
DI void sfor(F&& f) { if constexpr (I < N) { f(IC<I>{}); sfor<I + 1, N>(f); } }
DI void touch(const void* p, unsigned ldsdummy) {
  asm volatile("s_mov_b32 m0, %1\n\ts_nop 0\n\tglobal_load_lds_dword %0, off" ::"v"(p), "s"(ldsdummy) : "memory");
}
DI void touch_s(const void* sbase, unsigned voff, unsigned ldsdummy) {
  asm volatile("s_mov_b32 m0, %2\n\ts_nop 0\n\tglobal_load_lds_dword %0, %1" ::"v"(voff), "s"(sbase), "s"(ldsdummy) : "memory");
}
DI void touch_s_sc1(const void* sbase, unsigned voff, unsigned ldsdummy) {
  asm volatile("s_mov_b32 m0, %2\n\ts_nop 0\n\tglobal_load_lds_dword %0, %1 sc1" ::"v"(voff), "s"(sbase), "s"(ldsdummy) : "memory");
}
DI void store16_sc1(void* sbase, unsigned voff, u32x4 v) {
  asm volatile("global_store_dwordx4 %0, %1, %2 sc1\n\ts_nop 1" ::"v"(voff), "v"(v), "s"(sbase) : "memory");
}
#define SYNC(N) do { asm volatile("s_waitcnt vmcnt(%0) lgkmcnt(0)\n\ts_barrier" ::"n"(N) : "memory"); __builtin_amdgcn_sched_barrier(0); } while (0)
#define LSYNC() do { asm volatile("s_waitcnt lgkmcnt(0)\n\ts_barrier" ::: "memory"); __builtin_amdgcn_sched_barrier(0); } while (0)
DI u64 gload(const GAS u64* p) { return __hip_atomic_load(p, __ATOMIC_RELAXED, __HIP_MEMORY_SCOPE_AGENT); }
template <int OFF>
DI void gstore(const void* sbase, unsigned voff, float v, unsigned tag) {
  const u32x2 d = {__float_as_uint(v), tag};
  asm volatile("global_store_dwordx2 %0, %1, %2 offset:%3 sc1" ::"v"(voff), "v"(d), "s"(sbase), "i"(OFF) : "memory");
}
DI u32x2 ld8(const void* sbase, unsigned voff) { u32x2 t; asm volatile("global_load_dwordx2 %0, %1, %2" : "=v"(t) : "v"(voff), "s"(sbase) : "memory"); return t; }
DI u32x4 ld16(const void* sbase, unsigned voff) { u32x4 t; asm volatile("global_load_dwordx4 %0, %1, %2" : "=v"(t) : "v"(voff), "s"(sbase) : "memory"); return t; }
DI unsigned ld4_sc1(const void* sbase, unsigned voff) { unsigned t; asm volatile("global_load_dword %0, %1, %2 sc1" : "=v"(t) : "v"(voff), "s"(sbase) : "memory"); return t; }
DI bf16x8 ld16_sc1(const void* sbase, unsigned voff) { bf16x8 t; asm volatile("global_load_dwordx4 %0, %1, %2 sc1" : "=v"(t) : "v"(voff), "s"(sbase) : "memory"); return t; }
DI u32x4 ld16u_sc1(const void* sbase, unsigned voff) { u32x4 t; asm volatile("global_load_dwordx4 %0, %1, %2 sc1" : "=v"(t) : "v"(voff), "s"(sbase) : "memory"); return t; }
DI u32x2 ld8_sc1(const void* sbase, unsigned voff) { u32x2 t; asm volatile("global_load_dwordx2 %0, %1, %2 sc1" : "=v"(t) : "v"(voff), "s"(sbase) : "memory"); return t; }
DI u32x2 ld8_wait(const void* sbase, unsigned voff) { u32x2 t; asm volatile("global_load_dwordx2 %0, %1, %2 sc1\n\ts_waitcnt vmcnt(0)" : "=&v"(t) : "v"(voff), "s"(sbase) : "memory"); return t; }
DI u32x4 ld16_wait(const void* sbase, unsigned voff) { u32x4 t; asm volatile("global_load_dwordx4 %0, %1, %2 sc1\n\ts_waitcnt vmcnt(0)" : "=&v"(t) : "v"(voff), "s"(sbase) : "memory"); return t; }
DI unsigned ld4_wait(const void* sbase, unsigned voff) { unsigned t; asm volatile("global_load_dword %0, %1, %2 sc1\n\ts_waitcnt vmcnt(0)" : "=&v"(t) : "v"(voff), "s"(sbase) : "memory"); return t; }
DI const char* sptr(const void* base, unsigned off) { return (const char*)base + (unsigned)__builtin_amdgcn_readfirstlane(off); }
DI void store8_sc1(void* sbase, unsigned voff, u32x2 v) {
  asm volatile("global_store_dwordx2 %0, %1, %2 sc1" ::"v"(voff), "v"(v), "s"(sbase) : "memory");
}
DI void store4_sc1(void* sbase, unsigned voff, unsigned v) {
  asm volatile("global_store_dword %0, %1, %2 sc1" ::"v"(voff), "v"(v), "s"(sbase) : "memory");
}
DI bool spin_fail(unsigned& spins, GAS unsigned* tmo, unsigned tag) {
  __builtin_amdgcn_s_sleep(1);
  if (++spins > SPIN_CAP) { __hip_atomic_store(tmo, tag, __ATOMIC_RELAXED, __HIP_MEMORY_SCOPE_AGENT); return true; }
  if ((spins & 63u) == 0u)
    return (unsigned)__builtin_amdgcn_readfirstlane(__hip_atomic_load(tmo, __ATOMIC_RELAXED, __HIP_MEMORY_SCOPE_AGENT)) == tag;
  return false;
}
DI float phi1(float t) {
  return (__builtin_amdgcn_exp2f(fminf(t, 0.f) * 1.4426950408889634f) + fmaxf(t, 0.f)) + 1e-6f;
}
DI f32x2 cfma(f32x2 a, f32x2 h, f32x2 u) {
  f32x2 t, o;
  asm("v_pk_fma_f32 %0, %1, %2, %3 op_sel_hi:[0,1,1]" : "=v"(t) : "v"(a), "v"(h), "v"(u));
  asm("v_pk_fma_f32 %0, %1, %2, %3 op_sel:[1,1,0] op_sel_hi:[1,0,1] neg_lo:[1,0,0]" : "=v"(o) : "v"(a), "v"(h), "v"(t));
  return o;
}
DI f32x2 cfma_1(f32x2 a, f32x2 h, f32x2 u) {
  f32x2 t;
  asm("v_pk_fma_f32 %0, %1, %2, %3 op_sel_hi:[0,1,1]" : "=v"(t) : "v"(a), "v"(h), "v"(u));
  return t;
}
DI f32x2 cfma_2(f32x2 a, f32x2 h, f32x2 t) {
  f32x2 o;
  asm("v_pk_fma_f32 %0, %1, %2, %3 op_sel:[1,1,0] op_sel_hi:[1,0,1] neg_lo:[1,0,0]" : "=v"(o) : "v"(a), "v"(h), "v"(t));
  return o;
}
DI f32x2 cco_1(f32x2 a, f32x2 h) {
  f32x2 t;
  asm("v_pk_mul_f32 %0, %1, %2 op_sel_hi:[0,1] neg_hi:[1,0]" : "=v"(t) : "v"(a), "v"(h));
  return t;
}
DI f32x2 cco_2(f32x2 a, f32x2 h, f32x2 t) {
  f32x2 o;
  asm("v_pk_fma_f32 %0, %1, %2, %3 op_sel:[1,1,0] op_sel_hi:[1,0,1] neg_lo:[1,0,0] neg_hi:[1,0,0]" : "=v"(o) : "v"(a), "v"(h), "v"(t));
  return o;
}
DI f32x2 cmul(f32x2 a, f32x2 h) {
  f32x2 t, o;
  asm("v_pk_mul_f32 %0, %1, %2 op_sel:[1,1] op_sel_hi:[1,0]" : "=v"(t) : "v"(a), "v"(h));
  asm("v_pk_fma_f32 %0, %1, %2, %3 op_sel_hi:[0,1,1] neg_lo:[0,0,1]" : "=v"(o) : "v"(a), "v"(h), "v"(t));
  return o;
}
DI f32x2 cmul_conj_out(f32x2 a, f32x2 h) {
  f32x2 t, o;
  asm("v_pk_mul_f32 %0, %1, %2 op_sel_hi:[0,1] neg_hi:[1,0]" : "=v"(t) : "v"(a), "v"(h));
  asm("v_pk_fma_f32 %0, %1, %2, %3 op_sel:[1,1,0] op_sel_hi:[1,0,1] neg_lo:[1,0,0] neg_hi:[1,0,0]" : "=v"(o) : "v"(a), "v"(h), "v"(t));
  return o;
}
DI f32x2 csq(f32x2 a) { return cmul(a, a); }
DI unsigned phi2(float a, float b) {
  const f32x2 t = {a, b};
  const f32x2 l = t * 1.4426950408889634f;
  f32x2 e, m;
  e.x = __builtin_amdgcn_fmed3f(__builtin_amdgcn_exp2f(l.x), 0.f, 1.f);
  e.y = __builtin_amdgcn_fmed3f(__builtin_amdgcn_exp2f(l.y), 0.f, 1.f);
  m.x = __builtin_amdgcn_fmed3f(a, 0.f, __builtin_inff()); m.y = __builtin_amdgcn_fmed3f(b, 0.f, __builtin_inff());
  const f32x2 p = (e + m) + 1e-6f;
  return pk2(p.x, p.y);
}
#define TOUCH1 1
#define TOUCH2 1
#define MAP48 1
#define TOUCHD 0
#define TOUCHA 0
constexpr int TAN = TOUCHA ? 1 : 0;
constexpr int TDN = TOUCHD ? 1 : 0, T1N = 0, T2N = TOUCH2 ? 2 : 0, TCN = TOUCH1 ? 1 : 0;
#define SB() __builtin_amdgcn_sched_barrier(0)
#define VWAIT(N) do { asm volatile("s_waitcnt vmcnt(%0)" ::"n"(N) : "memory"); SB(); } while (0)

#define STAMP_DECL
#define STAMP(i)
#define CSTAMP(i)
#define STAMP_OUT(ws, k)
__global__ __launch_bounds__(256, 1) void k_fused(const u16* __restrict__ x, const u16* __restrict__ lre, const u16* __restrict__ lim,
                                                  const u16* __restrict__ ldt, const u16* __restrict__ bre, const u16* __restrict__ bim,
                                                  const u16* __restrict__ wqkv, const u16* __restrict__ gw, const u16* __restrict__ cre,
                                                  const u16* __restrict__ cim, const u16* __restrict__ wo, const u16* __restrict__ dskip,
                                                  char* __restrict__ ws, float* __restrict__ out) {
  extern __shared__ __attribute__((aligned(16))) char lds_[];
  LAS char* lds = (LAS char*)lds_;
  const unsigned ldsb = (unsigned)(size_t)lds;
  asm volatile("" ::"s"(x), "s"(lre), "s"(lim), "s"(ldt), "s"(bre), "s"(bim), "s"(wqkv), "s"(gw), "s"(ws));
  const int tid = threadIdx.x, lane = tid & 63;
  const int w = __builtin_amdgcn_readfirstlane(tid >> 6);
  const int xcd = blockIdx.x & 7, idx = blockIdx.x >> 3;
  const int rb = 4 * (xcd & 3) + (idx & 3), cg = 8 * (xcd >> 2) + (idx >> 2);
  const int r = lane & 15, g = lane >> 4;
  GAS unsigned* tmo = (GAS unsigned*)(ws + WS_TMO);
  STAMP_DECL
  STAMP(0);
  CSTAMP(30);

  constexpr unsigned SLOT = 20480;
  constexpr unsigned MISC = 0, A0 = 10240, A1 = A0 + SLOT, A2 = A0 + 2 * SLOT, A3 = A0 + 3 * SLOT, B0 = A0 + 4 * SLOT, B1 = A0 + 5 * SLOT,
                     B2 = A0 + 6 * SLOT;
  constexpr unsigned L_E = MISC;
  constexpr unsigned L_G = MISC + 5120;
  constexpr unsigned L_KVT = MISC + 5632;
  constexpr unsigned L_DUMMY = MISC + 7424;
  constexpr unsigned L_H = MISC + 7680;
  constexpr unsigned L_C = MISC + 8704;
  constexpr unsigned ASLOT[4] = {A0, A1, A2, A3};
  constexpr unsigned BSLOT[3] = {B0, B1, B2};
  constexpr unsigned KVSLOT[5] = {B2, A2, B0, A3, A0};
  constexpr unsigned QSLOT[5] = {B1, B1 + 12288, B1 + 24576, B1 + 36864, B0};

  unsigned xoff[2];
  {
    const int l3 = lane >> 3;
    const unsigned row = 128 * rb + 32 * w + 8 * (l3 >> 2) + (l3 & 3), sw = ((lane & 7) ^ (l3 >> 1)) << 4;
    xoff[0] = row * 1280 + sw;
    xoff[1] = (row + 16) * 1280 + (sw ^ 64);
  }
  const char* const xb1 = (const char*)x + 4 * 1280;
  unsigned boff[5];
  const u16* bbase[5];
#pragma unroll
  for (int i = 0; i < 5; ++i) {
    const int n = 5 * w + i;
    const int tt = n >> 2, rr = 4 * (n & 3) + g;
    const int isim = tt < 4 ? (tt & 1) : (n >= 18);
    const int cbase = tt < 4 ? 16 * (tt >> 1) : 32, cmask = tt < 4 ? 15 : 7;
    const int chl = cbase + (rr & cmask);
    bbase[i] = isim ? bim : bre;
    boff[i] = (40 * cg + chl) * 1280 + ((r ^ rr) << 4);
  }
  const unsigned xdst = ldsb + 32 * w * 128, bdst = ldsb + 5 * w * 1024, qdst = ldsb + 3 * w * 1024;
  auto gx = [&](auto S, auto N) { constexpr int s = S, n = N; glds<0, ASLOT[s & 3] + n * 1024, s * 128>(n < 2 ? (const char*)x : xb1, xoff[n & 1], xdst); };
  auto gb = [&](auto BE, auto I) { constexpr int be = BE, i = I; glds<0, BSLOT[be % 3] + i * 1024, be * 256>(bbase[i], boff[i], bdst); };
  const unsigned lin = lane * 16;
  const char* kvsrc = ws + WS_WKV + cg * 102400 + w * 5120 + 2048;
  const char* qsrc = ws + WS_WQ + cg * 61440 + w * 3072 + 1024;
  auto gkv = [&](auto J, auto I) { constexpr int j = J, i = I; glds<1, KVSLOT[j] + i * 1024, i * 1024 - 2048>(kvsrc + j * 20480, lin, bdst); };
  auto gq = [&](auto M, auto I) { constexpr int m = M, i = I; glds<1, QSLOT[m] + i * 1024, i * 1024 - 1024>(qsrc + m * 12288, lin, qdst); };

  unsigned p_lre, p_lim, p_ldt, p_ep, p_gw;
  {
    asm volatile("global_load_dword %0, %1, %2 sc1" : "=v"(p_ep) : "v"(WS_EPOCH), "s"(ws) : "memory");
    const int l39 = lane < 40 ? lane : 39, ch = 40 * cg + l39;
    asm volatile("global_load_ushort %0, %1, %2" : "=v"(p_lre) : "v"(2 * ch), "s"(lre) : "memory");
    asm volatile("global_load_ushort %0, %1, %2" : "=v"(p_lim) : "v"(2 * ch), "s"(lim) : "memory");
    asm volatile("global_load_ushort %0, %1, %2" : "=v"(p_ldt) : "v"(2 * ch), "s"(ldt) : "memory");
    asm volatile("global_load_ushort %0, %1, %2" : "=v"(p_gw) : "v"(2 * (40 * rb + l39)), "s"(gw) : "memory");
  }
  sfor<0, 4>([&](auto N) { gx(IC<0>{}, N); });
  sfor<0, 5>([&](auto I) { gb(IC<0>{}, I); });
  SB();
  u32x4 cq[3];
  u32x2 ct[3];
  unsigned csrc[3], cdst[3];
  const int cj = tid & 15, crow0 = tid >> 4;
  const bool ctail = (0x4300u >> cj) & 1u, cgate = cj == 15;
  const int crow[3] = {crow0, crow0 + 16, 32 + (crow0 & 7)};
  sfor<0, 4>([&](auto N) { gx(IC<1>{}, N); });
  {
    const unsigned i = idx * 4 + w;
    u64 pc;
    unsigned clen;
    asm volatile("s_getpc_b64 %0\n.Lkf_pc:\n\ts_mov_b32 %1, .Lkf_end-.Lkf_pc" : "=s"(pc), "=s"(clen));
    const char* cp = (const char*)pc + (min(i * 384 + (lane & 3) * 128, clen - 4u) & ~3u);
    const char* tp = (lane & 1) ? (const char*)out + min(i, 79u) * 65536 : ws + WS_A + min(i, 119u) * 65536;
    touch(lane >= 4 && lane < 7 ? cp : tp, ldsb + L_DUMMY);
  }
  {
    const unsigned sec = (0x0009aa55u >> (2 * cj)) & 3u;
    const unsigned off4 = (unsigned)(0x0475204475207520ull >> (4 * cj)) & 15u;
    const unsigned colb = 2 * (sec * 640 + 40 * cg + 4 * off4) + 40 * rb * 3840;
    const unsigned rbytes = cj >= 10 ? 96 : 160;
    const unsigned dbase = cj >= 10 ? WS_WQ + cg * 61440 + (cj - 10) * 16 : WS_WKV + cg * 102400 + cj * 16;
#pragma unroll
    for (int it = 0; it < 3; ++it) {
      const unsigned k = 40 * rb + crow[it];
      csrc[it] = colb + crow[it] * 3840;
      cdst[it] = dbase + (k ^ (((k >> 3) & 1) << 2)) * rbytes;
    }
    asm volatile("" : "+v"(csrc[0]), "+v"(csrc[1]), "+v"(csrc[2]), "+v"(cdst[0]), "+v"(cdst[1]), "+v"(cdst[2]));
  }
  auto cload = [&](auto N) {
    constexpr int n = N, it = n >> 1;
    if constexpr (n & 1) ct[it] = ld8(wqkv, ctail ? csrc[it] + 40 : 0u);
    else cq[it] = ld16(wqkv, csrc[it]);
  };
  auto cstore = [&](auto IT) {
    constexpr int it = IT;
    u32x4 v = cq[it];
    const unsigned gwv = __shfl(p_gw, crow[it]);
    v[0] = cgate ? gwv : v[0]; v[1] = cgate ? 0u : v[1];
    v[2] = ctail ? ct[it][0] : (cgate ? 0u : v[2]); v[3] = ctail ? ct[it][1] : (cgate ? 0u : v[3]);
    store16_sc1(ws, cdst[it], v);
  };
  STAMP(1);

  f32x2 c_a, c_a8;
  float c_lmag, c_rev;
  f32x2 pa[3], pc[3], pw[3];
  f32x2 pj[4];
  asm volatile("s_waitcnt vmcnt(%5)" : "+v"(p_lre), "+v"(p_lim), "+v"(p_ldt), "+v"(p_ep), "+v"(p_gw) : "n"(13 + TCN) : "memory");
  const unsigned tag = __builtin_amdgcn_readfirstlane(p_ep) + 1u;
  {
    const float lr = bf2f(p_lre), li = bf2f(p_lim);
    const float dt = rbf(__expf(bf2f(p_ldt)));
    c_lmag = dt * lr * 1.4426950408889634f; c_rev = dt * li * 0.15915494309189535f;
    const float er = __builtin_amdgcn_exp2f(c_lmag), rev = __builtin_amdgcn_fractf(c_rev);
    c_a = f32x2{er * __builtin_amdgcn_cosf(rev), er * __builtin_amdgcn_sinf(rev)};
    const float nr = c_a.x - 1.f, ni = c_a.y, iden = 1.f / (lr * lr + li * li);
    const float cr = (nr * lr + ni * li) * iden, ci = (ni * lr - nr * li) * iden;
    c_a8 = csq(csq(csq(c_a)));
#pragma unroll
    for (int p = 0; p < 3; ++p) {
      const int src = p < 2 ? 16 * p + r : 32 + (r & 7);
      pa[p] = f32x2{__shfl(c_a.x, src), __shfl(c_a.y, src)};
      pc[p] = f32x2{2.f * __shfl(cr, src), 2.f * __shfl(ci, src)};
    }
    asm volatile("" : "+v"(pa[0]), "+v"(pa[1]), "+v"(pa[2]), "+v"(pc[0]), "+v"(pc[1]), "+v"(pc[2]), "+v"(c_a8),
                 "+v"(c_lmag), "+v"(c_rev));
  }
  STAMP(2);

  bf16x8 xa[20][2];
  f32x4 accS[5][2];
#pragma unroll
  for (int t = 0; t < 5; ++t) { accS[t][0] = f32x4{0, 0, 0, 0}; accS[t][1] = f32x4{0, 0, 0, 0}; }
  bf16x8 bq[2][5];
  LAS char* xrd[2];
  LAS char* brd[4];
#pragma unroll
  for (int kk = 0; kk < 2; ++kk) xrd[kk] = lds + A0 + (32 * w + r) * 128 + (((4 * kk + g) ^ ((r >> 1) & 7)) << 4);
#pragma unroll
  for (int kq = 0; kq < 4; ++kq) brd[kq] = lds + B0 + r * 256 + (((4 * kq + g) ^ r) << 4);
  auto rd1 = [&](auto KS, auto I) {
    constexpr int ks = KS, i = I;
    if constexpr (i < 2) xa[ks][i] = lds_frag(xrd[ks & 1] + (ASLOT[(ks >> 1) & 3] - A0) + i * 2048);
    else bq[ks & 1][i - 2] = lds_frag(brd[ks & 3] + (BSLOT[(ks >> 2) % 3] - B0) + (i - 2) * 4096);
  };
  unsigned cf = 0;
  const GAS unsigned* cfp = (const GAS unsigned*)(ws + WS_CFLAG) + cg * 16 + r;

  auto dma1 = [&](auto S, auto J) {
    constexpr int s = S, j = J;
    if constexpr (s == 0) {
      if constexpr (j < 4) gx(IC<2>{}, J); else if constexpr (j < 9) gb(IC<1>{}, IC<j - 4>{}); else if constexpr (j < 15) cload(IC<j - 9>{});
    } else if constexpr (s == 1) {
      if constexpr (j < 4) gx(IC<3>{}, J);
      else if constexpr (j < 8) gx(IC<4>{}, IC<j - 4>{});
      else if constexpr (j < 13) gb(IC<2>{}, IC<j - 8>{});
    } else if constexpr (s == 2 && j >= 100) {
      cstore(IC<j - 100>{});
    } else if constexpr (s <= 6) {
      if constexpr (j < 4) gx(IC<s + 3>{}, J);
      else if constexpr (s <= 5) {
        constexpr int be = 2 + (s >> 1), i = (s & 1) ? j - 1 : j - 4;
        if constexpr ((s & 1) ? j < 6 : j < 7) gb(IC<be>{}, IC<i>{});
      } else if constexpr (j == 4) {
        cf = ld4_sc1(ws, WS_CFLAG + (cg * 16 + r) * 4);
      }
    } else if constexpr (s == 7) {
      const u32x2 z = {0u, tag - 1u};
      if constexpr (j < 4) store8_sc1(ws, WS_PART + ((cg * 16 + rb) * 848 + min(tid + 256 * j, 847)) * 8, z);
      else if constexpr (j == 4) store8_sc1(ws, WS_AGG + ((cg * 16 + rb) * 80 + min(tid, 79)) * 8, z);
      else if constexpr (j == 5) store8_sc1(ws, WS_FIN + (cg * 848 + min(53 * rb + min(tid, 52), 847)) * 8, z);
    } else if constexpr (s == 8) { if constexpr (j < 5) gkv(IC<0>{}, J); }
    else if constexpr (s == 9) { if constexpr (j < 5) gkv(IC<1>{}, J); }
  };

  SYNC(4 + TCN);
  STAMP(3);
  sfor<0, 7>([&](auto I) { rd1(IC<0>{}, I); });
  SB();
  sfor<0, 20>([&](auto KS) {
    constexpr int ks = KS, s = (ks + 1) >> 1, first = ks & 1;
    if constexpr (first && ks + 1 < 20) {
      if constexpr (s == 1) VWAIT(15 + TCN);
      if constexpr (s == 2) {
        asm volatile("s_waitcnt vmcnt(13)" : "+v"(cq[0]), "+v"(cq[1]), "+v"(cq[2]), "+v"(ct[0]), "+v"(ct[1]), "+v"(ct[2])::"memory");
        SYNC(13);
        sfor<0, 3>([&](auto IT) { dma1(IC<2>{}, IC<100 + IT>{}); });
        STAMP(4);
      }
      if constexpr (s == 3) VWAIT(19);
      if constexpr (s == 4) {
        SYNC(13);
        if (tid == 0) __hip_atomic_store((GAS unsigned*)(ws + WS_CFLAG) + cg * 16 + rb, tag, __ATOMIC_RELAXED, __HIP_MEMORY_SCOPE_AGENT);
      }
      if constexpr (s == 5) { VWAIT(16); STAMP(5); }
      if constexpr (s == 6) SYNC(13);
      if constexpr (s == 7) VWAIT(14);
      if constexpr (s == 8) {
        asm volatile("s_waitcnt vmcnt(6)" : "+v"(cf)::"memory");
        SYNC(6);
        if (!__all(cf == tag)) {
          unsigned spins = 0;
          for (;;) {
            cf = __hip_atomic_load(cfp, __ATOMIC_RELAXED, __HIP_MEMORY_SCOPE_AGENT);
            if (__all(cf == tag)) break;
            if (spin_fail(spins, tmo, tag)) break;
          }
        }
        STAMP(6);
      }
    }
    sfor<0, 10>([&](auto I) {
      constexpr int i = I;
      accS[i >> 1][i & 1] = mfma(xa[ks][i & 1], bq[ks & 1][i >> 1], accS[i >> 1][i & 1]);
      if constexpr (ks + 1 < 20 && i < 7) rd1(IC<ks + 1>{}, I);
      if constexpr (ks == 0) { if constexpr (i < 9) dma1(IC<0>{}, I); else sfor<9, 15>([&](auto J) { dma1(IC<0>{}, J); }); }
      else if constexpr (ks < 19) {
        constexpr int gi = first ? i : 10 + i;
        if constexpr (s == 1) { if constexpr (gi < 13) dma1(IC<1>{}, IC<gi>{}); }
        else if constexpr (s == 8 || s == 9) { if constexpr (gi % 4 == 1) dma1(IC<s>{}, IC<gi / 4>{}); }
        else if constexpr (gi % 3 == 1) dma1(IC<s>{}, IC<gi / 3>{});
      }
      SB();
    });
  });
  STAMP(7);

  f32x2 ub[3][8];
  {
#pragma unroll
    for (int t = 0; t < 8; ++t) {
      ub[0][t] = f32x2{accS[0][t >> 2][t & 3], accS[1][t >> 2][t & 3]};
      ub[1][t] = f32x2{accS[2][t >> 2][t & 3], accS[3][t >> 2][t & 3]};
      const float v = accS[4][t >> 2][t & 3];
      const float o = __builtin_amdgcn_update_dpp(0.f, v, 0x128, 0xf, 0xf, false);
      ub[2][t] = r < 8 ? f32x2{v, o} : f32x2{o, v};
    }
    f32x2 e[3] = {ub[0][0], ub[1][0], ub[2][0]};
    SB();
#pragma unroll
    for (int t = 1; t < 8; ++t) {
      f32x2 t1[3];
#pragma unroll
      for (int p = 0; p < 3; ++p) t1[p] = cfma_1(pa[p], e[p], ub[p][t]);
      SB();
#pragma unroll
      for (int p = 0; p < 3; ++p) e[p] = cfma_2(pa[p], e[p], t1[p]);
      SB();
    }
    LAS char* ep = lds + L_E + ((4 * w + g) * 40 + r) * 8;
    *(LAS f32x2*)ep = e[0];
    *(LAS f32x2*)(ep + 128) = e[1];
    if (r < 8) *(LAS f32x2*)(ep + 256) = e[2];
  }
  STAMP(8);

  f32x4 accK[5][2];
#pragma unroll
  for (int t = 0; t < 5; ++t) { accK[t][0] = f32x4{0, 0, 0, 0}; accK[t][1] = f32x4{0, 0, 0, 0}; }
  const int q4 = r >> 2, pp = r & 3;
  LAS char* kv0 = lds + (8 * g + q4) * 160 + 8 * pp + 640 * (g & 1);
  LAS char* kv1 = lds + (8 * g + q4) * 160 + 8 * pp + 640 * (1 - (g & 1));
  bf16x4 bh[2][5][2];
  LAS char* kb[2][2];
  auto base2 = [&](auto J) {
    constexpr int j = J;
    kb[j & 1][0] = kv0 + KVSLOT[j]; kb[j & 1][1] = kv1 + KVSLOT[j];
    asm volatile("" : "+v"(kb[j & 1][0]), "+v"(kb[j & 1][1]));
  };
  auto rd2 = [&](auto KS, auto I) {
    constexpr int ks = KS, i = I;
    constexpr unsigned o = (ks & 3) * 5120 + 32 * (i >> 1);
    bh[ks & 1][i >> 1][i & 1] = __builtin_amdgcn_ds_read_tr16_b64_v4i16((LAS bf16x4*)(kb[(ks >> 2) & 1][i & 1] + o));
  };
  auto cat = [](bf16x4 a, bf16x4 b) { return __builtin_shufflevector(a, b, 0, 1, 2, 3, 4, 5, 6, 7); };
  const unsigned t2row = (80 * (idx >> 2) + 20 * (idx & 3) + 10 * (w >> 1)) * 1280;
  const unsigned t2off0 = t2row + (min(lane, 39) >> 2) * 1280 + (lane & 3) * 128;
  const unsigned t2l = min(lane, 59), t2q = (t2l * 10923) >> 16;
  const unsigned t2off1 = t2row + t2q * 1280 + (4 + t2l - 6 * t2q) * 128;
  const char* t2p = (const char*)((w & 1) ? cim : cre);
  u32x4 cw;
  unsigned cwsrc, cwdst;
  {
    const unsigned sc = 200 * (16 * rb + cg) + min(tid, 199);
    cwsrc = sc * 16;
    const unsigned k = (sc * 52429u) >> 22, cc = sc - 80 * k, jw = (cc * 6554) >> 16, ch = cc - 10 * jw;
    const unsigned cgq = (k * 1639) >> 16, rem = k - 40 * cgq, hh = rem >= 20, e = rem - 20 * hh;
    const unsigned j = 4 * hh + (e & 3), qq = 5 * cgq + (e >> 2), oc = qq & 3;
    cwdst = WS_WO + ((jw * 20 + (qq >> 2)) * 32 + 8 * oc + (j ^ ((oc & 1) << 2))) * 160 + ch * 16;
  }
  auto dma2 = [&](auto J, auto N) {
    constexpr int j = J, n = N;
    if constexpr (j == 0) { if constexpr (n < 10) gkv(IC<2 + n / 5>{}, IC<n % 5>{}); else if constexpr (n == 10) cw = ld16(wo, cwsrc); }
    else if constexpr (j == 1) { if constexpr (n < 5) gkv(IC<4>{}, N); }
    else if constexpr (j == 2) { if constexpr (n < 6) gq(IC<n / 3>{}, IC<n % 3>{}); }
    else if constexpr (j == 3) { if constexpr (n < 6) gq(IC<2 + n / 3>{}, IC<n % 3>{}); else if constexpr (n == 6) store16_sc1(ws, cwdst, cw); }
    else { if constexpr (n < 3) gq(IC<4>{}, N); else if constexpr (n < 3 + T2N) touch_s(t2p, n == 3 ? t2off0 : t2off1, ldsb + L_DUMMY); }
  };
  base2(IC<0>{});
  SYNC(5);
  STAMP(9);
  sfor<0, 10>([&](auto I) { rd2(IC<0>{}, I); });
  SB();
  sfor<0, 20>([&](auto KS) {
    constexpr int ks = KS, j = (ks + 1) >> 2, ph = (ks + 1) & 3;
    if constexpr (ph == 0 && ks + 1 < 20) {
      base2(IC<j>{});
      if constexpr (j == 1) SYNC(11);
      if constexpr (j == 2) SYNC(11);
      if constexpr (j == 3) { asm volatile("s_waitcnt vmcnt(11)" : "+v"(cw)::"memory"); SYNC(11); }
      if constexpr (j == 4) SYNC(13);
    }
    sfor<0, 10>([&](auto I) {
      constexpr int i = I;
      accK[i >> 1][i & 1] = mfma(xa[ks][i & 1], cat(bh[ks & 1][i >> 1][0], bh[ks & 1][i >> 1][1]), accK[i >> 1][i & 1]);
      if constexpr (ks + 1 < 20) rd2(IC<ks + 1>{}, I);
      if constexpr (j == 0) { if constexpr (i % 3 == 1 || i == 9) dma2(IC<0>{}, IC<4 * ks + (i == 9 ? 3 : i / 3)>{}); }
      else if constexpr (ph < 3 && ks < 19 && (i == 1 || i == 4 || i == 7)) dma2(IC<j>{}, IC<3 * ph + (i - 1) / 3>{});
      SB();
    });
  });
  STAMP(10);

  {
    bf16x8 kf[5];
#pragma unroll
    for (int t = 0; t < 5; ++t) {
      unsigned u[4];
#pragma unroll
      for (int h2 = 0; h2 < 4; ++h2) {
        const float a = accK[t][h2 >> 1][(h2 & 1) * 2], b = accK[t][h2 >> 1][(h2 & 1) * 2 + 1];
        if (t == 2 || t == 3) u[h2] = pk2(a, b);
        else if (t < 2) u[h2] = phi2(a, b);
        else { const unsigned ph = phi2(a, b), raw = pk2(a, b); u[h2] = r < 8 ? ph : raw; }
      }
      kf[t] = frag4(u[0], u[1], u[2], u[3]);
      *(LAS bf16x8*)(lds + A3 + (w * 5 + t) * 1024 + lane * 16) = kf[t];
    }
    SYNC(T2N);
    const bf16x8 ones = frag4(0x3f803f80u, 0x3f803f80u, 0x3f803f80u, 0x3f803f80u);
    const char* part = ws + WS_PART + (cg * 16 + rb) * 848 * 8;
    LAS char* st = lds + A3 + lane * 16;
    auto job = [&](LAS char* pa_, LAS char* pb_, bool onesb) {
      f32x4 d = {0, 0, 0, 0};
#pragma unroll
      for (int sw = 0; sw < 4; ++sw) {
        const bf16x8 a = lds_frag(pa_ + sw * 5120);
        bf16x8 b = lds_frag(pb_ + sw * 5120);
        if (onesb && r < 8) b = ones;
        d = mfma(a, b, d);
      }
      return d;
    };
    if (w < 2) {
      const int h = w;
      const f32x4 d0 = job(st + h * 1024, st + (2 + h) * 1024, false);
      const f32x4 d1 = job(st + 4096, st + (2 + h) * 1024, false);
      const unsigned o0 = (420 * h + 80 * g + r) * 8, o1 = (420 * h + 320 + r) * 8;
      sfor<0, 4>([&](auto I) { constexpr int i = I; gstore<160 * i>(part, o0, d0[i], tag); });
      if (g == h) sfor<0, 4>([&](auto I) { constexpr int i = I; gstore<160 * i>(part, o1, d1[i], tag); });
    } else if (w == 2) {
      const f32x4 d0 = job(st, st + 4096, true);
      const f32x4 d1 = job(st + 1024, st + 4096, true);
      const f32x4 d2 = job(st + 4096, st + 4096, true);
      const bool k0 = (r >> 2) == 2, k1 = (r >> 2) == 3, kq = (r >> 2) == 2 + g;
      const unsigned o0 = (k0 ? 80 * g + 16 + (r & 3) : 400 + 4 * g) * 8, st0 = k0 ? 160 : 8;
      const unsigned o1 = (420 + (k1 ? 80 * g + 16 + (r & 3) : 400 + 4 * g)) * 8, st1 = k1 ? 160 : 8;
      const unsigned o2 = (420 * g + (kq ? 336 + (r & 3) : 416)) * 8, st2 = kq ? 160 : 8;
      if (k0 || r == 0) sfor<0, 4>([&](auto I) { constexpr int i = I; gstore<0>(part, o0 + i * st0, d0[i], tag); });
      if (k1 || r == 0) sfor<0, 4>([&](auto I) { constexpr int i = I; gstore<0>(part, o1 + i * st1, d1[i], tag); });
      if (g < 2 && (kq || r == 0)) sfor<0, 4>([&](auto I) { constexpr int i = I; gstore<0>(part, o2 + i * st2, d2[i], tag); });
    } else {
      if (lane < 40) {
        f32x2 e[16];
#pragma unroll
        for (int s = 0; s < 16; ++s) e[s] = *(LAS f32x2*)(lds + L_E + (s * 40 + lane) * 8);
        f32x2 run = {0.f, 0.f};
#pragma unroll
        for (int s = 0; s < 16; ++s) {
          *(LAS f32x2*)(lds + L_E + (s * 40 + lane) * 8) = run;
          run = cfma(c_a8, run, e[s]);
        }
        const char* ag = ws + WS_AGG + (cg * 16 + rb) * 640;
        gstore<0>(ag, lane * 16, run.x, tag); gstore<8>(ag, lane * 16, run.y, tag);
      }
    }
  }
  STAMP(11);

  const int T = 4 * xcd + (idx & 3), jt = idx >> 2;
  constexpr unsigned UNIT = 10240, R1 = B0 + 25600;
  const unsigned dA = ldsb + A0 + w * 2 * UNIT, dB = ldsb + R1 + w * UNIT;
  const char* cb = sptr((w & 1) ? cim : cre, (w >> 1) * 128);
  unsigned coff[2];
  {
    const int l3 = lane >> 3;
    coff[0] = (80 * jt + l3) * 1280 + (((lane & 7) ^ (l3 >> 1)) << 4);
    coff[1] = coff[0] ^ 64;
  }
  auto gc = [&](auto M, auto N) {
    constexpr int m = M, n = N;
    if constexpr (m == 2) glds<0, n * 1024, m * 256>(cb + n * 10240, coff[n & 1], dB);
    else glds<0, (m == 1 || m == 4 ? UNIT : 0) + n * 1024, m * 256>(cb + n * 10240, coff[n & 1], dA);
  };

  f32x4 accQ[3][2];
#pragma unroll
  for (int t = 0; t < 3; ++t) { accQ[t][0] = f32x4{0, 0, 0, 0}; accQ[t][1] = f32x4{0, 0, 0, 0}; }
  LAS char* q0 = lds + (8 * g + q4) * 96 + 8 * pp + 384 * (g & 1);
  LAS char* q1 = lds + (8 * g + q4) * 96 + 8 * pp + 384 * (1 - (g & 1));
  auto base3 = [&](auto M) {
    constexpr int m = M;
    kb[m & 1][0] = q0 + QSLOT[m]; kb[m & 1][1] = q1 + QSLOT[m];
    asm volatile("" : "+v"(kb[m & 1][0]), "+v"(kb[m & 1][1]));
  };
  auto rd3 = [&](auto KS, auto I) {
    constexpr int ks = KS, i = I;
    constexpr unsigned o = (ks & 3) * 3072 + 32 * (i >> 1);
    bh[ks & 1][i >> 1][i & 1] = __builtin_amdgcn_ds_read_tr16_b64_v4i16((LAS bf16x4*)(kb[(ks >> 2) & 1][i & 1] + o));
  };
  const int i1 = 53 * rb + lane;
  const bool act1 = lane < 53 && i1 < 840;
  const unsigned x1off = (act1 ? i1 : 0) * 8, x2off = min(lane, 39) * 16;
  const char* x1src = sptr(ws, WS_PART + (cg * 16 + 4 * w) * 6784);
  const char* x2src = sptr(ws, WS_AGG + cg * 16 * 640);
  u32x2 xh[4];
  u32x4 xl[4];
  auto xload = [&](auto N) {
    constexpr int n = N;
    if constexpr (n < 4) xh[n] = ld8_sc1(x1src + n * 6784, x1off);
    else { const int j = w + 4 * (n - 4); xl[n - 4] = ld16u_sc1(x2src + (j < rb ? j : 0) * 640, x2off); }
  };
  base3(IC<0>{});
  SB();
  sfor<0, 6>([&](auto I) { rd3(IC<0>{}, I); });
  SB();
  sfor<0, 20>([&](auto KS) {
    constexpr int ks = KS;
    if constexpr (((ks + 1) & 3) == 0 && ks + 1 < 20) { base3(IC<(ks + 1) / 4>{}); SB(); }
    if constexpr (ks == 1) { asm volatile("s_barrier" ::: "memory"); SB(); }
    sfor<0, 6>([&](auto I) {
      constexpr int i = I;
      accQ[i >> 1][i & 1] = mfma(cat(bh[ks & 1][i >> 1][0], bh[ks & 1][i >> 1][1]), xa[ks][i & 1], accQ[i >> 1][i & 1]);
      if constexpr (ks + 1 < 20) rd3(IC<ks + 1>{}, I);
      if constexpr (ks >= 2 && ks < 12 && (i == 1 || i == 4)) gc(IC<(ks - 2) / 5>{}, IC<2 * ((ks - 2) % 5) + (i == 4)>{});
      if constexpr (ks >= 14 && ks < 18 && (i == 1 || i == 4)) xload(IC<2 * (ks - 14) + (i == 4)>{});
      SB();
    });
  });
  STAMP(12);

  float gv[2];
#pragma unroll
  for (int mt = 0; mt < 2; ++mt) {
    const float s = rbf(accQ[2][mt][0]);
    const float gg = rbf(__builtin_amdgcn_rcpf(1.f + __expf(-s)));
    const int tl = 32 * w + 8 * (r >> 2) + 4 * mt + (r & 3);
    if (g == 2) {
      *(LAS float*)(lds + L_G + tl * 4) = gg;
      if (cg == 0) store4_sc1(ws, WS_G + (128 * rb + tl) * 4, __float_as_uint(gg));
    }
    gv[mt] = __shfl(gg, 32 + r);
  }
  {
    auto cpow = [](float lmag, float rev, float n) {
      const float m = __builtin_amdgcn_exp2f(n * lmag), f = __builtin_amdgcn_fractf(n * rev);
      return f32x2{m * __builtin_amdgcn_cosf(f), m * __builtin_amdgcn_sinf(f)};
    };
    const float nseg = (float)(8 * (4 * w + g));
#pragma unroll
    for (int p = 0; p < 3; ++p) {
      const int src = p < 2 ? 16 * p + r : 32 + (r & 7);
      pw[p] = cpow(__shfl(c_lmag, src), __shfl(c_rev, src), nseg);
    }
#pragma unroll
    for (int i = 0; i < 4; ++i) pj[i] = cpow(c_lmag, c_rev, (float)(128 * max(rb - 1 - w - 4 * i, 0)));
    asm volatile("" : "+v"(pw[0]), "+v"(pw[1]), "+v"(pw[2]), "+v"(pj[0]), "+v"(pj[1]), "+v"(pj[2]), "+v"(pj[3]), "+v"(gv[0]), "+v"(gv[1]));
  }
  STAMP(13);

  asm volatile("s_waitcnt vmcnt(0)"
               : "+v"(xh[0]), "+v"(xh[1]), "+v"(xh[2]), "+v"(xh[3]), "+v"(xl[0]), "+v"(xl[1]), "+v"(xl[2]), "+v"(xl[3])::"memory");
  {
    unsigned spins = 0;
    for (;;) {
      bool ok = true;
#pragma unroll
      for (int n = 0; n < 4; ++n) {
        ok &= xh[n][1] == tag || !act1;
        ok &= (xl[n][1] == tag && xl[n][3] == tag) || w + 4 * n >= rb;
      }
      if (__all(ok)) break;
      if (spin_fail(spins, tmo, tag)) break;
#pragma unroll
      for (int n = 0; n < 4; ++n) {
        const int j = w + 4 * n;
        xh[n] = ld8_wait(x1src + n * 6784, x1off);
        xl[n] = ld16_wait(x2src + (j < rb ? j : 0) * 640, x2off);
      }
    }
    *(LAS float*)(lds + L_H + tid * 4) =
        (__uint_as_float(xh[0][0]) + __uint_as_float(xh[1][0])) + (__uint_as_float(xh[2][0]) + __uint_as_float(xh[3][0]));
    f32x2 c = {0.f, 0.f};
#pragma unroll
    for (int n = 0; n < 4; ++n)
      if (w + 4 * n < rb) c = cfma(pj[n], f32x2{__uint_as_float(xl[n][0]), __uint_as_float(xl[n][2])}, c);
    if (lane < 40) *(LAS f32x2*)(lds + L_C + (w * 40 + lane) * 8) = c;
  }
  LAS char* crA[2];
  LAS char* crB[2];
#pragma unroll
  for (int kk = 0; kk < 2; ++kk) {
    const int lp = r * 128 + (((4 * kk + g) ^ ((r >> 1) & 7)) << 4);
    crA[kk] = lds + A0 + w * 2 * UNIT + lp; crB[kk] = lds + R1 + w * UNIT + lp;
  }
  asm volatile("" : "+v"(crA[0]), "+v"(crA[1]), "+v"(crB[0]), "+v"(crB[1]));
  bf16x8 bu[4][5];
#pragma unroll
  for (int kp = 0; kp < 4; ++kp)
#pragma unroll
    for (int nt = 0; nt < 5; ++nt) bu[kp][nt] = lds_frag(crA[kp & 1] + (kp >> 1) * UNIT + nt * 2048);
  STAMP(14);
  LSYNC();
  STAMP(15);
  if (w == 0) {
    const float s = (*(LAS float*)(lds + L_H + lane * 4) + *(LAS float*)(lds + L_H + 256 + lane * 4)) +
                    (*(LAS float*)(lds + L_H + 512 + lane * 4) + *(LAS float*)(lds + L_H + 768 + lane * 4));
    if (act1) gstore<0>(sptr(ws, WS_FIN + cg * 848 * 8), i1 * 8, s, tag);
  }

  u32x2 fv[4];
  const unsigned foff[4] = {(unsigned)tid * 8, (unsigned)(tid + 256) * 8, (unsigned)(tid + 512) * 8, (unsigned)(tid < 72 ? tid + 768 : 0) * 8};
  const char* fsrc = sptr(ws, WS_FIN + cg * 848 * 8);
  {
    const f32x4 ga = *(LAS f32x4*)(lds + L_G + (32 * w + 8 * g) * 4), gb4 = *(LAS f32x4*)(lds + L_G + (32 * w + 8 * g + 4) * 4);
    LAS char* img = lds + B0 + w * 6400;
    f32x2 h[3];
#pragma unroll
    for (int p = 0; p < 3; ++p) {
      const int chl = p < 2 ? 16 * p + r : 32 + (r & 7);
      LAS char* cp = lds + L_C + chl * 8;
      const f32x2 cin = (*(LAS f32x2*)cp + *(LAS f32x2*)(cp + 320)) + (*(LAS f32x2*)(cp + 640) + *(LAS f32x2*)(cp + 960));
      h[p] = cfma(pw[p], cin, *(LAS f32x2*)(lds + L_E + ((4 * w + g) * 40 + chl) * 8));
    }
    SB();
    sfor<0, 8>([&](auto TT) {
      constexpr int t = TT;
      f32x2 t1[3];
#pragma unroll
      for (int p = 0; p < 3; ++p) t1[p] = cfma_1(pa[p], h[p], ub[p][t]);
      SB();
#pragma unroll
      for (int p = 0; p < 3; ++p) h[p] = cfma_2(pa[p], h[p], t1[p]);
      SB();
#pragma unroll
      for (int p = 0; p < 3; ++p) t1[p] = cco_1(pc[p], h[p]);
      SB();
#pragma unroll
      for (int p = 0; p < 3; ++p) t1[p] = cco_2(pc[p], h[p], t1[p]);
      SB();
#pragma unroll
      for (int p = 0; p < 3; ++p) ub[p][t] = t1[p] * (t < 4 ? ga[t & 3] : gb4[t & 3]);
      gc(IC<2>{}, TT);
      SB();
    });
#pragma unroll
    for (int p = 0; p < 3; ++p) {
#pragma unroll
      for (int mt = 0; mt < 2; ++mt) {
        const f32x2* y = &ub[p][4 * mt];
        const u32x2 vr = {pk2(y[0].x, y[1].x), pk2(y[2].x, y[3].x)};
        const u32x2 vi = {pk2(y[0].y, y[1].y), pk2(y[2].y, y[3].y)};
        if (p < 2) {
          *(LAS u32x2*)(img + mt * 3200 + (16 * p + r) * 40 + 8 * g) = vr;
          *(LAS u32x2*)(img + mt * 3200 + (40 + 16 * p + r) * 40 + 8 * g) = vi;
        } else {
          const int row = r < 8 ? 32 + r : 72 + (r - 8);
          *(LAS u32x2*)(img + mt * 3200 + row * 40 + 8 * g) = r < 8 ? vr : vi;
        }
      }
    }
    gc(IC<2>{}, IC<8>{}); gc(IC<2>{}, IC<9>{});
    asm volatile("s_waitcnt lgkmcnt(0)" ::: "memory");
#pragma unroll
    for (int f = 0; f < 5; ++f) {
      const int mt = f < 4 ? (f >> 1) : (g >> 1);
      const int o8 = f < 4 ? 4 * (f & 1) + g : 8 + (g & 1);
      LAS char* p0 = img + mt * 3200 + (8 * o8 + q4) * 40 + 8 * pp;
      const bf16x8 v = lds_frag_tr(p0, p0 + 160);
      const int part = o8 >= 5, o = 5 * cg + (part ? o8 - 5 : o8);
      const int q = 16 * (o >> 3) + 8 * part + (o & 7);
      const int rt = 8 * rb + 2 * w + mt;
      store16_sc1(ws, WS_A + (rt * 240 + q) * 256 + r * 16, __builtin_bit_cast(u32x4, v));
    }
  }
  STAMP(16);

#pragma unroll
  for (int it = 0; it < 4; ++it) fv[it] = ld8_sc1(fsrc, foff[it]);
  bf16x8 qf[2][2];
#pragma unroll
  for (int mt = 0; mt < 2; ++mt) {
    unsigned tl[2];
#pragma unroll
    for (int h2 = 0; h2 < 2; ++h2) {
      const unsigned ph = phi2(accQ[2][mt][2 * h2], accQ[2][mt][2 * h2 + 1]);
      tl[h2] = g < 2 ? ph : 0u;
    }
#pragma unroll
    for (int h = 0; h < 2; ++h)
      qf[h][mt] = frag4(phi2(accQ[h][mt][0], accQ[h][mt][1]), phi2(accQ[h][mt][2], accQ[h][mt][3]), tl[0], tl[1]);
  }
  asm volatile("" : "+v"(qf[0][0]), "+v"(qf[0][1]), "+v"(qf[1][0]), "+v"(qf[1][1]));
  unsigned kdst[4];
#pragma unroll
  for (int it = 0; it < 4; ++it) {
    const int i = tid + 256 * it, h = i >= 420, ii = i - 420 * h;
    const int dd = (ii * 3277) >> 16;
    const int e = ii < 400 ? ii - 20 * dd : 20, d = ii < 400 ? dd : ii - 400;
    kdst[it] = L_KVT + (h * 21 + e) * 40 + d * 2;
  }
  asm volatile("" : "+v"(kdst[0]), "+v"(kdst[1]), "+v"(kdst[2]), "+v"(kdst[3]));
  asm volatile("s_waitcnt vmcnt(0)\n\ts_barrier" : "+v"(fv[0]), "+v"(fv[1]), "+v"(fv[2]), "+v"(fv[3])::"memory");
  if (tid == 0) __hip_atomic_store((GAS unsigned*)(ws + WS_HFLAG) + rb * 16 + cg, tag, __ATOMIC_RELAXED, __HIP_MEMORY_SCOPE_AGENT);
  {
    unsigned spins = 0;
    for (;;) {
      const bool ok = fv[0][1] == tag && fv[1][1] == tag && fv[2][1] == tag && fv[3][1] == tag;
      if (__all(ok)) break;
      if (spin_fail(spins, tmo, tag)) break;
#pragma unroll
      for (int it = 0; it < 4; ++it) fv[it] = ld8_wait(fsrc, foff[it]);
    }
#pragma unroll
    for (int it = 0; it < 4; ++it) {
      const float f = __uint_as_float(fv[it][0]);
      if (it < 3 || tid < 72) *(LAS u16*)(lds + kdst[it]) = (u16)pk2(f, f);
    }
  }
  STAMP(17);
  LSYNC();

  {
    f32x4 num[2][2], nt[2], den[2][2];
#pragma unroll
    for (int mt = 0; mt < 2; ++mt) nt[mt] = f32x4{0, 0, 0, 0};
    sfor<0, 2>([&](auto H) {
      constexpr int h = H;
      LAS char* kbp = lds + L_KVT + h * 840;
      const u32x2 z2 = {0u, 0u};
      const u32x2 ml = *(LAS u32x2*)(kbp + r * 40 + 8 * g), mh = *(LAS u32x2*)(kbp + r * 40 + 32);
      const u32x2 tl = *(LAS u32x2*)(kbp + (16 + (r & 3)) * 40 + 8 * g), th = *(LAS u32x2*)(kbp + (16 + (r & 3)) * 40 + 32);
      const u32x2 dl = *(LAS u32x2*)(kbp + 800 + 8 * g), dh = *(LAS u32x2*)(kbp + 800 + 32);
      const bool gh = g == h, rv = (r >> 2) == h;
      const u32x2 mh2 = gh ? mh : z2, dh2 = gh ? dh : z2, tl2 = rv ? tl : z2, th2 = (rv && gh) ? th : z2;
      const bf16x8 am = frag4(ml[0], ml[1], mh2[0], mh2[1]);
      const bf16x8 at = frag4(tl2[0], tl2[1], th2[0], th2[1]);
      const bf16x8 ad = frag4(dl[0], dl[1], dh2[0], dh2[1]);
#pragma unroll
      for (int mt = 0; mt < 2; ++mt) {
        num[h][mt] = mfma(am, qf[h][mt], f32x4{0, 0, 0, 0});
        nt[mt] = mfma(at, qf[h][mt], nt[mt]);
        den[h][mt] = mfma(ad, qf[h][mt], f32x4{0, 0, 0, 0});
      }
    });
    sfor<0, 2>([&](auto MT) {
      constexpr int mt = MT;
      const float og = rbf(1.f - gv[mt]);
      float sc[2];
#pragma unroll
      for (int h = 0; h < 2; ++h) sc[h] = og * __builtin_amdgcn_rcpf(den[h][mt][0] + 1e-6f);
      const int rt = 8 * rb + 2 * w + mt;
      const u32x4 ov = {pk2(num[0][mt][0] * sc[0], num[0][mt][1] * sc[0]), pk2(num[0][mt][2] * sc[0], num[0][mt][3] * sc[0]),
                        pk2(num[1][mt][0] * sc[1], num[1][mt][1] * sc[1]), pk2(num[1][mt][2] * sc[1], num[1][mt][3] * sc[1])};
      store16_sc1(ws, WS_A + (rt * 240 + 160 + 5 * cg + g) * 256 + r * 16, ov);
      const float st = g == 0 ? sc[0] : sc[1];
      const u32x2 tv = {pk2(nt[mt][0] * st, nt[mt][1] * st), pk2(nt[mt][2] * st, nt[mt][3] * st)};
      if (g < 2) store8_sc1(ws, WS_A + (rt * 240 + 160 + 5 * cg + 4) * 256 + r * 16 + 8 * g, tv);
    });
  }
  STAMP(18);

  u32x2 exv[5], edv[5];
  unsigned egv[5], eoff[5], elds[5], goff[5];
#pragma unroll
  for (int it = 0; it < 5; ++it) {
    const int item = tid + 256 * it, tp = (item * 3277) >> 16, dq = item - 20 * tp;
    const int mt = tp >> 4, rr = tp & 15;
    const int t = 64 * T + 32 * (mt >> 1) + 8 * (rr >> 2) + 4 * (mt & 1) + (rr & 3);
    const int d = 80 * jt + 4 * dq;
    eoff[it] = (t * 640 + d) * 4; elds[it] = (tp * 84 + 4 * dq) * 4;
    exv[it] = ld8(x, eoff[it] >> 1);
    edv[it] = ld8(dskip, d * 2);
    goff[it] = WS_G + t * 4;
  }
  {
    const unsigned hoff = WS_HFLAG + ((T >> 1) * 16 + r) * 4;
    unsigned spins = 0;
    for (;;) {
      const unsigned f = ld4_wait(ws, hoff);
      if (__all(f == tag)) break;
      if (spin_fail(spins, tmo, tag)) break;
    }
  }
  STAMP(19);

  const char* abase = sptr(ws, WS_A + 4 * T * 240 * 256);
  const unsigned alane = lane * 16;
  bf16x8 a[4][4];
  auto ga = [&](auto KP) {
    constexpr int kp = KP;
    const unsigned q = kp < 10 ? 32 * (kp >> 1) + 16 * (w >> 1) + 8 * (w & 1) + 4 * (kp & 1) : 160 + 16 * (kp - 10) + 4 * w;
    const char* p = abase + q * 256;
    sfor<0, 4>([&](auto MT) {
      constexpr int mt = MT;
      a[kp & 3][mt] = ld16_sc1(p + mt * 240 * 256, alane);
    });
  };
  ga(IC<0>{}); ga(IC<1>{}); ga(IC<2>{});
#pragma unroll
  for (int it = 0; it < 5; ++it) egv[it] = ld4_sc1(ws, goff[it]);
  sfor<0, 10>([&](auto N) { gc(IC<3>{}, N); });
  if (TOUCHA) {
    const unsigned li = 40 * w + min(lane, 39);
    const unsigned rtl = li / 40, rem = li - 40 * rtl;
    touch_s_sc1(abase, rtl * 240 * 256 + (20 * jt) * 256 + rem * 128, ldsb + L_DUMMY);
  }
  asm volatile("s_barrier" ::: "memory");
  if (tid == 0) {
    __hip_atomic_store((GAS unsigned*)(ws + WS_OFLAG) + rb * 16 + cg, tag, __ATOMIC_RELAXED, __HIP_MEMORY_SCOPE_AGENT);
    if (blockIdx.x == 0) __hip_atomic_store((GAS unsigned*)(ws + WS_EPOCH), tag, __ATOMIC_RELAXED, __HIP_MEMORY_SCOPE_AGENT);
  }
  asm volatile("" : "+v"(exv[0]), "+v"(exv[1]), "+v"(exv[2]), "+v"(exv[3]), "+v"(exv[4]), "+v"(edv[0]), "+v"(edv[1]), "+v"(edv[2]),
               "+v"(edv[3]), "+v"(edv[4])::"memory");
  f32x4 skip[5];
#pragma unroll
  for (int it = 0; it < 5; ++it) {
    const unsigned s0 = pk2(blo(exv[it][0]) * blo(edv[it][0]), bhi(exv[it][0]) * bhi(edv[it][0]));
    const unsigned s1 = pk2(blo(exv[it][1]) * blo(edv[it][1]), bhi(exv[it][1]) * bhi(edv[it][1]));
    skip[it] = f32x4{blo(s0), bhi(s0), blo(s1), bhi(s1)};
  }
  sfor<0, 10>([&](auto N) { gc(IC<4>{}, N); });
  asm volatile("" : "+v"(skip[0]), "+v"(skip[1]), "+v"(skip[2]), "+v"(skip[3]), "+v"(skip[4]), "+v"(eoff[0]), "+v"(eoff[1]), "+v"(eoff[2]),
               "+v"(eoff[3]), "+v"(eoff[4]), "+v"(elds[0]), "+v"(elds[1]), "+v"(elds[2]), "+v"(elds[3]), "+v"(elds[4]));
  SB();
  STAMP(20);

  const char* wsrc = sptr(ws, WS_WO + (jt * 20 + w) * 5120 + 2048);
  auto gw_ = [&](auto M, auto N) {
    constexpr int m = M, n = N;
    if constexpr (m < 2) glds<1, m * 5120 + n * 1024, n * 1024 - 2048>(wsrc + m * 4 * 5120, lin, dB);
    else glds<1, (m - 2) * 5120 + n * 1024, n * 1024 - 2048>(wsrc + m * 4 * 5120, lin, dA);
  };
  const int wlp0 = (8 * g + q4 + 4 * (g & 1)) * 160 + 8 * pp, wlp1 = (8 * g + q4 + 4 * (1 - (g & 1))) * 160 + 8 * pp;
  LAS char* wrA[2] = {lds + A0 + w * 2 * UNIT + wlp0, lds + A0 + w * 2 * UNIT + wlp1};
  LAS char* wrB[2] = {lds + R1 + w * UNIT + wlp0, lds + R1 + w * UNIT + wlp1};
  asm volatile("" : "+v"(wrA[0]), "+v"(wrA[1]), "+v"(wrB[0]), "+v"(wrB[1]));
  bf16x8 bf[2][5];
  auto rd = [&](auto KP, auto I) {
    constexpr int kp = KP, i = I;
    if constexpr (kp < 4) {
    } else if constexpr (kp < 10) {
      if constexpr (i < 5) bf[kp & 1][i] = lds_frag(kp < 6 ? crB[kp & 1] + i * 2048 : crA[kp & 1] + (kp < 8 ? 0 : UNIT) + i * 2048);
    } else {
      constexpr int m = kp - 10;
      bh[kp & 1][i >> 1][i & 1] = __builtin_amdgcn_ds_read_tr16_b64_v4i16(
          (LAS bf16x4*)(m < 2 ? wrB[i & 1] + m * 5120 + 32 * (i >> 1) : wrA[i & 1] + (m - 2) * 5120 + 32 * (i >> 1)));
    }
  };
  auto bfrag = [&](auto KP, auto NT) {
    constexpr int kp = KP, nt = NT;
    if constexpr (kp < 4) return bu[kp][nt];
    else if constexpr (kp < 10) return bf[kp & 1][nt];
    else return (bf16x8)__builtin_shufflevector(bh[kp & 1][nt][0], bh[kp & 1][nt][1], 0, 1, 2, 3, 4, 5, 6, 7);
  };
  f32x4 acc[4][5];
#pragma unroll
  for (int mt = 0; mt < 4; ++mt)
#pragma unroll
    for (int nt = 0; nt < 5; ++nt) acc[mt][nt] = f32x4{0, 0, 0, 0};
  unsigned of = 0;
  const unsigned ooff = WS_OFLAG + ((T >> 1) * 16 + r) * 4;
#define AWAITN(N, S) do { asm volatile("s_waitcnt vmcnt(%4) lgkmcnt(0)" : "+v"(a[S][0]), "+v"(a[S][1]), "+v"(a[S][2]), "+v"(a[S][3]) : "n"(N) : "memory"); SB(); } while (0)
#define AWAIT(N, S) do { asm volatile("s_waitcnt vmcnt(" #N ") lgkmcnt(0)" : "+v"(a[S][0]), "+v"(a[S][1]), "+v"(a[S][2]), "+v"(a[S][3])::"memory"); SB(); } while (0)
  sfor<0, 15>([&](auto KP) {
    constexpr int kp = KP, sl = kp & 3;
    if constexpr (kp <= 2) AWAITN(33 + TAN, sl);
    if constexpr (kp == 3 || kp == 4) AWAIT(8, sl);
    if constexpr (kp == 3) asm volatile("" : "+v"(egv[0]), "+v"(egv[1]), "+v"(egv[2]), "+v"(egv[3]), "+v"(egv[4])::"memory");
    if constexpr (kp == 5) AWAIT(9, sl);
    if constexpr (kp == 6) AWAIT(19, sl);
    if constexpr (kp == 13) AWAIT(4, sl);
    if constexpr (kp == 7 || kp == 9) AWAIT(18, sl);
    if constexpr (kp == 7) asm volatile("" : "+v"(of)::"memory");
    if constexpr (kp == 8) AWAIT(28, sl);
    if constexpr (kp == 12) AWAIT(13, sl);
    if constexpr (kp == 10) AWAIT(23, sl);
    if constexpr (kp == 11) AWAIT(13, sl);
    if constexpr (kp == 14) AWAIT(0, sl);
    if constexpr (kp == 7) {
      if (!__all(of == tag)) {
        unsigned spins = 0;
        for (;;) {
          of = ld4_wait(ws, ooff);
          if (__all(of == tag)) break;
          if (spin_fail(spins, tmo, tag)) break;
        }
      }
      SB();
    }
    if constexpr (kp == 5) STAMP(21);
    if constexpr (kp == 10) STAMP(22);
    if constexpr (kp + 3 < 15) ga(IC<kp + 3>{});
    sfor<0, 20>([&](auto I) {
      constexpr int i = I, nt = i >> 2, mt = i & 3;
      acc[mt][nt] = mfma(bfrag(KP, IC<nt>{}), a[sl][mt], acc[mt][nt]);
      if constexpr (kp + 1 < 15 && i < 10) rd(IC<kp + 1>{}, I);
      if constexpr ((kp == 5 || kp == 7) && (i & 1) == 1) gw_(IC<kp - 5 + (i >= 10)>{}, IC<(i % 10) / 2>{});
      if constexpr (kp == 9 && (i & 3) == 1) gw_(IC<4>{}, IC<(i >> 2)>{});
      if constexpr (kp == 4 && i == 19) of = ld4_sc1(ws, ooff);
      SB();
    });
  });
  STAMP(23);
  LSYNC();
#pragma unroll
  for (int mt = 0; mt < 4; ++mt)
#pragma unroll
    for (int nt = 0; nt < 5; ++nt)
      *(LAS f32x4*)(lds + ((w * 64 + 16 * mt + r) * 84 + 16 * nt + 4 * g) * 4) = acc[mt][nt];
  LSYNC();
  STAMP(24);
#pragma unroll
  for (int it = 0; it < 5; ++it) {
    const f32x4 s0 = *(LAS f32x4*)(lds + elds[it]), s1 = *(LAS f32x4*)(lds + elds[it] + 64 * 84 * 4);
    const f32x4 s2 = *(LAS f32x4*)(lds + elds[it] + 2 * 64 * 84 * 4), s3 = *(LAS f32x4*)(lds + elds[it] + 3 * 64 * 84 * 4);
    *(f32x4*)((char*)out + eoff[it]) = ((s0 + s1) + (s2 + s3)) + skip[it] * __uint_as_float(egv[it]);
  }
  asm volatile(".Lkf_end:");
  STAMP(25);
  CSTAMP(31);
  STAMP_OUT(ws, 0);
}

extern "C" void kernel_launch(void* const* d_in, const int* in_sizes, int n_in, void* d_out, int out_size, void* d_ws,
                              size_t ws_size, hipStream_t stream) {
  const u16* x = (const u16*)d_in[0];
  const u16* lre = (const u16*)d_in[1];
  const u16* lim = (const u16*)d_in[2];
  const u16* bre = (const u16*)d_in[3];
  const u16* bim = (const u16*)d_in[4];
  const u16* cre = (const u16*)d_in[5];
  const u16* cim = (const u16*)d_in[6];
  const u16* ldt = (const u16*)d_in[7];
  const u16* dskip = (const u16*)d_in[8];
  const u16* wqkv = (const u16*)d_in[10];
  const u16* wo = (const u16*)d_in[11];
  const u16* gw = (const u16*)d_in[12];
  constexpr int LDS = 163840;
  (void)hipFuncSetAttribute((const void*)k_fused, hipFuncAttributeMaxDynamicSharedMemorySize, LDS);
  k_fused<<<256, 256, LDS, stream>>>(x, lre, lim, ldt, bre, bim, wqkv, gw, cre, cim, wo, dskip, (char*)d_ws, (float*)d_out);
}
```

```cpp
#include <hip/hip_runtime.h>
#include <stdint.h>

typedef unsigned short u16;
typedef unsigned long long u64;
typedef short bf16x8 __attribute__((ext_vector_type(8)));
typedef short bf16x4 __attribute__((ext_vector_type(4)));
typedef float f32x4 __attribute__((ext_vector_type(4)));
typedef float f32x2 __attribute__((ext_vector_type(2)));
typedef unsigned u32x4 __attribute__((ext_vector_type(4)));
typedef unsigned u32x2 __attribute__((ext_vector_type(2)));
typedef __bf16 bf16v2 __attribute__((ext_vector_type(2)));
#define LAS __attribute__((address_space(3)))
#define GAS __attribute__((address_space(1)))
#define DI __device__ __forceinline__

constexpr unsigned WS_CFLAG = 0;
constexpr unsigned WS_HFLAG = 1024;
constexpr unsigned WS_OFLAG = 3072;
constexpr unsigned WS_TMO   = 2048;
constexpr unsigned WS_EPOCH = 2304;
constexpr unsigned WS_AGG   = 4096;
constexpr unsigned WS_PART  = WS_AGG + 16 * 16 * 80 * 8;
constexpr unsigned WS_FIN   = WS_PART + 16 * 16 * 848 * 8;
constexpr unsigned WS_ZEND  = WS_FIN + 16 * 848 * 8;
constexpr unsigned WS_WKV   = (WS_ZEND + 4095) / 4096 * 4096;
constexpr unsigned WS_WQ    = WS_WKV + 16 * 102400;
constexpr unsigned WS_WO    = WS_WQ + 16 * 61440;
constexpr unsigned WS_A     = WS_WO + 8 * 20 * 5120;
constexpr unsigned WS_G     = WS_A + 128 * 240 * 256;
constexpr unsigned WS_STAMP = (WS_G + 8192 + 4095) / 4096 * 4096;
constexpr unsigned SPIN_CAP = 1u << 15;

DI unsigned pk2(float a, float b) { f32x2 p = {a, b}; return __builtin_bit_cast(unsigned, __builtin_convertvector(p, bf16v2)); }
DI float blo(unsigned u) { return __uint_as_float(u << 16); }
DI float bhi(unsigned u) { return __uint_as_float(u & 0xffff0000u); }
DI float rbf(float a) { return blo(pk2(a, a)); }
DI float bf2f(unsigned v) { return __uint_as_float(v << 16); }
DI bf16x8 frag4(unsigned a, unsigned b, unsigned c, unsigned d) { u32x4 v = {a, b, c, d}; return __builtin_bit_cast(bf16x8, v); }
DI f32x4 mfma(bf16x8 a, bf16x8 b, f32x4 c) { return __builtin_amdgcn_mfma_f32_16x16x32_bf16(a, b, c, 0, 0, 0); }
DI bf16x8 lds_frag(LAS char* p) { return *(LAS bf16x8*)p; }
DI bf16x8 lds_frag_tr(LAS char* p0, LAS char* p1) {
  bf16x4 a = __builtin_amdgcn_ds_read_tr16_b64_v4i16((LAS bf16x4*)p0);
  bf16x4 b = __builtin_amdgcn_ds_read_tr16_b64_v4i16((LAS bf16x4*)p1);
  return __builtin_shufflevector(a, b, 0, 1, 2, 3, 4, 5, 6, 7);
}
template <int SC1, unsigned IMM, int GOFF = 0>
DI void glds(const void* sbase, unsigned voff, unsigned ldsbase) {
  if constexpr (SC1)
    asm volatile("s_add_u32 m0, %2, %3\n\ts_nop 0\n\tglobal_load_lds_dwordx4 %0, %1 offset:%4 sc1" ::"v"(voff), "s"(sbase), "s"(ldsbase), "i"(IMM - GOFF), "i"(GOFF) : "memory", "scc");
  else
    asm volatile("s_add_u32 m0, %2, %3\n\ts_nop 0\n\tglobal_load_lds_dwordx4 %0, %1 offset:%4" ::"v"(voff), "s"(sbase), "s"(ldsbase), "i"(IMM - GOFF), "i"(GOFF) : "memory", "scc");
}
template <int V> struct IC { static constexpr int value = V; constexpr operator int() const { return V; } };
template <int I, int N, class F>
DI void sfor(F&& f) { if constexpr (I < N) { f(IC<I>{}); sfor<I + 1, N>(f); } }
DI void touch(const void* p, unsigned ldsdummy) {
  asm volatile("s_mov_b32 m0, %1\n\ts_nop 0\n\tglobal_load_lds_dword %0, off" ::"v"(p), "s"(ldsdummy) : "memory");
}
DI void touch_s(const void* sbase, unsigned voff, unsigned ldsdummy) {
  asm volatile("s_mov_b32 m0, %2\n\ts_nop 0\n\tglobal_load_lds_dword %0, %1" ::"v"(voff), "s"(sbase), "s"(ldsdummy) : "memory");
}
DI void touch_s_sc1(const void* sbase, unsigned voff, unsigned ldsdummy) {
  asm volatile("s_mov_b32 m0, %2\n\ts_nop 0\n\tglobal_load_lds_dword %0, %1 sc1" ::"v"(voff), "s"(sbase), "s"(ldsdummy) : "memory");
}
DI void store16_sc1(void* sbase, unsigned voff, u32x4 v) {
  asm volatile("global_store_dwordx4 %0, %1, %2 sc1\n\ts_nop 1" ::"v"(voff), "v"(v), "s"(sbase) : "memory");
}
#define SYNC(N) do { asm volatile("s_waitcnt vmcnt(%0) lgkmcnt(0)\n\ts_barrier" ::"n"(N) : "memory"); __builtin_amdgcn_sched_barrier(0); } while (0)
#define LSYNC() do { asm volatile("s_waitcnt lgkmcnt(0)\n\ts_barrier" ::: "memory"); __builtin_amdgcn_sched_barrier(0); } while (0)
DI u64 gload(const GAS u64* p) { return __hip_atomic_load(p, __ATOMIC_RELAXED, __HIP_MEMORY_SCOPE_AGENT); }
template <int OFF>
DI void gstore(const void* sbase, unsigned voff, float v, unsigned tag) {
  const u32x2 d = {__float_as_uint(v), tag};
  asm volatile("global_store_dwordx2 %0, %1, %2 offset:%3 sc1" ::"v"(voff), "v"(d), "s"(sbase), "i"(OFF) : "memory");
}
DI u32x2 ld8(const void* sbase, unsigned voff) { u32x2 t; asm volatile("global_load_dwordx2 %0, %1, %2" : "=v"(t) : "v"(voff), "s"(sbase) : "memory"); return t; }
DI u32x4 ld16(const void* sbase, unsigned voff) { u32x4 t; asm volatile("global_load_dwordx4 %0, %1, %2" : "=v"(t) : "v"(voff), "s"(sbase) : "memory"); return t; }
DI unsigned ld4_sc1(const void* sbase, unsigned voff) { unsigned t; asm volatile("global_load_dword %0, %1, %2 sc1" : "=v"(t) : "v"(voff), "s"(sbase) : "memory"); return t; }
DI bf16x8 ld16_sc1(const void* sbase, unsigned voff) { bf16x8 t; asm volatile("global_load_dwordx4 %0, %1, %2 sc1" : "=v"(t) : "v"(voff), "s"(sbase) : "memory"); return t; }
DI u32x4 ld16u_sc1(const void* sbase, unsigned voff) { u32x4 t; asm volatile("global_load_dwordx4 %0, %1, %2 sc1" : "=v"(t) : "v"(voff), "s"(sbase) : "memory"); return t; }
DI u32x2 ld8_sc1(const void* sbase, unsigned voff) { u32x2 t; asm volatile("global_load_dwordx2 %0, %1, %2 sc1" : "=v"(t) : "v"(voff), "s"(sbase) : "memory"); return t; }
DI u32x2 ld8_wait(const void* sbase, unsigned voff) { u32x2 t; asm volatile("global_load_dwordx2 %0, %1, %2 sc1\n\ts_waitcnt vmcnt(0)" : "=&v"(t) : "v"(voff), "s"(sbase) : "memory"); return t; }
DI u32x4 ld16_wait(const void* sbase, unsigned voff) { u32x4 t; asm volatile("global_load_dwordx4 %0, %1, %2 sc1\n\ts_waitcnt vmcnt(0)" : "=&v"(t) : "v"(voff), "s"(sbase) : "memory"); return t; }
DI unsigned ld4_wait(const void* sbase, unsigned voff) { unsigned t; asm volatile("global_load_dword %0, %1, %2 sc1\n\ts_waitcnt vmcnt(0)" : "=&v"(t) : "v"(voff), "s"(sbase) : "memory"); return t; }
DI const char* uptr(const void* p) {
  const u64 v = (u64)p;
  return (const char*)(((u64)(unsigned)__builtin_amdgcn_readfirstlane((unsigned)(v >> 32)) << 32) | (unsigned)__builtin_amdgcn_readfirstlane((unsigned)v));
}
DI const char* sptr(const void* base, unsigned off) { return (const char*)base + (unsigned)__builtin_amdgcn_readfirstlane(off); }
DI void store8_sc1(void* sbase, unsigned voff, u32x2 v) {
  asm volatile("global_store_dwordx2 %0, %1, %2 sc1" ::"v"(voff), "v"(v), "s"(sbase) : "memory");
}
DI void store4_sc1(void* sbase, unsigned voff, unsigned v) {
  asm volatile("global_store_dword %0, %1, %2 sc1" ::"v"(voff), "v"(v), "s"(sbase) : "memory");
}
DI bool spin_fail(unsigned& spins, GAS unsigned* tmo, unsigned tag) {
  __builtin_amdgcn_s_sleep(1);
  if (++spins > SPIN_CAP) { __hip_atomic_store(tmo, tag, __ATOMIC_RELAXED, __HIP_MEMORY_SCOPE_AGENT); return true; }
  if ((spins & 63u) == 0u)
    return (unsigned)__builtin_amdgcn_readfirstlane(__hip_atomic_load(tmo, __ATOMIC_RELAXED, __HIP_MEMORY_SCOPE_AGENT)) == tag;
  return false;
}
DI float phi1(float t) {
  return (__builtin_amdgcn_exp2f(fminf(t, 0.f) * 1.4426950408889634f) + fmaxf(t, 0.f)) + 1e-6f;
}
DI f32x2 cfma(f32x2 a, f32x2 h, f32x2 u) {
  f32x2 t, o;
  asm("v_pk_fma_f32 %0, %1, %2, %3 op_sel_hi:[0,1,1]" : "=v"(t) : "v"(a), "v"(h), "v"(u));
  asm("v_pk_fma_f32 %0, %1, %2, %3 op_sel:[1,1,0] op_sel_hi:[1,0,1] neg_lo:[1,0,0]" : "=v"(o) : "v"(a), "v"(h), "v"(t));
  return o;
}
DI f32x2 cfma_1(f32x2 a, f32x2 h, f32x2 u) {
  f32x2 t;
  asm("v_pk_fma_f32 %0, %1, %2, %3 op_sel_hi:[0,1,1]" : "=v"(t) : "v"(a), "v"(h), "v"(u));
  return t;
}
DI f32x2 cfma_2(f32x2 a, f32x2 h, f32x2 t) {
  f32x2 o;
  asm("v_pk_fma_f32 %0, %1, %2, %3 op_sel:[1,1,0] op_sel_hi:[1,0,1] neg_lo:[1,0,0]" : "=v"(o) : "v"(a), "v"(h), "v"(t));
  return o;
}
DI f32x2 cco_1(f32x2 a, f32x2 h) {
  f32x2 t;
  asm("v_pk_mul_f32 %0, %1, %2 op_sel_hi:[0,1] neg_hi:[1,0]" : "=v"(t) : "v"(a), "v"(h));
  return t;
}
DI f32x2 cco_2(f32x2 a, f32x2 h, f32x2 t) {
  f32x2 o;
  asm("v_pk_fma_f32 %0, %1, %2, %3 op_sel:[1,1,0] op_sel_hi:[1,0,1] neg_lo:[1,0,0] neg_hi:[1,0,0]" : "=v"(o) : "v"(a), "v"(h), "v"(t));
  return o;
}
DI f32x2 cmul(f32x2 a, f32x2 h) {
  f32x2 t, o;
  asm("v_pk_mul_f32 %0, %1, %2 op_sel:[1,1] op_sel_hi:[1,0]" : "=v"(t) : "v"(a), "v"(h));
  asm("v_pk_fma_f32 %0, %1, %2, %3 op_sel_hi:[0,1,1] neg_lo:[0,0,1]" : "=v"(o) : "v"(a), "v"(h), "v"(t));
  return o;
}
DI f32x2 cmul_conj_out(f32x2 a, f32x2 h) {
  f32x2 t, o;
  asm("v_pk_mul_f32 %0, %1, %2 op_sel_hi:[0,1] neg_hi:[1,0]" : "=v"(t) : "v"(a), "v"(h));
  asm("v_pk_fma_f32 %0, %1, %2, %3 op_sel:[1,1,0] op_sel_hi:[1,0,1] neg_lo:[1,0,0] neg_hi:[1,0,0]" : "=v"(o) : "v"(a), "v"(h), "v"(t));
  return o;
}
DI f32x2 csq(f32x2 a) { return cmul(a, a); }
DI unsigned phi2(float a, float b) {
  f32x2 t = {a, b};
  asm("" : "+v"(t));
  a = t.x; b = t.y;
  const f32x2 l = t * 1.4426950408889634f;
  f32x2 e, m;
  e.x = __builtin_amdgcn_fmed3f(__builtin_amdgcn_exp2f(l.x), 0.f, 1.f);
  e.y = __builtin_amdgcn_fmed3f(__builtin_amdgcn_exp2f(l.y), 0.f, 1.f);
  m.x = __builtin_amdgcn_fmed3f(a, 0.f, __builtin_inff()); m.y = __builtin_amdgcn_fmed3f(b, 0.f, __builtin_inff());
  const f32x2 p = (e + m) + 1e-6f;
  return pk2(p.x, p.y);
}
#define TOUCH1 1
#define TOUCH2 1
#define MAP48 1
#define TOUCHD 0
#define TOUCHW 1
#define TOUCHA 0
constexpr int TAN = TOUCHA ? 1 : 0;
constexpr int TDN = TOUCHD ? 1 : 0, T1N = 0, T2N = TOUCH2 ? 2 : 0, TCN = TOUCH1 ? 1 : 0;
#define SB() __builtin_amdgcn_sched_barrier(0)
#define VWAIT(N) do { asm volatile("s_waitcnt vmcnt(%0)" ::"n"(N) : "memory"); SB(); } while (0)

#define STAMP_DECL
#define STAMP(i)
#define CSTAMP(i)
#define STAMP_OUT(ws, k)
__global__ __launch_bounds__(256, 1) void k_fused(const u16* __restrict__ x, const u16* __restrict__ lre, const u16* __restrict__ lim,
                                                  const u16* __restrict__ ldt, const u16* __restrict__ bre, const u16* __restrict__ bim,
                                                  const u16* __restrict__ wqkv, const u16* __restrict__ gw, const u16* __restrict__ cre,
                                                  const u16* __restrict__ cim, const u16* __restrict__ wo, const u16* __restrict__ dskip,
                                                  char* __restrict__ ws, float* __restrict__ out) {
  extern __shared__ __attribute__((aligned(16))) char lds_[];
  LAS char* lds = (LAS char*)lds_;
  const unsigned ldsb = (unsigned)(size_t)lds;
  asm volatile("" ::"s"(x), "s"(lre), "s"(lim), "s"(ldt), "s"(bre), "s"(bim), "s"(wqkv), "s"(gw), "s"(ws));
  const int tid = threadIdx.x, lane = tid & 63;
  const int w = __builtin_amdgcn_readfirstlane(tid >> 6);
  const int xcd = blockIdx.x & 7, idx = blockIdx.x >> 3;
  const int rb = 4 * (xcd & 3) + (idx & 3), cg = 8 * (xcd >> 2) + (idx >> 2);
  const int r = lane & 15, g = lane >> 4;
  GAS unsigned* tmo = (GAS unsigned*)(ws + WS_TMO);
  STAMP_DECL
  STAMP(0);
  CSTAMP(30);

  constexpr unsigned SLOT = 20480;
  constexpr unsigned MISC = 0, A0 = 10240, A1 = A0 + SLOT, A2 = A0 + 2 * SLOT, A3 = A0 + 3 * SLOT, B0 = A0 + 4 * SLOT, B1 = A0 + 5 * SLOT,
                     B2 = A0 + 6 * SLOT;
  constexpr unsigned L_E = MISC;
  constexpr unsigned L_G = MISC + 5120;
  constexpr unsigned L_KVT = MISC + 5632;
  constexpr unsigned L_DUMMY = MISC + 7424;
  constexpr unsigned L_H = MISC + 7680;
  constexpr unsigned L_C = MISC + 8704;
  constexpr unsigned ASLOT[4] = {A0, A1, A2, A3};
  constexpr unsigned BSLOT[3] = {B0, B1, B2};
  constexpr unsigned KVSLOT[5] = {B2, A2, B0, A3, A0};
  constexpr unsigned QSLOT[5] = {B1, B1 + 12288, B1 + 24576, B1 + 36864, B0};

  unsigned xoff[2];
  {
    const int l3 = lane >> 3;
    const unsigned row = 128 * rb + 32 * w + 8 * (l3 >> 2) + (l3 & 3), sw = ((lane & 7) ^ (l3 >> 1)) << 4;
    xoff[0] = row * 1280 + sw;
    xoff[1] = (row + 16) * 1280 + (sw ^ 64);
  }
  const char* const xb1 = (const char*)x + 4 * 1280;
  unsigned boff[5];
  const u16* bbase[5];
#pragma unroll
  for (int i = 0; i < 5; ++i) {
    const int n = 5 * w + i;
    const int tt = n >> 2, rr = 4 * (n & 3) + g;
    const int isim = tt < 4 ? (tt & 1) : (n >= 18);
    const int cbase = tt < 4 ? 16 * (tt >> 1) : 32, cmask = tt < 4 ? 15 : 7;
    const int chl = cbase + (rr & cmask);
    bbase[i] = (const u16*)uptr(isim ? bim : bre);
    boff[i] = (40 * cg + chl) * 1280 + ((r ^ rr) << 4);
  }
  const unsigned xdst = ldsb + 32 * w * 128, bdst = ldsb + 5 * w * 1024, qdst = ldsb + 3 * w * 1024;
  auto gx = [&](auto S, auto N) { constexpr int s = S, n = N; glds<0, ASLOT[s & 3] + n * 1024, s * 128>(n < 2 ? (const char*)x : xb1, xoff[n & 1], xdst); };
  auto gb = [&](auto BE, auto I) { constexpr int be = BE, i = I; glds<0, BSLOT[be % 3] + i * 1024, be * 256>(bbase[i], boff[i], bdst); };
  const unsigned lin = lane * 16;
  const char* kvsrc = sptr(ws, WS_WKV + cg * 102400 + w * 5120 + 2048);
  const char* qsrc = sptr(ws, WS_WQ + cg * 61440 + w * 3072 + 1024);
  auto gkv = [&](auto J, auto I) { constexpr int j = J, i = I; glds<1, KVSLOT[j] + i * 1024, i * 1024 - 2048>(kvsrc + j * 20480, lin, bdst); };
  auto gq = [&](auto M, auto I) { constexpr int m = M, i = I; glds<1, QSLOT[m] + i * 1024, i * 1024 - 1024>(qsrc + m * 12288, lin, qdst); };

  unsigned p_lre, p_lim, p_ldt, p_ep, p_gw;
  {
    asm volatile("global_load_dword %0, %1, %2 sc1" : "=v"(p_ep) : "v"(WS_EPOCH), "s"(ws) : "memory");
    const int l39 = lane < 40 ? lane : 39, ch = 40 * cg + l39;
    asm volatile("global_load_ushort %0, %1, %2" : "=v"(p_lre) : "v"(2 * ch), "s"(lre) : "memory");
    asm volatile("global_load_ushort %0, %1, %2" : "=v"(p_lim) : "v"(2 * ch), "s"(lim) : "memory");
    asm volatile("global_load_ushort %0, %1, %2" : "=v"(p_ldt) : "v"(2 * ch), "s"(ldt) : "memory");
    asm volatile("global_load_ushort %0, %1, %2" : "=v"(p_gw) : "v"(2 * (40 * rb + l39)), "s"(gw) : "memory");
  }
  sfor<0, 4>([&](auto N) { gx(IC<0>{}, N); });
  sfor<0, 5>([&](auto I) { gb(IC<0>{}, I); });
  SB();
  u32x4 cq[3];
  u32x2 ct[3];
  unsigned csrc[3], cdst[3];
  const int cj = tid & 15, crow0 = tid >> 4;
  const bool ctail = (0x4300u >> cj) & 1u, cgate = cj == 15;
  const int crow[3] = {crow0, crow0 + 16, 32 + (crow0 & 7)};
  sfor<0, 4>([&](auto N) { gx(IC<1>{}, N); });
  {
    const unsigned i = idx * 4 + w;
    u64 pc;
    unsigned clen;
    asm volatile("s_getpc_b64 %0\n.Lkf_pc:\n\ts_mov_b32 %1, .Lkf_end-.Lkf_pc" : "=s"(pc), "=s"(clen));
    const char* cp = (const char*)pc + (min(i * 384 + (lane & 3) * 128, clen - 4u) & ~3u);
    const char* tp = (lane & 1) ? (const char*)out + min(i, 79u) * 65536 : ws + WS_A + min(i, 119u) * 65536;
    const char* dp = (const char*)dskip + (idx >> 2) * 160 + (lane & 1) * 128;
    const char* wp = (const char*)wo + (16 * rb + cg) * 3200 + min((unsigned)(7 * w + (lane & 7)) * 128, 3199u);
    tp = lane >= 16 && lane < 23 ? wp : (lane >= 8 && lane < 10 ? dp : tp);
    touch(lane >= 4 && lane < 7 ? cp : tp, ldsb + L_DUMMY);
  }
  {
    const unsigned sec = (0x0009aa55u >> (2 * cj)) & 3u;
    const unsigned off4 = (unsigned)(0x0475204475207520ull >> (4 * cj)) & 15u;
    const unsigned colb = 2 * (sec * 640 + 40 * cg + 4 * off4) + 40 * rb * 3840;
    const unsigned rbytes = cj >= 10 ? 96 : 160;
    const unsigned dbase = cj >= 10 ? WS_WQ + cg * 61440 + (cj - 10) * 16 : WS_WKV + cg * 102400 + cj * 16;
#pragma unroll
    for (int it = 0; it < 3; ++it) {
      const unsigned k = 40 * rb + crow[it];
      csrc[it] = colb + crow[it] * 3840;
      cdst[it] = dbase + (k ^ (((k >> 3) & 1) << 2)) * rbytes;
    }
    asm volatile("" : "+v"(csrc[0]), "+v"(csrc[1]), "+v"(csrc[2]), "+v"(cdst[0]), "+v"(cdst[1]), "+v"(cdst[2]));
  }
  auto cload = [&](auto N) {
    constexpr int n = N, it = n >> 1;
    if constexpr (n & 1) ct[it] = ld8(wqkv, ctail ? csrc[it] + 40 : 0u);
    else cq[it] = ld16(wqkv, csrc[it]);
  };
  auto cstore = [&](auto IT) {
    constexpr int it = IT;
    u32x4 v = cq[it];
    const unsigned gwv = __shfl(p_gw, crow[it]);
    v[0] = cgate ? gwv : v[0]; v[1] = cgate ? 0u : v[1];
    v[2] = ctail ? ct[it][0] : (cgate ? 0u : v[2]); v[3] = ctail ? ct[it][1] : (cgate ? 0u : v[3]);
    store16_sc1(ws, cdst[it], v);
  };
  STAMP(1);

  f32x2 c_a, c_a8;
  float c_lmag, c_rev;
  f32x2 pa[3], pc[3], pw[3];
  f32x2 pj[4];
  asm volatile("s_waitcnt vmcnt(%5)" : "+v"(p_lre), "+v"(p_lim), "+v"(p_ldt), "+v"(p_ep), "+v"(p_gw) : "n"(13 + TCN) : "memory");
  const unsigned tag = __builtin_amdgcn_readfirstlane(p_ep) + 1u;
  {
    const float lr = bf2f(p_lre), li = bf2f(p_lim);
    const float dt = rbf(__expf(bf2f(p_ldt)));
    c_lmag = dt * lr * 1.4426950408889634f; c_rev = dt * li * 0.15915494309189535f;
    const float er = __builtin_amdgcn_exp2f(c_lmag), rev = __builtin_amdgcn_fractf(c_rev);
    c_a = f32x2{er * __builtin_amdgcn_cosf(rev), er * __builtin_amdgcn_sinf(rev)};
    const float nr = c_a.x - 1.f, ni = c_a.y, iden = 1.f / (lr * lr + li * li);
    const float cr = (nr * lr + ni * li) * iden, ci = (ni * lr - nr * li) * iden;
    c_a8 = csq(csq(csq(c_a)));
#pragma unroll
    for (int p = 0; p < 3; ++p) {
      const int src = p < 2 ? 16 * p + r : 32 + (r & 7);
      pa[p] = f32x2{__shfl(c_a.x, src), __shfl(c_a.y, src)};
      pc[p] = f32x2{2.f * __shfl(cr, src), 2.f * __shfl(ci, src)};
    }
    asm volatile("" : "+v"(pa[0]), "+v"(pa[1]), "+v"(pa[2]), "+v"(pc[0]), "+v"(pc[1]), "+v"(pc[2]), "+v"(c_a8),
                 "+v"(c_lmag), "+v"(c_rev));
  }
  STAMP(2);

  bf16x8 xa[20][2];
  f32x4 accS[5][2];
#pragma unroll
  for (int t = 0; t < 5; ++t) { accS[t][0] = f32x4{0, 0, 0, 0}; accS[t][1] = f32x4{0, 0, 0, 0}; }
  bf16x8 bq[2][5];
  LAS char* xrd[2];
  LAS char* brd[4];
#pragma unroll
  for (int kk = 0; kk < 2; ++kk) xrd[kk] = lds + A0 + (32 * w + r) * 128 + (((4 * kk + g) ^ ((r >> 1) & 7)) << 4);
#pragma unroll
  for (int kq = 0; kq < 4; ++kq) brd[kq] = lds + B0 + r * 256 + (((4 * kq + g) ^ r) << 4);
  auto rd1 = [&](auto KS, auto I) {
    constexpr int ks = KS, i = I;
    if constexpr (i < 2) xa[ks][i] = lds_frag(xrd[ks & 1] + (ASLOT[(ks >> 1) & 3] - A0) + i * 2048);
    else bq[ks & 1][i - 2] = lds_frag(brd[ks & 3] + (BSLOT[(ks >> 2) % 3] - B0) + (i - 2) * 4096);
  };
  unsigned cf = 0;
  const GAS unsigned* cfp = (const GAS unsigned*)(ws + WS_CFLAG) + cg * 16 + r;

  auto dma1 = [&](auto S, auto J) {
    constexpr int s = S, j = J;
    if constexpr (s == 0) {
      if constexpr (j < 4) gx(IC<2>{}, J); else if constexpr (j < 9) gb(IC<1>{}, IC<j - 4>{}); else if constexpr (j < 15) cload(IC<j - 9>{});
    } else if constexpr (s == 1) {
      if constexpr (j < 4) gx(IC<3>{}, J);
      else if constexpr (j < 8) gx(IC<4>{}, IC<j - 4>{});
      else if constexpr (j < 13) gb(IC<2>{}, IC<j - 8>{});
    } else if constexpr (s == 2 && j >= 100) {
      cstore(IC<j - 100>{});
    } else if constexpr (s <= 6) {
      if constexpr (j < 4) gx(IC<s + 3>{}, J);
      else if constexpr (s <= 5) {
        constexpr int be = 2 + (s >> 1), i = (s & 1) ? j - 1 : j - 4;
        if constexpr ((s & 1) ? j < 6 : j < 7) gb(IC<be>{}, IC<i>{});
      } else if constexpr (j == 4) {
        cf = ld4_sc1(ws, WS_CFLAG + (cg * 16 + r) * 4);
      }
    } else if constexpr (s == 7) {
      const u32x4 z4 = {0u, tag - 1u, 0u, tag - 1u};
      if constexpr (j < 2) store16_sc1(ws, WS_PART + (cg * 16 + rb) * 6784 + min(tid + 256 * j, 423) * 16, z4);
      else if constexpr (j == 2) {
        if (w & 1) store8_sc1(ws, WS_FIN + (cg * 848 + min(53 * rb + min(lane, 52), 847)) * 8, u32x2{0u, tag - 1u});
        else store16_sc1(ws, WS_AGG + (cg * 16 + rb) * 640 + min(lane, 39) * 16, z4);
      }
    } else if constexpr (s == 8) { if constexpr (j < 5) gkv(IC<0>{}, J); }
    else if constexpr (s == 9) { if constexpr (j < 5) gkv(IC<1>{}, J); }
  };

  SYNC(4 + TCN);
  STAMP(3);
  sfor<0, 7>([&](auto I) { rd1(IC<0>{}, I); });
  SB();
  sfor<0, 20>([&](auto KS) {
    constexpr int ks = KS, s = (ks + 1) >> 1, first = ks & 1;
    if constexpr (first && ks + 1 < 20) {
      if constexpr (s == 1) VWAIT(15 + TCN);
      if constexpr (s == 2) {
        asm volatile("s_waitcnt vmcnt(%6)" : "+v"(cq[0]), "+v"(cq[1]), "+v"(cq[2]), "+v"(ct[0]), "+v"(ct[1]), "+v"(ct[2]) : "n"(13) : "memory");
        SYNC(13);
        sfor<0, 3>([&](auto IT) { dma1(IC<2>{}, IC<100 + IT>{}); });
        STAMP(4);
      }
      if constexpr (s == 3) VWAIT(19);
      if constexpr (s == 4) {
        SYNC(13);
        if (tid == 0) __hip_atomic_store((GAS unsigned*)(ws + WS_CFLAG) + cg * 16 + rb, tag, __ATOMIC_RELAXED, __HIP_MEMORY_SCOPE_AGENT);
      }
      if constexpr (s == 5) { VWAIT(16); STAMP(5); }
      if constexpr (s == 6) SYNC(13);
      if constexpr (s == 7) VWAIT(14);
      if constexpr (s == 8) {
        asm volatile("s_waitcnt vmcnt(3)" : "+v"(cf)::"memory");
        SYNC(3);
        if (!__all(cf == tag)) {
          unsigned spins = 0;
          for (;;) {
            cf = __hip_atomic_load(cfp, __ATOMIC_RELAXED, __HIP_MEMORY_SCOPE_AGENT);
            if (__all(cf == tag)) break;
            if (spin_fail(spins, tmo, tag)) break;
          }
        }
        STAMP(6);
      }
    }
    { bf16x8 t0 = xa[ks][0], t1 = xa[ks][1]; asm("" : "+a"(t0), "+a"(t1)); xa[ks][0] = t0; xa[ks][1] = t1; }
    sfor<0, 10>([&](auto I) {
      constexpr int i = I;
      accS[i >> 1][i & 1] = mfma(xa[ks][i & 1], bq[ks & 1][i >> 1], accS[i >> 1][i & 1]);
      if constexpr (ks + 1 < 20 && i < 7) rd1(IC<ks + 1>{}, I);
      if constexpr (ks == 0) { if constexpr (i < 9) dma1(IC<0>{}, I); else sfor<9, 15>([&](auto J) { dma1(IC<0>{}, J); }); }
      else if constexpr (ks < 19) {
        constexpr int gi = first ? i : 10 + i;
        if constexpr (s == 1) { if constexpr (gi < 13) dma1(IC<1>{}, IC<gi>{}); }
        else if constexpr (s == 8 || s == 9) { if constexpr (gi % 4 == 1) dma1(IC<s>{}, IC<gi / 4>{}); }
        else if constexpr (gi % 3 == 1) dma1(IC<s>{}, IC<gi / 3>{});
      }
      SB();
    });
  });
  STAMP(7);

  f32x2 ub[3][8];
  {
#pragma unroll
    for (int t = 0; t < 8; ++t) {
      ub[0][t] = f32x2{accS[0][t >> 2][t & 3], accS[1][t >> 2][t & 3]};
      ub[1][t] = f32x2{accS[2][t >> 2][t & 3], accS[3][t >> 2][t & 3]};
      const float v = accS[4][t >> 2][t & 3];
      const float o = __builtin_amdgcn_update_dpp(0.f, v, 0x128, 0xf, 0xf, false);
      ub[2][t] = r < 8 ? f32x2{v, o} : f32x2{o, v};
    }
    f32x2 e[3] = {ub[0][0], ub[1][0], ub[2][0]};
    SB();
#pragma unroll
    for (int t = 1; t < 8; ++t) {
      f32x2 t1[3];
#pragma unroll
      for (int p = 0; p < 3; ++p) t1[p] = cfma_1(pa[p], e[p], ub[p][t]);
      SB();
#pragma unroll
      for (int p = 0; p < 3; ++p) e[p] = cfma_2(pa[p], e[p], t1[p]);
      SB();
    }
    LAS char* ep = lds + L_E + ((4 * w + g) * 40 + r) * 8;
    *(LAS f32x2*)ep = e[0];
    *(LAS f32x2*)(ep + 128) = e[1];
    if (r < 8) *(LAS f32x2*)(ep + 256) = e[2];
  }
  STAMP(8);

  f32x4 accK[5][2];
#pragma unroll
  for (int t = 0; t < 5; ++t) { accK[t][0] = f32x4{0, 0, 0, 0}; accK[t][1] = f32x4{0, 0, 0, 0}; }
  const int q4 = r >> 2, pp = r & 3;
  LAS char* kv0 = lds + (8 * g + q4) * 160 + 8 * pp + 640 * (g & 1);
  LAS char* kv1 = lds + (8 * g + q4) * 160 + 8 * pp + 640 * (1 - (g & 1));
  bf16x4 bh[2][5][2];
  LAS char* kb[2][2];
  auto base2 = [&](auto J) {
    constexpr int j = J;
    kb[j & 1][0] = kv0 + KVSLOT[j]; kb[j & 1][1] = kv1 + KVSLOT[j];
    asm volatile("" : "+v"(kb[j & 1][0]), "+v"(kb[j & 1][1]));
  };
  auto rd2 = [&](auto KS, auto I) {
    constexpr int ks = KS, i = I;
    constexpr unsigned o = (ks & 3) * 5120 + 32 * (i >> 1);
    bh[ks & 1][i >> 1][i & 1] = __builtin_amdgcn_ds_read_tr16_b64_v4i16((LAS bf16x4*)(kb[(ks >> 2) & 1][i & 1] + o));
  };
  auto cat = [](bf16x4 a, bf16x4 b) { return __builtin_shufflevector(a, b, 0, 1, 2, 3, 4, 5, 6, 7); };
  const unsigned t2row = (80 * (idx >> 2) + 20 * (idx & 3) + 10 * (w >> 1)) * 1280;
  const unsigned t2off0 = t2row + (min(lane, 39) >> 2) * 1280 + (lane & 3) * 128;
  const unsigned t2l = min(lane, 59), t2q = (t2l * 10923) >> 16;
  const unsigned t2off1 = t2row + t2q * 1280 + (4 + t2l - 6 * t2q) * 128;
  const char* t2p = uptr((w & 1) ? cim : cre);
  u32x4 cw;
  unsigned cwsrc, cwdst;
  {
    const unsigned sc = 200 * (16 * rb + cg) + min(tid, 199);
    cwsrc = sc * 16;
    const unsigned k = (sc * 52429u) >> 22, cc = sc - 80 * k, jw = (cc * 6554) >> 16, ch = cc - 10 * jw;
    const unsigned cgq = (k * 1639) >> 16, rem = k - 40 * cgq, hh = rem >= 20, e = rem - 20 * hh;
    const unsigned j = 4 * hh + (e & 3), qq = 5 * cgq + (e >> 2), oc = qq & 3;
    cwdst = WS_WO + ((jw * 20 + (qq >> 2)) * 32 + 8 * oc + (j ^ ((oc & 1) << 2))) * 160 + ch * 16;
  }
  auto dma2 = [&](auto J, auto N) {
    constexpr int j = J, n = N;
    if constexpr (j == 0) { if constexpr (n < 10) gkv(IC<2 + n / 5>{}, IC<n % 5>{}); else if constexpr (n == 10) cw = ld16(wo, cwsrc); }
    else if constexpr (j == 1) { if constexpr (n < 5) gkv(IC<4>{}, N); }
    else if constexpr (j == 2) { if constexpr (n < 6) gq(IC<n / 3>{}, IC<n % 3>{}); }
    else if constexpr (j == 3) { if constexpr (n < 6) gq(IC<2 + n / 3>{}, IC<n % 3>{}); else if constexpr (n == 6) store16_sc1(ws, cwdst, cw); }
    else { if constexpr (n < 3) gq(IC<4>{}, N); else if constexpr (n < 3 + T2N) touch_s(t2p, n == 3 ? t2off0 : t2off1, ldsb + L_DUMMY); }
  };
  base2(IC<0>{});
  SYNC(5);
  STAMP(9);
  sfor<0, 10>([&](auto I) { rd2(IC<0>{}, I); });
  SB();
  sfor<0, 20>([&](auto KS) {
    constexpr int ks = KS, j = (ks + 1) >> 2, ph = (ks + 1) & 3;
    if constexpr (ph == 0 && ks + 1 < 20) {
      base2(IC<j>{});
      if constexpr (j == 1) SYNC(11);
      if constexpr (j == 2) SYNC(11);
      if constexpr (j == 3) { asm volatile("s_waitcnt vmcnt(11)" : "+v"(cw)::"memory"); SYNC(11); }
      if constexpr (j == 4) SYNC(13);
    }
    sfor<0, 10>([&](auto I) {
      constexpr int i = I;
      accK[i >> 1][i & 1] = mfma(xa[ks][i & 1], cat(bh[ks & 1][i >> 1][0], bh[ks & 1][i >> 1][1]), accK[i >> 1][i & 1]);
      if constexpr (ks + 1 < 20) rd2(IC<ks + 1>{}, I);
      if constexpr (j == 0) { if constexpr (i % 3 == 1 || i == 9) dma2(IC<0>{}, IC<4 * ks + (i == 9 ? 3 : i / 3)>{}); }
      else if constexpr (ph < 3 && ks < 19 && (i == 1 || i == 4 || i == 7)) dma2(IC<j>{}, IC<3 * ph + (i - 1) / 3>{});
      SB();
    });
  });
  STAMP(10);

  {
    bf16x8 kf[5];
#pragma unroll
    for (int t = 0; t < 5; ++t) {
      unsigned u[4];
#pragma unroll
      for (int h2 = 0; h2 < 4; ++h2) {
        const float a = accK[t][h2 >> 1][(h2 & 1) * 2], b = accK[t][h2 >> 1][(h2 & 1) * 2 + 1];
        if (t == 2 || t == 3) u[h2] = pk2(a, b);
        else if (t < 2) u[h2] = phi2(a, b);
        else { const unsigned ph = phi2(a, b), raw = pk2(a, b); u[h2] = r < 8 ? ph : raw; }
      }
      kf[t] = frag4(u[0], u[1], u[2], u[3]);
      *(LAS bf16x8*)(lds + A3 + (w * 5 + t) * 1024 + lane * 16) = kf[t];
    }
    SYNC(T2N);
    const bf16x8 ones = frag4(0x3f803f80u, 0x3f803f80u, 0x3f803f80u, 0x3f803f80u);
    const char* part = sptr(ws, WS_PART + (cg * 16 + rb) * 848 * 8);
    LAS char* st = lds + A3 + lane * 16;
    auto job = [&](LAS char* pa_, LAS char* pb_, bool onesb) {
      f32x4 d = {0, 0, 0, 0};
#pragma unroll
      for (int sw = 0; sw < 4; ++sw) {
        const bf16x8 a = lds_frag(pa_ + sw * 5120);
        bf16x8 b = lds_frag(pb_ + sw * 5120);
        if (onesb && r < 8) b = ones;
        d = mfma(a, b, d);
      }
      return d;
    };
    if (w < 2) {
      const int h = w;
      const f32x4 d0 = job(st + h * 1024, st + (2 + h) * 1024, false);
      const f32x4 d1 = job(st + 4096, st + (2 + h) * 1024, false);
      const unsigned o0 = (420 * h + 80 * g + r) * 8, o1 = (420 * h + 320 + r) * 8;
      sfor<0, 4>([&](auto I) { constexpr int i = I; gstore<160 * i>(part, o0, d0[i], tag); });
      if (g == h) sfor<0, 4>([&](auto I) { constexpr int i = I; gstore<160 * i>(part, o1, d1[i], tag); });
    } else if (w == 2) {
      const f32x4 d0 = job(st, st + 4096, true);
      const f32x4 d1 = job(st + 1024, st + 4096, true);
      const f32x4 d2 = job(st + 4096, st + 4096, true);
      const bool k0 = (r >> 2) == 2, k1 = (r >> 2) == 3, kq = (r >> 2) == 2 + g;
      const unsigned o0 = (k0 ? 80 * g + 16 + (r & 3) : 400 + 4 * g) * 8, st0 = k0 ? 160 : 8;
      const unsigned o1 = (420 + (k1 ? 80 * g + 16 + (r & 3) : 400 + 4 * g)) * 8, st1 = k1 ? 160 : 8;
      const unsigned o2 = (420 * g + (kq ? 336 + (r & 3) : 416)) * 8, st2 = kq ? 160 : 8;
      if (k0 || r == 0) sfor<0, 4>([&](auto I) { constexpr int i = I; gstore<0>(part, o0 + i * st0, d0[i], tag); });
      if (k1 || r == 0) sfor<0, 4>([&](auto I) { constexpr int i = I; gstore<0>(part, o1 + i * st1, d1[i], tag); });
      if (g < 2 && (kq || r == 0)) sfor<0, 4>([&](auto I) { constexpr int i = I; gstore<0>(part, o2 + i * st2, d2[i], tag); });
    } else {
      if (lane < 40) {
        f32x2 e[16];
#pragma unroll
        for (int s = 0; s < 16; ++s) e[s] = *(LAS f32x2*)(lds + L_E + (s * 40 + lane) * 8);
        f32x2 run = {0.f, 0.f};
#pragma unroll
        for (int s = 0; s < 16; ++s) {
          *(LAS f32x2*)(lds + L_E + (s * 40 + lane) * 8) = run;
          run = cfma(c_a8, run, e[s]);
        }
        const char* ag = sptr(ws, WS_AGG + (cg * 16 + rb) * 640);
        gstore<0>(ag, lane * 16, run.x, tag); gstore<8>(ag, lane * 16, run.y, tag);
      }
    }
  }
  STAMP(11);

  const int T = 4 * xcd + (idx & 3), jt = idx >> 2;
  constexpr unsigned UNIT = 10240, R1 = B0 + 25600;
  const unsigned dA = ldsb + A0 + w * 2 * UNIT, dB = ldsb + R1 + w * UNIT;
  const char* cb = uptr((const char*)((w & 1) ? cim : cre) + (w >> 1) * 128);
  unsigned coff[2];
  {
    const int l3 = lane >> 3;
    coff[0] = (80 * jt + l3) * 1280 + (((lane & 7) ^ (l3 >> 1)) << 4);
    coff[1] = coff[0] ^ 64;
  }
  auto gc = [&](auto M, auto N) {
    constexpr int m = M, n = N;
    if constexpr (m == 2) glds<0, n * 1024, m * 256>(cb + n * 10240, coff[n & 1], dB);
    else glds<0, (m == 1 || m == 4 ? UNIT : 0) + n * 1024, m * 256>(cb + n * 10240, coff[n & 1], dA);
  };

  f32x4 accQ[3][2];
#pragma unroll
  for (int t = 0; t < 3; ++t) { accQ[t][0] = f32x4{0, 0, 0, 0}; accQ[t][1] = f32x4{0, 0, 0, 0}; }
  LAS char* q0 = lds + (8 * g + q4) * 96 + 8 * pp + 384 * (g & 1);
  LAS char* q1 = lds + (8 * g + q4) * 96 + 8 * pp + 384 * (1 - (g & 1));
  auto base3 = [&](auto M) {
    constexpr int m = M;
    kb[m & 1][0] = q0 + QSLOT[m]; kb[m & 1][1] = q1 + QSLOT[m];
    asm volatile("" : "+v"(kb[m & 1][0]), "+v"(kb[m & 1][1]));
  };
  auto rd3 = [&](auto KS, auto I) {
    constexpr int ks = KS, i = I;
    constexpr unsigned o = (ks & 3) * 3072 + 32 * (i >> 1);
    bh[ks & 1][i >> 1][i & 1] = __builtin_amdgcn_ds_read_tr16_b64_v4i16((LAS bf16x4*)(kb[(ks >> 2) & 1][i & 1] + o));
  };
  const int i1 = 53 * rb + lane;
  const bool act1 = lane < 53 && i1 < 840;
  const unsigned x1off = (act1 ? i1 : 0) * 8, x2off = min(lane, 39) * 16;
  const char* x1src = sptr(ws, WS_PART + (cg * 16 + 4 * w) * 6784);
  const char* x2src = sptr(ws, WS_AGG + cg * 16 * 640);
  u32x2 xh[4];
  u32x4 xl[4];
  auto xload = [&](auto N) {
    constexpr int n = N;
    if constexpr (n < 4) xh[n] = ld8_sc1(x1src + n * 6784, x1off);
    else { const int j = w + 4 * (n - 4); xl[n - 4] = ld16u_sc1(x2src + (j < rb ? j : 0) * 640, x2off); }
  };
  base3(IC<0>{});
  SB();
  sfor<0, 6>([&](auto I) { rd3(IC<0>{}, I); });
  SB();
  sfor<0, 20>([&](auto KS) {
    constexpr int ks = KS;
    if constexpr (((ks + 1) & 3) == 0 && ks + 1 < 20) { base3(IC<(ks + 1) / 4>{}); SB(); }
    if constexpr (ks == 1) { asm volatile("s_barrier" ::: "memory"); SB(); }
    sfor<0, 6>([&](auto I) {
      constexpr int i = I;
      accQ[i >> 1][i & 1] = mfma(cat(bh[ks & 1][i >> 1][0], bh[ks & 1][i >> 1][1]), xa[ks][i & 1], accQ[i >> 1][i & 1]);
      if constexpr (ks + 1 < 20) rd3(IC<ks + 1>{}, I);
      if constexpr (ks >= 2 && ks < 12 && (i == 1 || i == 4)) gc(IC<(ks - 2) / 5>{}, IC<2 * ((ks - 2) % 5) + (i == 4)>{});
      if constexpr (ks >= 14 && ks < 18 && (i == 1 || i == 4)) xload(IC<2 * (ks - 14) + (i == 4)>{});
      SB();
    });
  });
  STAMP(12);

  float gv[2];
#pragma unroll
  for (int mt = 0; mt < 2; ++mt) {
    const float s = rbf(accQ[2][mt][0]);
    const float gg = rbf(__builtin_amdgcn_rcpf(1.f + __expf(-s)));
    const int tl = 32 * w + 8 * (r >> 2) + 4 * mt + (r & 3);
    if (g == 2) {
      *(LAS float*)(lds + L_G + tl * 4) = gg;
      if (cg == 0) store4_sc1(ws, WS_G + (128 * rb + tl) * 4, __float_as_uint(gg));
    }
    gv[mt] = __shfl(gg, 32 + r);
  }
  {
    auto cpow = [](float lmag, float rev, float n) {
      const float m = __builtin_amdgcn_exp2f(n * lmag), f = __builtin_amdgcn_fractf(n * rev);
      return f32x2{m * __builtin_amdgcn_cosf(f), m * __builtin_amdgcn_sinf(f)};
    };
    const float nseg = (float)(8 * (4 * w + g));
#pragma unroll
    for (int p = 0; p < 3; ++p) {
      const int src = p < 2 ? 16 * p + r : 32 + (r & 7);
      pw[p] = cpow(__shfl(c_lmag, src), __shfl(c_rev, src), nseg);
    }
#pragma unroll
    for (int i = 0; i < 4; ++i) pj[i] = cpow(c_lmag, c_rev, (float)(128 * max(rb - 1 - w - 4 * i, 0)));
    asm volatile("" : "+v"(pw[0]), "+v"(pw[1]), "+v"(pw[2]), "+v"(pj[0]), "+v"(pj[1]), "+v"(pj[2]), "+v"(pj[3]), "+v"(gv[0]), "+v"(gv[1]));
  }
  STAMP(13);

  asm volatile("s_waitcnt vmcnt(0)"
               : "+v"(xh[0]), "+v"(xh[1]), "+v"(xh[2]), "+v"(xh[3]), "+v"(xl[0]), "+v"(xl[1]), "+v"(xl[2]), "+v"(xl[3])::"memory");
  {
    unsigned spins = 0;
    for (;;) {
      bool ok = true;
#pragma unroll
      for (int n = 0; n < 4; ++n) {
        ok &= xh[n][1] == tag || !act1;
        ok &= (xl[n][1] == tag && xl[n][3] == tag) || w + 4 * n >= rb;
      }
      if (__all(ok)) break;
      if (spin_fail(spins, tmo, tag)) break;
#pragma unroll
      for (int n = 0; n < 4; ++n) {
        const int j = w + 4 * n;
        xh[n] = ld8_wait(x1src + n * 6784, x1off);
        xl[n] = ld16_wait(x2src + (j < rb ? j : 0) * 640, x2off);
      }
    }
    *(LAS float*)(lds + L_H + tid * 4) =
        (__uint_as_float(xh[0][0]) + __uint_as_float(xh[1][0])) + (__uint_as_float(xh[2][0]) + __uint_as_float(xh[3][0]));
    f32x2 c = {0.f, 0.f};
#pragma unroll
    for (int n = 0; n < 4; ++n)
      if (w + 4 * n < rb) c = cfma(pj[n], f32x2{__uint_as_float(xl[n][0]), __uint_as_float(xl[n][2])}, c);
    if (lane < 40) *(LAS f32x2*)(lds + L_C + (w * 40 + lane) * 8) = c;
  }
  LAS char* crA[2];
  LAS char* crB[2];
#pragma unroll
  for (int kk = 0; kk < 2; ++kk) {
    const int lp = r * 128 + (((4 * kk + g) ^ ((r >> 1) & 7)) << 4);
    crA[kk] = lds + A0 + w * 2 * UNIT + lp; crB[kk] = lds + R1 + w * UNIT + lp;
  }
  asm volatile("" : "+v"(crA[0]), "+v"(crA[1]), "+v"(crB[0]), "+v"(crB[1]));
  bf16x8 bu[4][5];
#pragma unroll
  for (int kp = 0; kp < 4; ++kp)
#pragma unroll
    for (int nt = 0; nt < 5; ++nt) bu[kp][nt] = lds_frag(crA[kp & 1] + (kp >> 1) * UNIT + nt * 2048);
  STAMP(14);
  LSYNC();
  STAMP(15);
  if (w == 0) {
    const float s = (*(LAS float*)(lds + L_H + lane * 4) + *(LAS float*)(lds + L_H + 256 + lane * 4)) +
                    (*(LAS float*)(lds + L_H + 512 + lane * 4) + *(LAS float*)(lds + L_H + 768 + lane * 4));
    if (act1) gstore<0>(sptr(ws, WS_FIN + cg * 848 * 8), i1 * 8, s, tag);
  }

  u32x2 fv[4];
  const unsigned foff[4] = {(unsigned)tid * 8, (unsigned)(tid + 256) * 8, (unsigned)(tid + 512) * 8, (unsigned)(tid < 72 ? tid + 768 : 0) * 8};
  const char* fsrc = sptr(ws, WS_FIN + cg * 848 * 8);
  {
    const f32x4 ga = *(LAS f32x4*)(lds + L_G + (32 * w + 8 * g) * 4), gb4 = *(LAS f32x4*)(lds + L_G + (32 * w + 8 * g + 4) * 4);
    LAS char* img = lds + B0 + w * 6400;
    f32x2 h[3];
#pragma unroll
    for (int p = 0; p < 3; ++p) {
      const int chl = p < 2 ? 16 * p + r : 32 + (r & 7);
      LAS char* cp = lds + L_C + chl * 8;
      const f32x2 cin = (*(LAS f32x2*)cp + *(LAS f32x2*)(cp + 320)) + (*(LAS f32x2*)(cp + 640) + *(LAS f32x2*)(cp + 960));
      h[p] = cfma(pw[p], cin, *(LAS f32x2*)(lds + L_E + ((4 * w + g) * 40 + chl) * 8));
    }
    SB();
    sfor<0, 8>([&](auto TT) {
      constexpr int t = TT;
      f32x2 t1[3];
#pragma unroll
      for (int p = 0; p < 3; ++p) t1[p] = cfma_1(pa[p], h[p], ub[p][t]);
      SB();
#pragma unroll
      for (int p = 0; p < 3; ++p) h[p] = cfma_2(pa[p], h[p], t1[p]);
      SB();
#pragma unroll
      for (int p = 0; p < 3; ++p) t1[p] = cco_1(pc[p], h[p]);
      SB();
#pragma unroll
      for (int p = 0; p < 3; ++p) t1[p] = cco_2(pc[p], h[p], t1[p]);
      SB();
#pragma unroll
      for (int p = 0; p < 3; ++p) ub[p][t] = t1[p] * (t < 4 ? ga[t & 3] : gb4[t & 3]);
      gc(IC<2>{}, TT);
      SB();
    });
#pragma unroll
    for (int p = 0; p < 3; ++p) {
#pragma unroll
      for (int mt = 0; mt < 2; ++mt) {
        const f32x2* y = &ub[p][4 * mt];
        const u32x2 vr = {pk2(y[0].x, y[1].x), pk2(y[2].x, y[3].x)};
        const u32x2 vi = {pk2(y[0].y, y[1].y), pk2(y[2].y, y[3].y)};
        if (p < 2) {
          *(LAS u32x2*)(img + mt * 3200 + (16 * p + r) * 40 + 8 * g) = vr;
          *(LAS u32x2*)(img + mt * 3200 + (40 + 16 * p + r) * 40 + 8 * g) = vi;
        } else {
          const int row = r < 8 ? 32 + r : 72 + (r - 8);
          *(LAS u32x2*)(img + mt * 3200 + row * 40 + 8 * g) = r < 8 ? vr : vi;
        }
      }
    }
    gc(IC<2>{}, IC<8>{}); gc(IC<2>{}, IC<9>{});
    asm volatile("s_waitcnt lgkmcnt(0)" ::: "memory");
#pragma unroll
    for (int f = 0; f < 5; ++f) {
      const int mt = f < 4 ? (f >> 1) : (g >> 1);
      const int o8 = f < 4 ? 4 * (f & 1) + g : 8 + (g & 1);
      LAS char* p0 = img + mt * 3200 + (8 * o8 + q4) * 40 + 8 * pp;
      const bf16x8 v = lds_frag_tr(p0, p0 + 160);
      const int part = o8 >= 5, o = 5 * cg + (part ? o8 - 5 : o8);
      const int q = 16 * (o >> 3) + 8 * part + (o & 7);
      const int rt = 8 * rb + 2 * w + mt;
      store16_sc1(ws, WS_A + (rt * 240 + q) * 256 + r * 16, __builtin_bit_cast(u32x4, v));
    }
  }
  STAMP(16);

#pragma unroll
  for (int it = 0; it < 4; ++it) fv[it] = ld8_sc1(fsrc, foff[it]);
  bf16x8 qf[2][2];
#pragma unroll
  for (int mt = 0; mt < 2; ++mt) {
    unsigned tl[2];
#pragma unroll
    for (int h2 = 0; h2 < 2; ++h2) {
      const unsigned ph = phi2(accQ[2][mt][2 * h2], accQ[2][mt][2 * h2 + 1]);
      tl[h2] = g < 2 ? ph : 0u;
    }
#pragma unroll
    for (int h = 0; h < 2; ++h)
      qf[h][mt] = frag4(phi2(accQ[h][mt][0], accQ[h][mt][1]), phi2(accQ[h][mt][2], accQ[h][mt][3]), tl[0], tl[1]);
  }
  asm volatile("" : "+v"(qf[0][0]), "+v"(qf[0][1]), "+v"(qf[1][0]), "+v"(qf[1][1]));
  unsigned kdst[4];
#pragma unroll
  for (int it = 0; it < 4; ++it) {
    const int i = tid + 256 * it, h = i >= 420, ii = i - 420 * h;
    const int dd = (ii * 3277) >> 16;
    const int e = ii < 400 ? ii - 20 * dd : 20, d = ii < 400 ? dd : ii - 400;
    kdst[it] = L_KVT + (h * 21 + e) * 40 + d * 2;
  }
  asm volatile("" : "+v"(kdst[0]), "+v"(kdst[1]), "+v"(kdst[2]), "+v"(kdst[3]));
  asm volatile("s_waitcnt vmcnt(0)\n\ts_barrier" : "+v"(fv[0]), "+v"(fv[1]), "+v"(fv[2]), "+v"(fv[3])::"memory");
  if (tid == 0) __hip_atomic_store((GAS unsigned*)(ws + WS_HFLAG) + rb * 16 + cg, tag, __ATOMIC_RELAXED, __HIP_MEMORY_SCOPE_AGENT);
  {
    unsigned spins = 0;
    for (;;) {
      const bool ok = fv[0][1] == tag && fv[1][1] == tag && fv[2][1] == tag && fv[3][1] == tag;
      if (__all(ok)) break;
      if (spin_fail(spins, tmo, tag)) break;
#pragma unroll
      for (int it = 0; it < 4; ++it) fv[it] = ld8_wait(fsrc, foff[it]);
    }
#pragma unroll
    for (int it = 0; it < 4; ++it) {
      const float f = __uint_as_float(fv[it][0]);
      if (it < 3 || tid < 72) *(LAS u16*)(lds + kdst[it]) = (u16)pk2(f, f);
    }
  }
  STAMP(17);
  LSYNC();

  {
    f32x4 num[2][2], nt[2], den[2][2];
#pragma unroll
    for (int mt = 0; mt < 2; ++mt) nt[mt] = f32x4{0, 0, 0, 0};
    sfor<0, 2>([&](auto H) {
      constexpr int h = H;
      LAS char* kbp = lds + L_KVT + h * 840;
      const u32x2 z2 = {0u, 0u};
      const u32x2 ml = *(LAS u32x2*)(kbp + r * 40 + 8 * g), mh = *(LAS u32x2*)(kbp + r * 40 + 32);
      const u32x2 tl = *(LAS u32x2*)(kbp + (16 + (r & 3)) * 40 + 8 * g), th = *(LAS u32x2*)(kbp + (16 + (r & 3)) * 40 + 32);
      const u32x2 dl = *(LAS u32x2*)(kbp + 800 + 8 * g), dh = *(LAS u32x2*)(kbp + 800 + 32);
      const bool gh = g == h, rv = (r >> 2) == h;
      const u32x2 mh2 = gh ? mh : z2, dh2 = gh ? dh : z2, tl2 = rv ? tl : z2, th2 = (rv && gh) ? th : z2;
      const bf16x8 am = frag4(ml[0], ml[1], mh2[0], mh2[1]);
      const bf16x8 at = frag4(tl2[0], tl2[1], th2[0], th2[1]);
      const bf16x8 ad = frag4(dl[0], dl[1], dh2[0], dh2[1]);
#pragma unroll
      for (int mt = 0; mt < 2; ++mt) {
        num[h][mt] = mfma(am, qf[h][mt], f32x4{0, 0, 0, 0});
        nt[mt] = mfma(at, qf[h][mt], nt[mt]);
        den[h][mt] = mfma(ad, qf[h][mt], f32x4{0, 0, 0, 0});
      }
    });
    sfor<0, 2>([&](auto MT) {
      constexpr int mt = MT;
      const float og = rbf(1.f - gv[mt]);
      float sc[2];
#pragma unroll
      for (int h = 0; h < 2; ++h) sc[h] = og * __builtin_amdgcn_rcpf(den[h][mt][0] + 1e-6f);
      const int rt = 8 * rb + 2 * w + mt;
      const u32x4 ov = {pk2(num[0][mt][0] * sc[0], num[0][mt][1] * sc[0]), pk2(num[0][mt][2] * sc[0], num[0][mt][3] * sc[0]),
                        pk2(num[1][mt][0] * sc[1], num[1][mt][1] * sc[1]), pk2(num[1][mt][2] * sc[1], num[1][mt][3] * sc[1])};
      store16_sc1(ws, WS_A + (rt * 240 + 160 + 5 * cg + g) * 256 + r * 16, ov);
      const float st = g == 0 ? sc[0] : sc[1];
      const u32x2 tv = {pk2(nt[mt][0] * st, nt[mt][1] * st), pk2(nt[mt][2] * st, nt[mt][3] * st)};
      if (g < 2) store8_sc1(ws, WS_A + (rt * 240 + 160 + 5 * cg + 4) * 256 + r * 16 + 8 * g, tv);
    });
  }
  STAMP(18);

  const unsigned hoff = WS_HFLAG + ((T >> 1) * 16 + r) * 4;
  unsigned hf = ld4_sc1(ws, hoff);
  u32x2 exv[5], edv[5];
  unsigned egv[5], eoff[5], elds[5], goff[5];
#pragma unroll
  for (int it = 0; it < 5; ++it) {
    const int item = tid + 256 * it, tp = (item * 3277) >> 16, dq = item - 20 * tp;
    const int mt = tp >> 4, rr = tp & 15;
    const int t = 64 * T + 32 * (mt >> 1) + 8 * (rr >> 2) + 4 * (mt & 1) + (rr & 3);
    const int d = 80 * jt + 4 * dq;
    eoff[it] = (t * 640 + d) * 4; elds[it] = (tp * 84 + 4 * dq) * 4;
    exv[it] = ld8(x, eoff[it] >> 1);
    edv[it] = ld8(dskip, d * 2);
    goff[it] = WS_G + t * 4;
  }
  asm volatile("s_waitcnt vmcnt(10)" : "+v"(hf)::"memory");
  if (!__all(hf == tag)) {
    unsigned spins = 0;
#pragma clang loop unroll(disable)
    for (;;) {
      hf = ld4_wait(ws, hoff);
      if (__all(hf == tag)) break;
      if (spin_fail(spins, tmo, tag)) break;
    }
  }
  STAMP(19);

  const char* abase = sptr(ws, WS_A + 4 * T * 240 * 256);
  const unsigned alane = lane * 16;
  bf16x8 a[4][4];
  auto ga = [&](auto KP) {
    constexpr int kp = KP;
    const unsigned q = kp < 10 ? 32 * (kp >> 1) + 16 * (w >> 1) + 8 * (w & 1) + 4 * (kp & 1) : 160 + 16 * (kp - 10) + 4 * w;
    const char* p = abase + q * 256;
    sfor<0, 4>([&](auto MT) {
      constexpr int mt = MT;
      a[kp & 3][mt] = ld16_sc1(p + mt * 240 * 256, alane);
    });
  };
  ga(IC<0>{}); ga(IC<1>{}); ga(IC<2>{});
#pragma unroll
  for (int it = 0; it < 5; ++it) egv[it] = ld4_sc1(ws, goff[it]);
  sfor<0, 10>([&](auto N) { gc(IC<3>{}, N); });
  asm volatile("s_barrier" ::: "memory");
  if (tid == 0) {
    __hip_atomic_store((GAS unsigned*)(ws + WS_OFLAG) + rb * 16 + cg, tag, __ATOMIC_RELAXED, __HIP_MEMORY_SCOPE_AGENT);
    if (blockIdx.x == 0) __hip_atomic_store((GAS unsigned*)(ws + WS_EPOCH), tag, __ATOMIC_RELAXED, __HIP_MEMORY_SCOPE_AGENT);
  }
  asm volatile("" : "+v"(eoff[0]), "+v"(eoff[1]), "+v"(eoff[2]), "+v"(eoff[3]), "+v"(eoff[4]), "+v"(elds[0]), "+v"(elds[1]), "+v"(elds[2]),
               "+v"(elds[3]), "+v"(elds[4]));
  SB();
  STAMP(20);

  const char* wsrc = sptr(ws, WS_WO + (jt * 20 + w) * 5120 + 2048);
  auto gw_ = [&](auto M, auto N) {
    constexpr int m = M, n = N;
    if constexpr (m < 2) glds<1, m * 5120 + n * 1024, n * 1024 - 2048>(wsrc + m * 4 * 5120, lin, dB);
    else glds<1, (m - 2) * 5120 + n * 1024, n * 1024 - 2048>(wsrc + m * 4 * 5120, lin, dA);
  };
  const int wlp0 = (8 * g + q4 + 4 * (g & 1)) * 160 + 8 * pp, wlp1 = (8 * g + q4 + 4 * (1 - (g & 1))) * 160 + 8 * pp;
  LAS char* wrA[2] = {lds + A0 + w * 2 * UNIT + wlp0, lds + A0 + w * 2 * UNIT + wlp1};
  LAS char* wrB[2] = {lds + R1 + w * UNIT + wlp0, lds + R1 + w * UNIT + wlp1};
  asm volatile("" : "+v"(wrA[0]), "+v"(wrA[1]), "+v"(wrB[0]), "+v"(wrB[1]));
  bf16x8 bf[2][5];
  auto rd = [&](auto KP, auto I) {
    constexpr int kp = KP, i = I;
    if constexpr (kp < 4) {
    } else if constexpr (kp < 10) {
      if constexpr (i < 5) bf[kp & 1][i] = lds_frag(kp < 6 ? crB[kp & 1] + i * 2048 : crA[kp & 1] + (kp < 8 ? 0 : UNIT) + i * 2048);
    } else {
      constexpr int m = kp - 10;
      bh[kp & 1][i >> 1][i & 1] = __builtin_amdgcn_ds_read_tr16_b64_v4i16(
          (LAS bf16x4*)(m < 2 ? wrB[i & 1] + m * 5120 + 32 * (i >> 1) : wrA[i & 1] + (m - 2) * 5120 + 32 * (i >> 1)));
    }
  };
  auto bfrag = [&](auto KP, auto NT) {
    constexpr int kp = KP, nt = NT;
    if constexpr (kp < 4) return bu[kp][nt];
    else if constexpr (kp < 10) return bf[kp & 1][nt];
    else return (bf16x8)__builtin_shufflevector(bh[kp & 1][nt][0], bh[kp & 1][nt][1], 0, 1, 2, 3, 4, 5, 6, 7);
  };
  f32x4 acc[4][5];
#pragma unroll
  for (int mt = 0; mt < 4; ++mt)
#pragma unroll
    for (int nt = 0; nt < 5; ++nt) acc[mt][nt] = f32x4{0, 0, 0, 0};
  unsigned of = 0;
  const unsigned ooff = WS_OFLAG + ((T >> 1) * 16 + r) * 4;
#define AWAITN(N, S) do { asm volatile("s_waitcnt vmcnt(%4) lgkmcnt(0)" : "+v"(a[S][0]), "+v"(a[S][1]), "+v"(a[S][2]), "+v"(a[S][3]) : "n"(N) : "memory"); SB(); } while (0)
#define AWAIT(N, S) do { asm volatile("s_waitcnt vmcnt(" #N ") lgkmcnt(0)" : "+v"(a[S][0]), "+v"(a[S][1]), "+v"(a[S][2]), "+v"(a[S][3])::"memory"); SB(); } while (0)
  sfor<0, 15>([&](auto KP) {
    constexpr int kp = KP, sl = kp & 3;
    if constexpr (kp == 0) {
      AWAIT(23, sl);
      asm volatile("" : "+v"(exv[0]), "+v"(exv[1]), "+v"(exv[2]), "+v"(exv[3]), "+v"(exv[4]), "+v"(edv[0]), "+v"(edv[1]), "+v"(edv[2]),
                   "+v"(edv[3]), "+v"(edv[4])::"memory");
    }
    if constexpr (kp == 1) AWAIT(26, sl);
    if constexpr (kp == 2) AWAIT(29, sl);
    if constexpr (kp == 3) {
      AWAIT(18, sl);
      asm volatile("" : "+v"(egv[0]), "+v"(egv[1]), "+v"(egv[2]), "+v"(egv[3]), "+v"(egv[4])::"memory");
    }
    if constexpr (kp == 4) AWAIT(15, sl);
    if constexpr (kp == 5 || kp == 11 || kp == 12) AWAIT(13, sl);
    if constexpr (kp == 6) AWAIT(19, sl);
    if constexpr (kp == 7 || kp == 9) AWAIT(18, sl);
    if constexpr (kp == 7) asm volatile("" : "+v"(of)::"memory");
    if constexpr (kp == 8) AWAIT(28, sl);
    if constexpr (kp == 10) AWAIT(23, sl);
    if constexpr (kp == 13) AWAIT(4, sl);
    if constexpr (kp == 14) AWAIT(0, sl);
    if constexpr (kp == 7) {
      if (!__all(of == tag)) {
        unsigned spins = 0;
        for (;;) {
          of = ld4_wait(ws, ooff);
          if (__all(of == tag)) break;
          if (spin_fail(spins, tmo, tag)) break;
        }
      }
      SB();
    }
    if constexpr (kp == 5) STAMP(21);
    if constexpr (kp == 10) STAMP(22);
    if constexpr (kp + 3 < 15) ga(IC<kp + 3>{});
    sfor<0, 20>([&](auto I) {
      constexpr int i = I, nt = i >> 2, mt = i & 3;
      acc[mt][nt] = mfma(bfrag(KP, IC<nt>{}), a[sl][mt], acc[mt][nt]);
      if constexpr (kp + 1 < 15 && i < 10) rd(IC<kp + 1>{}, I);
      if constexpr (kp < 3 && (i == 3 || i == 9 || i == 15 || (kp == 2 && i == 18))) gc(IC<4>{}, IC<3 * kp + (i == 18 ? 3 : i / 6)>{});
      if constexpr ((kp == 5 || kp == 7) && (i & 1) == 1) gw_(IC<kp - 5 + (i >= 10)>{}, IC<(i % 10) / 2>{});
      if constexpr (kp == 9 && (i & 3) == 1) gw_(IC<4>{}, IC<(i >> 2)>{});
      if constexpr (kp == 4 && i == 19) of = ld4_sc1(ws, ooff);
      SB();
    });
  });
  STAMP(23);
  LSYNC();
#pragma unroll
  for (int mt = 0; mt < 4; ++mt)
#pragma unroll
    for (int nt = 0; nt < 5; ++nt)
      *(LAS f32x4*)(lds + ((w * 64 + 16 * mt + r) * 84 + 16 * nt + 4 * g) * 4) = acc[mt][nt];
  f32x4 skip[5];
#pragma unroll
  for (int it = 0; it < 5; ++it) {
    const unsigned s0 = pk2(blo(exv[it][0]) * blo(edv[it][0]), bhi(exv[it][0]) * bhi(edv[it][0]));
    const unsigned s1 = pk2(blo(exv[it][1]) * blo(edv[it][1]), bhi(exv[it][1]) * bhi(edv[it][1]));
    skip[it] = f32x4{blo(s0), bhi(s0), blo(s1), bhi(s1)} * __uint_as_float(egv[it]);
  }
  LSYNC();
  STAMP(24);
#pragma unroll
  for (int it = 0; it < 5; ++it) {
    const f32x4 s0 = *(LAS f32x4*)(lds + elds[it]), s1 = *(LAS f32x4*)(lds + elds[it] + 64 * 84 * 4);
    const f32x4 s2 = *(LAS f32x4*)(lds + elds[it] + 2 * 64 * 84 * 4), s3 = *(LAS f32x4*)(lds + elds[it] + 3 * 64 * 84 * 4);
    *(f32x4*)((char*)out + eoff[it]) = ((s0 + s1) + (s2 + s3)) + skip[it];
  }
  asm volatile(".Lkf_end:");
  STAMP(25);
  CSTAMP(31);
  STAMP_OUT(ws, 0);
}

extern "C" void kernel_launch(void* const* d_in, const int* in_sizes, int n_in, void* d_out, int out_size, void* d_ws,
                              size_t ws_size, hipStream_t stream) {
  const u16* x = (const u16*)d_in[0];
  const u16* lre = (const u16*)d_in[1];
  const u16* lim = (const u16*)d_in[2];
  const u16* bre = (const u16*)d_in[3];
  const u16* bim = (const u16*)d_in[4];
  const u16* cre = (const u16*)d_in[5];
  const u16* cim = (const u16*)d_in[6];
  const u16* ldt = (const u16*)d_in[7];
  const u16* dskip = (const u16*)d_in[8];
  const u16* wqkv = (const u16*)d_in[10];
  const u16* wo = (const u16*)d_in[11];
  const u16* gw = (const u16*)d_in[12];
  constexpr int LDS = 163840;
  (void)hipFuncSetAttribute((const void*)k_fused, hipFuncAttributeMaxDynamicSharedMemorySize, LDS);
  k_fused<<<256, 256, LDS, stream>>>(x, lre, lim, ldt, bre, bim, wqkv, gw, cre, cim, wo, dskip, (char*)d_ws, (float*)d_out);
}
```

```cpp
#include <hip/hip_runtime.h>
#include <stdint.h>

typedef unsigned short u16;
typedef unsigned long long u64;
typedef short bf16x8 __attribute__((ext_vector_type(8)));
typedef short bf16x4 __attribute__((ext_vector_type(4)));
typedef float f32x4 __attribute__((ext_vector_type(4)));
typedef float f32x2 __attribute__((ext_vector_type(2)));
typedef unsigned u32x4 __attribute__((ext_vector_type(4)));
typedef unsigned u32x2 __attribute__((ext_vector_type(2)));
typedef __bf16 bf16v2 __attribute__((ext_vector_type(2)));
#define LAS __attribute__((address_space(3)))
#define GAS __attribute__((address_space(1)))
#define DI __device__ __forceinline__

constexpr unsigned WS_CFLAG = 0;
constexpr unsigned WS_HFLAG = 1024;
constexpr unsigned WS_OFLAG = 3072;
constexpr unsigned WS_TMO   = 2048;
constexpr unsigned WS_EPOCH = 2304;
constexpr unsigned WS_AGG   = 4096;
constexpr unsigned WS_PART  = WS_AGG + 16 * 16 * 80 * 8;
constexpr unsigned WS_FIN   = WS_PART + 16 * 16 * 848 * 8;
constexpr unsigned WS_ZEND  = WS_FIN + 16 * 848 * 8;
constexpr unsigned WS_WKV   = (WS_ZEND + 4095) / 4096 * 4096;
constexpr unsigned WS_WQ    = WS_WKV + 16 * 102400;
constexpr unsigned WS_WO    = WS_WQ + 16 * 61440;
constexpr unsigned WS_A     = WS_WO + 8 * 20 * 5120;
constexpr unsigned WS_G     = WS_A + 128 * 240 * 256;
constexpr unsigned SPIN_CAP = 1u << 15;

DI unsigned pk2(float a, float b) { f32x2 p = {a, b}; return __builtin_bit_cast(unsigned, __builtin_convertvector(p, bf16v2)); }
DI float blo(unsigned u) { return __uint_as_float(u << 16); }
DI float bhi(unsigned u) { return __uint_as_float(u & 0xffff0000u); }
DI float rbf(float a) { return blo(pk2(a, a)); }
DI float bf2f(unsigned v) { return __uint_as_float(v << 16); }
DI bf16x8 frag4(unsigned a, unsigned b, unsigned c, unsigned d) { u32x4 v = {a, b, c, d}; return __builtin_bit_cast(bf16x8, v); }
DI f32x4 mfma(bf16x8 a, bf16x8 b, f32x4 c) { return __builtin_amdgcn_mfma_f32_16x16x32_bf16(a, b, c, 0, 0, 0); }
DI bf16x8 lds_frag(LAS char* p) { return *(LAS bf16x8*)p; }
DI bf16x8 lds_frag_tr(LAS char* p0, LAS char* p1) {
  bf16x4 a = __builtin_amdgcn_ds_read_tr16_b64_v4i16((LAS bf16x4*)p0);
  bf16x4 b = __builtin_amdgcn_ds_read_tr16_b64_v4i16((LAS bf16x4*)p1);
  return __builtin_shufflevector(a, b, 0, 1, 2, 3, 4, 5, 6, 7);
}
template <int SC1, unsigned IMM, int GOFF = 0>
DI void glds(const void* sbase, unsigned voff, unsigned ldsbase) {
  if constexpr (SC1)
    asm volatile("s_add_u32 m0, %2, %3\n\ts_nop 0\n\tglobal_load_lds_dwordx4 %0, %1 offset:%4 sc1" ::"v"(voff), "s"(sbase), "s"(ldsbase), "i"(IMM - GOFF), "i"(GOFF) : "memory", "scc");
  else
    asm volatile("s_add_u32 m0, %2, %3\n\ts_nop 0\n\tglobal_load_lds_dwordx4 %0, %1 offset:%4" ::"v"(voff), "s"(sbase), "s"(ldsbase), "i"(IMM - GOFF), "i"(GOFF) : "memory", "scc");
}
template <int V> struct IC { static constexpr int value = V; constexpr operator int() const { return V; } };
template <int I, int N, class F>
DI void sfor(F&& f) { if constexpr (I < N) { f(IC<I>{}); sfor<I + 1, N>(f); } }
DI void touch(const void* p, unsigned ldsdummy) {
  asm volatile("s_mov_b32 m0, %1\n\ts_nop 0\n\tglobal_load_lds_dword %0, off" ::"v"(p), "s"(ldsdummy) : "memory");
}
DI void touch_s(const void* sbase, unsigned voff, unsigned ldsdummy) {
  asm volatile("s_mov_b32 m0, %2\n\ts_nop 0\n\tglobal_load_lds_dword %0, %1" ::"v"(voff), "s"(sbase), "s"(ldsdummy) : "memory");
}
DI void store16_sc1(void* sbase, unsigned voff, u32x4 v) {
  asm volatile("global_store_dwordx4 %0, %1, %2 sc1\n\ts_nop 1" ::"v"(voff), "v"(v), "s"(sbase) : "memory");
}
#define SYNC(N) do { asm volatile("s_waitcnt vmcnt(%0) lgkmcnt(0)\n\ts_barrier" ::"n"(N) : "memory"); __builtin_amdgcn_sched_barrier(0); } while (0)
#define LSYNC() do { asm volatile("s_waitcnt lgkmcnt(0)\n\ts_barrier" ::: "memory"); __builtin_amdgcn_sched_barrier(0); } while (0)
template <int OFF>
DI void gstore(const void* sbase, unsigned voff, float v, unsigned tag) {
  const u32x2 d = {__float_as_uint(v), tag};
  asm volatile("global_store_dwordx2 %0, %1, %2 offset:%3 sc1" ::"v"(voff), "v"(d), "s"(sbase), "i"(OFF) : "memory");
}
DI u32x2 ld8(const void* sbase, unsigned voff) { u32x2 t; asm volatile("global_load_dwordx2 %0, %1, %2" : "=v"(t) : "v"(voff), "s"(sbase) : "memory"); return t; }
DI u32x4 ld16(const void* sbase, unsigned voff) { u32x4 t; asm volatile("global_load_dwordx4 %0, %1, %2" : "=v"(t) : "v"(voff), "s"(sbase) : "memory"); return t; }
DI unsigned ld4_sc1(const void* sbase, unsigned voff) { unsigned t; asm volatile("global_load_dword %0, %1, %2 sc1" : "=v"(t) : "v"(voff), "s"(sbase) : "memory"); return t; }
DI bf16x8 ld16_sc1(const void* sbase, unsigned voff) { bf16x8 t; asm volatile("global_load_dwordx4 %0, %1, %2 sc1" : "=v"(t) : "v"(voff), "s"(sbase) : "memory"); return t; }
DI u32x4 ld16u_sc1(const void* sbase, unsigned voff) { u32x4 t; asm volatile("global_load_dwordx4 %0, %1, %2 sc1" : "=v"(t) : "v"(voff), "s"(sbase) : "memory"); return t; }
DI u32x2 ld8_sc1(const void* sbase, unsigned voff) { u32x2 t; asm volatile("global_load_dwordx2 %0, %1, %2 sc1" : "=v"(t) : "v"(voff), "s"(sbase) : "memory"); return t; }
DI u32x2 ld8_wait(const void* sbase, unsigned voff) { u32x2 t; asm volatile("global_load_dwordx2 %0, %1, %2 sc1\n\ts_waitcnt vmcnt(0)" : "=&v"(t) : "v"(voff), "s"(sbase) : "memory"); return t; }
DI u32x4 ld16_wait(const void* sbase, unsigned voff) { u32x4 t; asm volatile("global_load_dwordx4 %0, %1, %2 sc1\n\ts_waitcnt vmcnt(0)" : "=&v"(t) : "v"(voff), "s"(sbase) : "memory"); return t; }
DI unsigned ld4_wait(const void* sbase, unsigned voff) { unsigned t; asm volatile("global_load_dword %0, %1, %2 sc1\n\ts_waitcnt vmcnt(0)" : "=&v"(t) : "v"(voff), "s"(sbase) : "memory"); return t; }
DI const char* uptr(const void* p) {
  const u64 v = (u64)p;
  return (const char*)(((u64)(unsigned)__builtin_amdgcn_readfirstlane((unsigned)(v >> 32)) << 32) | (unsigned)__builtin_amdgcn_readfirstlane((unsigned)v));
}
DI const char* sptr(const void* base, unsigned off) { return (const char*)base + (unsigned)__builtin_amdgcn_readfirstlane(off); }
DI void store8_sc1(void* sbase, unsigned voff, u32x2 v) {
  asm volatile("global_store_dwordx2 %0, %1, %2 sc1" ::"v"(voff), "v"(v), "s"(sbase) : "memory");
}
DI void store4_sc1(void* sbase, unsigned voff, unsigned v) {
  asm volatile("global_store_dword %0, %1, %2 sc1" ::"v"(voff), "v"(v), "s"(sbase) : "memory");
}
DI bool spin_fail(unsigned& spins, GAS unsigned* tmo, unsigned tag) {
  __builtin_amdgcn_s_sleep(1);
  if (++spins > SPIN_CAP) { __hip_atomic_store(tmo, tag, __ATOMIC_RELAXED, __HIP_MEMORY_SCOPE_AGENT); return true; }
  if ((spins & 63u) == 0u)
    return (unsigned)__builtin_amdgcn_readfirstlane(__hip_atomic_load(tmo, __ATOMIC_RELAXED, __HIP_MEMORY_SCOPE_AGENT)) == tag;
  return false;
}
DI f32x2 cfma(f32x2 a, f32x2 h, f32x2 u) {
  f32x2 t, o;
  asm("v_pk_fma_f32 %0, %1, %2, %3 op_sel_hi:[0,1,1]" : "=v"(t) : "v"(a), "v"(h), "v"(u));
  asm("v_pk_fma_f32 %0, %1, %2, %3 op_sel:[1,1,0] op_sel_hi:[1,0,1] neg_lo:[1,0,0]" : "=v"(o) : "v"(a), "v"(h), "v"(t));
  return o;
}
DI f32x2 cfma_1(f32x2 a, f32x2 h, f32x2 u) {
  f32x2 t;
  asm("v_pk_fma_f32 %0, %1, %2, %3 op_sel_hi:[0,1,1]" : "=v"(t) : "v"(a), "v"(h), "v"(u));
  return t;
}
DI f32x2 cfma_2(f32x2 a, f32x2 h, f32x2 t) {
  f32x2 o;
  asm("v_pk_fma_f32 %0, %1, %2, %3 op_sel:[1,1,0] op_sel_hi:[1,0,1] neg_lo:[1,0,0]" : "=v"(o) : "v"(a), "v"(h), "v"(t));
  return o;
}
DI f32x2 cco_1(f32x2 a, f32x2 h) {
  f32x2 t;
  asm("v_pk_mul_f32 %0, %1, %2 op_sel_hi:[0,1] neg_hi:[1,0]" : "=v"(t) : "v"(a), "v"(h));
  return t;
}
DI f32x2 cco_2(f32x2 a, f32x2 h, f32x2 t) {
  f32x2 o;
  asm("v_pk_fma_f32 %0, %1, %2, %3 op_sel:[1,1,0] op_sel_hi:[1,0,1] neg_lo:[1,0,0] neg_hi:[1,0,0]" : "=v"(o) : "v"(a), "v"(h), "v"(t));
  return o;
}
DI f32x2 cmul(f32x2 a, f32x2 h) {
  f32x2 t, o;
  asm("v_pk_mul_f32 %0, %1, %2 op_sel:[1,1] op_sel_hi:[1,0]" : "=v"(t) : "v"(a), "v"(h));
  asm("v_pk_fma_f32 %0, %1, %2, %3 op_sel_hi:[0,1,1] neg_lo:[0,0,1]" : "=v"(o) : "v"(a), "v"(h), "v"(t));
  return o;
}
DI f32x2 csq(f32x2 a) { return cmul(a, a); }
DI unsigned phi2(float a, float b) {
  f32x2 t = {a, b};
  asm("" : "+v"(t));
  a = t.x; b = t.y;
  const f32x2 l = t * 1.4426950408889634f;
  f32x2 e, m;
  e.x = __builtin_amdgcn_fmed3f(__builtin_amdgcn_exp2f(l.x), 0.f, 1.f);
  e.y = __builtin_amdgcn_fmed3f(__builtin_amdgcn_exp2f(l.y), 0.f, 1.f);
  m.x = __builtin_amdgcn_fmed3f(a, 0.f, __builtin_inff()); m.y = __builtin_amdgcn_fmed3f(b, 0.f, __builtin_inff());
  const f32x2 p = (e + m) + 1e-6f;
  return pk2(p.x, p.y);
}
constexpr int T2N = 2, TCN = 1;
#define SB() __builtin_amdgcn_sched_barrier(0)
#define VWAIT(N) do { asm volatile("s_waitcnt vmcnt(%0)" ::"n"(N) : "memory"); SB(); } while (0)

__global__ __launch_bounds__(256, 1) void k_fused(const u16* __restrict__ x, const u16* __restrict__ lre, const u16* __restrict__ lim,
                                                  const u16* __restrict__ ldt, const u16* __restrict__ bre, const u16* __restrict__ bim,
                                                  const u16* __restrict__ wqkv, const u16* __restrict__ gw, const u16* __restrict__ cre,
                                                  const u16* __restrict__ cim, const u16* __restrict__ wo, const u16* __restrict__ dskip,
                                                  char* __restrict__ ws, float* __restrict__ out) {
  extern __shared__ __attribute__((aligned(16))) char lds_[];
  LAS char* lds = (LAS char*)lds_;
  const unsigned ldsb = (unsigned)(size_t)lds;
  asm volatile("" ::"s"(x), "s"(lre), "s"(lim), "s"(ldt), "s"(bre), "s"(bim), "s"(wqkv), "s"(gw), "s"(ws));
  __builtin_amdgcn_sched_barrier(0);
  const int tid = threadIdx.x, lane = tid & 63;
  const int w = __builtin_amdgcn_readfirstlane(tid >> 6);
  const int xcd = blockIdx.x & 7, idx = blockIdx.x >> 3;
  const int rb = 4 * (xcd & 3) + (idx & 3), cg = 8 * (xcd >> 2) + (idx >> 2);
  const int r = lane & 15, g = lane >> 4;
  GAS unsigned* tmo = (GAS unsigned*)(ws + WS_TMO);

  constexpr unsigned SLOT = 20480;
  constexpr unsigned MISC = 0, A0 = 10240, A1 = A0 + SLOT, A2 = A0 + 2 * SLOT, A3 = A0 + 3 * SLOT, B0 = A0 + 4 * SLOT, B1 = A0 + 5 * SLOT,
                     B2 = A0 + 6 * SLOT;
  constexpr unsigned L_E = MISC;
  constexpr unsigned L_G = MISC + 5120;
  constexpr unsigned L_KVT = MISC + 5632;
  constexpr unsigned L_DUMMY = MISC + 7424;
  constexpr unsigned L_H = MISC + 7680;
  constexpr unsigned L_C = MISC + 8704;
  constexpr unsigned ASLOT[4] = {A0, A1, A2, A3};
  constexpr unsigned BSLOT[3] = {B0, B1, B2};
  constexpr unsigned KVSLOT[5] = {B2, A2, B0, A3, A0};
  constexpr unsigned QSLOT[5] = {B1, B1 + 12288, B1 + 24576, B1 + 36864, B0};

  unsigned xoff[2];
  {
    const int l3 = lane >> 3;
    const unsigned row = 128 * rb + 32 * w + 8 * (l3 >> 2) + (l3 & 3), sw = ((lane & 7) ^ (l3 >> 1)) << 4;
    xoff[0] = row * 1280 + sw;
    xoff[1] = (row + 16) * 1280 + (sw ^ 64);
  }
  const char* const xb1 = (const char*)x + 4 * 1280;
  unsigned boff[2];
  {
    const int rr = 4 * w + g, sw = (r ^ rr) << 4;
    boff[0] = (40 * cg + rr) * 1280 + sw;
    boff[1] = (40 * cg + 32 + (rr & 7)) * 1280 + sw;
  }
  const char* const bb4 = uptr(w < 2 ? bre : bim);
  unsigned ro[5];
  {
    const unsigned r0 = (unsigned)(cg + rb) % 5u;
#pragma unroll
    for (int i = 0; i < 5; ++i) ro[i] = __builtin_amdgcn_readfirstlane((r0 + i >= 5 ? r0 + i - 5 : r0 + i) * 256);
  }
  const unsigned xdst = ldsb + 32 * w * 128, bdst = ldsb + 5 * w * 1024, bdst1 = ldsb + w * 1024, qdst = ldsb + 3 * w * 1024;
  auto gx = [&](auto S, auto N) { constexpr int s = S, n = N; glds<0, ASLOT[s & 3] + n * 1024, (s & 1) * 128>((n < 2 ? (const char*)x : xb1) + ro[s >> 1], xoff[n & 1], xdst); };
  auto gb = [&](auto BE, auto I) {
    constexpr int be = BE, i = I;
    if constexpr (i < 4) glds<0, BSLOT[be % 3] + i * 4096, 0>((const char*)((i & 1) ? bim : bre) + (i >> 1) * 20480 + ro[be], boff[0], bdst1);
    else glds<0, BSLOT[be % 3] + 4 * 4096, 0>(bb4 + ro[be], boff[1], bdst1);
  };
  const unsigned lin = lane * 16;
  const char* kvsrc = sptr(ws, WS_WKV + cg * 102400 + w * 5120 + 2048);
  const char* qsrc = sptr(ws, WS_WQ + cg * 61440 + w * 3072 + 1024);
  auto gkv = [&](auto J, auto I) { constexpr int j = J, i = I; glds<1, KVSLOT[j] + i * 1024, i * 1024 - 2048>(kvsrc + ro[j] * 80, lin, bdst); };
  auto gq = [&](auto M, auto I) { constexpr int m = M, i = I; glds<1, QSLOT[m] + i * 1024, i * 1024 - 1024>(qsrc + ro[m] * 48, lin, qdst); };

  unsigned p_lre, p_lim, p_ldt, p_ep, p_gw;
  {
    asm volatile("global_load_dword %0, %1, %2 sc1" : "=v"(p_ep) : "v"(WS_EPOCH), "s"(ws) : "memory");
    const int l39 = lane < 40 ? lane : 39, ch = 40 * cg + l39;
    asm volatile("global_load_ushort %0, %1, %2" : "=v"(p_lre) : "v"(2 * ch), "s"(lre) : "memory");
    asm volatile("global_load_ushort %0, %1, %2" : "=v"(p_lim) : "v"(2 * ch), "s"(lim) : "memory");
    asm volatile("global_load_ushort %0, %1, %2" : "=v"(p_ldt) : "v"(2 * ch), "s"(ldt) : "memory");
    asm volatile("global_load_ushort %0, %1, %2" : "=v"(p_gw) : "v"(2 * (40 * rb + l39)), "s"(gw) : "memory");
  }
  sfor<0, 4>([&](auto N) { gx(IC<0>{}, N); });
  sfor<0, 5>([&](auto I) { gb(IC<0>{}, I); });
  SB();
  u32x4 cq[3];
  u32x2 ct[3];
  unsigned csrc[3], cdst[3];
  const int cj = tid & 15, crow0 = tid >> 4;
  const bool ctail = (0x4300u >> cj) & 1u, cgate = cj == 15;
  const int crow[3] = {crow0, crow0 + 16, 32 + (crow0 & 7)};
  sfor<0, 4>([&](auto N) { gx(IC<1>{}, N); });
  {
    const unsigned i = idx * 4 + w;
    const char* tp = (lane & 1) ? (const char*)out + min(i, 79u) * 65536 : ws + WS_A + min(i, 119u) * 65536;
    const char* dp = (const char*)dskip + (idx >> 2) * 160 + (lane & 1) * 128;
    const char* wp = (const char*)wo + (16 * rb + cg) * 3200 + min((unsigned)(7 * w + (lane & 7)) * 128, 3196u);
    u64 pc;
    unsigned clen;
    asm volatile("s_getpc_b64 %0\n.Lkf_pc:\n\ts_mov_b32 %1, .Lkf_end-.Lkf_pc" : "=s"(pc), "=s"(clen));
    const char* cp = (const char*)pc + (min(i * 384 + (lane & 3) * 128, clen - 4u) & ~3u);
    tp = lane >= 4 && lane < 7 ? cp : tp;
    const unsigned po = (lane & 2) ? ((lane & 1) ? WS_PART + (cg * 16 + rb) * 6784 : WS_WO + (16 * rb + cg) * 3200)
                                   : ((lane & 1) ? WS_WQ + cg * 61440 + rb * 3840 : WS_WKV + cg * 102400 + rb * 6400);
    tp = lane >= 24 && lane < 28 ? ws + po : tp;
    touch(lane >= 16 && lane < 23 ? wp : (lane >= 8 && lane < 10 ? dp : tp), ldsb + L_DUMMY);
  }
  {
    const unsigned sec = (0x0009aa55u >> (2 * cj)) & 3u;
    const unsigned off4 = (unsigned)(0x0475204475207520ull >> (4 * cj)) & 15u;
    const unsigned colb = 2 * (sec * 640 + 40 * cg + 4 * off4) + 40 * rb * 3840;
    const unsigned rbytes = cj >= 10 ? 96 : 160;
    const unsigned dbase = cj >= 10 ? WS_WQ + cg * 61440 + (cj - 10) * 16 : WS_WKV + cg * 102400 + cj * 16;
#pragma unroll
    for (int it = 0; it < 3; ++it) {
      const unsigned k = 40 * rb + crow[it];
      csrc[it] = colb + crow[it] * 3840;
      cdst[it] = dbase + (k ^ (((k >> 3) & 1) << 2)) * rbytes;
    }
    asm volatile("" : "+v"(csrc[0]), "+v"(csrc[1]), "+v"(csrc[2]), "+v"(cdst[0]), "+v"(cdst[1]), "+v"(cdst[2]));
  }
  auto cload = [&](auto N) {
    constexpr int n = N, it = n >> 1;
    if constexpr (n & 1) ct[it] = ld8(wqkv, ctail ? csrc[it] + 40 : 0u);
    else cq[it] = ld16(wqkv, csrc[it]);
  };
  auto cstore = [&](auto IT) {
    constexpr int it = IT;
    u32x4 v = cq[it];
    const unsigned gwv = __shfl(p_gw, crow[it]);
    v[0] = cgate ? gwv : v[0]; v[1] = cgate ? 0u : v[1];
    v[2] = ctail ? ct[it][0] : (cgate ? 0u : v[2]); v[3] = ctail ? ct[it][1] : (cgate ? 0u : v[3]);
    store16_sc1(ws, cdst[it], v);
  };

  f32x2 c_a, c_a8;
  float c_lmag, c_rev;
  f32x2 pa[3], pc[3], pw[3];
  f32x2 pj[4];
  unsigned tag = 0;
  unsigned t2off0, t2off1, cwsrc, cwdst;
  unsigned hst[5], ost;
  unsigned imw, imw2, imr, imr4, lcp, lcp2;
  u32x4 cw;
  const char* t2p = uptr((w & 1) ? cim : cre);

  bf16x8 xa[20][2];
  f32x4 accS[5][2];
#pragma unroll
  for (int t = 0; t < 5; ++t) { accS[t][0] = f32x4{0, 0, 0, 0}; accS[t][1] = f32x4{0, 0, 0, 0}; }
  bf16x8 bq[2][5];
  LAS char* xrd[2];
  LAS char* brd[4];
#pragma unroll
  for (int kk = 0; kk < 2; ++kk) xrd[kk] = lds + A0 + (32 * w + r) * 128 + (((4 * kk + g) ^ ((r >> 1) & 7)) << 4);
#pragma unroll
  for (int kq = 0; kq < 4; ++kq) brd[kq] = lds + B0 + r * 256 + (((4 * kq + g) ^ r) << 4);
  auto rd1 = [&](auto KS, auto I) {
    constexpr int ks = KS, i = I;
    if constexpr (i < 2) xa[ks][i] = lds_frag(xrd[ks & 1] + (ASLOT[(ks >> 1) & 3] - A0) + i * 2048);
    else bq[ks & 1][i - 2] = lds_frag(brd[ks & 3] + (BSLOT[(ks >> 2) % 3] - B0) + (i - 2) * 4096);
  };
  unsigned cf = 0;
  const GAS unsigned* cfp = (const GAS unsigned*)(ws + WS_CFLAG) + cg * 16 + r;

  auto dma1 = [&](auto S, auto J) {
    constexpr int s = S, j = J;
    if constexpr (s == 0) {
      if constexpr (j < 4) gx(IC<2>{}, J); else if constexpr (j < 9) gb(IC<1>{}, IC<j - 4>{}); else if constexpr (j < 15) cload(IC<j - 9>{});
    } else if constexpr (s == 1) {
      if constexpr (j < 4) gx(IC<3>{}, J);
      else if constexpr (j < 8) gx(IC<4>{}, IC<j - 4>{});
      else if constexpr (j < 13) gb(IC<2>{}, IC<j - 8>{});
    } else if constexpr (s == 2 && j >= 100) {
      cstore(IC<j - 100>{});
    } else if constexpr (s <= 6) {
      if constexpr (j < 4) gx(IC<s + 3>{}, J);
      else if constexpr (s <= 5) {
        constexpr int be = 2 + (s >> 1), i = (s & 1) ? j - 1 : j - 4;
        if constexpr ((s & 1) ? j < 6 : j < 7) gb(IC<be>{}, IC<i>{});
      } else if constexpr (j == 4) {
        cf = ld4_sc1(ws, WS_CFLAG + (cg * 16 + r) * 4);
      }
    } else if constexpr (s == 7) {
      const u32x4 z4 = {0u, tag - 1u, 0u, tag - 1u};
      if constexpr (j < 2) store16_sc1(ws, WS_PART + (cg * 16 + rb) * 6784 + min(tid + 256 * j, 423) * 16, z4);
      else if constexpr (j == 2) {
        if (w & 1) store8_sc1(ws, WS_FIN + (cg * 848 + min(53 * rb + min(lane, 52), 847)) * 8, u32x2{0u, tag - 1u});
        else store16_sc1(ws, WS_AGG + (cg * 16 + rb) * 640 + min(lane, 39) * 16, z4);
      }
    } else if constexpr (s == 8) { if constexpr (j < 5) gkv(IC<0>{}, J); }
    else if constexpr (s == 9) { if constexpr (j < 5) gkv(IC<1>{}, J); }
  };

  SYNC(4 + TCN);
  sfor<0, 7>([&](auto I) { rd1(IC<0>{}, I); });
  SB();
  sfor<0, 20>([&](auto KS) {
    constexpr int ks = KS, s = (ks + 1) >> 1, first = ks & 1;
    if constexpr (first && ks + 1 < 20) {
      if constexpr (s == 1) VWAIT(15 + TCN);
      if constexpr (s == 2) {
        asm volatile("s_waitcnt vmcnt(%6)" : "+v"(cq[0]), "+v"(cq[1]), "+v"(cq[2]), "+v"(ct[0]), "+v"(ct[1]), "+v"(ct[2]) : "n"(13) : "memory");
        SYNC(13);
        sfor<0, 3>([&](auto IT) { dma1(IC<2>{}, IC<100 + IT>{}); });
      }
      if constexpr (s == 3) VWAIT(19);
      if constexpr (s == 4) {
        SYNC(13);
        if (tid == 0) __hip_atomic_store((GAS unsigned*)(ws + WS_CFLAG) + cg * 16 + rb, tag, __ATOMIC_RELAXED, __HIP_MEMORY_SCOPE_AGENT);
      }
      if constexpr (s == 5) VWAIT(16);
      if constexpr (s == 6) SYNC(13);
      if constexpr (s == 7) VWAIT(14);
      if constexpr (s == 8) {
        asm volatile("s_waitcnt vmcnt(3)" : "+v"(cf)::"memory");
        SYNC(3);
        if (!__all(cf == tag)) {
          unsigned spins = 0;
          for (;;) {
            cf = __hip_atomic_load(cfp, __ATOMIC_RELAXED, __HIP_MEMORY_SCOPE_AGENT);
            if (__all(cf == tag)) break;
            if (spin_fail(spins, tmo, tag)) break;
          }
        }
      }
    }
    { bf16x8 t0 = xa[ks][0], t1 = xa[ks][1]; asm("" : "+a"(t0), "+a"(t1)); xa[ks][0] = t0; xa[ks][1] = t1; }
    sfor<0, 10>([&](auto I) {
      constexpr int i = I;
      accS[i >> 1][i & 1] = mfma(xa[ks][i & 1], bq[ks & 1][i >> 1], accS[i >> 1][i & 1]);
      if constexpr (ks + 1 < 20 && i < 7) rd1(IC<ks + 1>{}, I);
      if constexpr (ks == 0) { if constexpr (i < 9) dma1(IC<0>{}, I); else sfor<9, 15>([&](auto J) { dma1(IC<0>{}, J); }); }
      else if constexpr (ks < 19) {
        constexpr int gi = first ? i : 10 + i;
        if constexpr (s == 1) { if constexpr (gi < 13) dma1(IC<1>{}, IC<gi>{}); }
        else if constexpr (s == 8 || s == 9) { if constexpr (gi % 4 == 1) dma1(IC<s>{}, IC<gi / 4>{}); }
        else if constexpr (gi % 3 == 1) dma1(IC<s>{}, IC<gi / 3>{});
      }
      SB();
    });
    if constexpr (ks == 0) {
  asm volatile("" : "+v"(p_lre), "+v"(p_lim), "+v"(p_ldt), "+v"(p_ep), "+v"(p_gw)::"memory");
  tag = __builtin_amdgcn_readfirstlane(p_ep) + 1u;
  {
    const float lr = bf2f(p_lre), li = bf2f(p_lim);
    const float dt = rbf(__expf(bf2f(p_ldt)));
    c_lmag = dt * lr * 1.4426950408889634f; c_rev = dt * li * 0.15915494309189535f;
    const float er = __builtin_amdgcn_exp2f(c_lmag), rev = __builtin_amdgcn_fractf(c_rev);
    c_a = f32x2{er * __builtin_amdgcn_cosf(rev), er * __builtin_amdgcn_sinf(rev)};
    const float nr = c_a.x - 1.f, ni = c_a.y, iden = 1.f / (lr * lr + li * li);
    const float cr = (nr * lr + ni * li) * iden, ci = (ni * lr - nr * li) * iden;
    c_a8 = csq(csq(csq(c_a)));
#pragma unroll
    for (int p = 0; p < 3; ++p) {
      const int src = p < 2 ? 16 * p + r : 32 + (r & 7);
      pa[p] = f32x2{__shfl(c_a.x, src), __shfl(c_a.y, src)};
      pc[p] = f32x2{2.f * __shfl(cr, src), 2.f * __shfl(ci, src)};
    }
    auto cpow = [](float lmag, float rev_, float n) {
      const float m = __builtin_amdgcn_exp2f(n * lmag), f = __builtin_amdgcn_fractf(n * rev_);
      return f32x2{m * __builtin_amdgcn_cosf(f), m * __builtin_amdgcn_sinf(f)};
    };
    const float nseg = (float)(8 * (4 * w + g));
#pragma unroll
    for (int p = 0; p < 3; ++p) {
      const int src = p < 2 ? 16 * p + r : 32 + (r & 7);
      pw[p] = cpow(__shfl(c_lmag, src), __shfl(c_rev, src), nseg);
    }
#pragma unroll
    for (int i = 0; i < 4; ++i) pj[i] = cpow(c_lmag, c_rev, (float)(128 * max(rb - 1 - w - 4 * i, 0)));
    asm volatile("" : "+v"(pw[0]), "+v"(pw[1]), "+v"(pw[2]), "+v"(pj[0]), "+v"(pj[1]), "+v"(pj[2]), "+v"(pj[3]));
    asm volatile("" : "+v"(pa[0]), "+v"(pa[1]), "+v"(pa[2]), "+v"(pc[0]), "+v"(pc[1]), "+v"(pc[2]), "+v"(c_a8));
  }
  { const unsigned t2row = (80 * (idx >> 2) + 20 * (idx & 3) + 10 * (w >> 1)) * 1280;
  t2off0 = t2row + (min(lane, 39) >> 2) * 1280 + (lane & 3) * 128;
  const unsigned t2l = min(lane, 59), t2q = (t2l * 10923) >> 16;
  t2off1 = t2row + t2q * 1280 + (4 + t2l - 6 * t2q) * 128;
  }
  {
    const unsigned sc = 200 * (16 * rb + cg) + min(tid, 199);
    cwsrc = sc * 16;
    const unsigned k = (sc * 52429u) >> 22, cc = sc - 80 * k, jw = (cc * 6554) >> 16, ch = cc - 10 * jw;
    const unsigned cgq = (k * 1639) >> 16, rem = k - 40 * cgq, hh = rem >= 20, e = rem - 20 * hh;
    const unsigned j = 4 * hh + (e & 3), qq = 5 * cgq + (e >> 2), oc = qq & 3;
    cwdst = WS_WO + ((jw * 20 + (qq >> 2)) * 32 + 8 * oc + (j ^ ((oc & 1) << 2))) * 160 + ch * 16;
  }
  asm volatile("" : "+v"(t2off0), "+v"(t2off1), "+v"(cwsrc), "+v"(cwdst));
  {
#pragma unroll
    for (int f = 0; f < 5; ++f) {
      const int mt = f < 4 ? (f >> 1) : (g >> 1);
      const int o8 = f < 4 ? 4 * (f & 1) + g : 8 + (g & 1);
      const int part = o8 >= 5, o = 5 * cg + (part ? o8 - 5 : o8);
      const int q = 16 * (o >> 3) + 8 * part + (o & 7);
      hst[f] = WS_A + ((8 * rb + 2 * w + mt) * 240 + q) * 256 + r * 16;
    }
    ost = WS_A + ((8 * rb + 2 * w) * 240 + 160 + 5 * cg) * 256 + r * 16;
    const unsigned img = B0 + w * 6400;
    imw = img + r * 40 + 8 * g;
    imw2 = img + (r < 8 ? 32 + r : 64 + r) * 40 + 8 * g;
    imr = img + (8 * g + (r >> 2)) * 40 + 8 * (r & 3);
    imr4 = img + (g >> 1) * 3200 + (64 + 8 * (g & 1) + (r >> 2)) * 40 + 8 * (r & 3);
    lcp = r * 8; lcp2 = (32 + (r & 7)) * 8;
    asm volatile("" : "+v"(hst[0]), "+v"(hst[1]), "+v"(hst[2]), "+v"(hst[3]), "+v"(hst[4]), "+v"(ost), "+v"(imw), "+v"(imw2), "+v"(imr),
                 "+v"(imr4), "+v"(lcp), "+v"(lcp2));
  }
      SB();
    }
  });

  f32x2 ub[3][8];
  {
#pragma unroll
    for (int t = 0; t < 8; ++t) {
      ub[0][t] = f32x2{accS[0][t >> 2][t & 3], accS[1][t >> 2][t & 3]};
      ub[1][t] = f32x2{accS[2][t >> 2][t & 3], accS[3][t >> 2][t & 3]};
      const float v = accS[4][t >> 2][t & 3];
      const float o = __builtin_amdgcn_update_dpp(0.f, v, 0x128, 0xf, 0xf, false);
      ub[2][t] = r < 8 ? f32x2{v, o} : f32x2{o, v};
    }
    f32x2 e[3] = {ub[0][0], ub[1][0], ub[2][0]};
    SB();
#pragma unroll
    for (int t = 1; t < 8; ++t) {
      f32x2 t1[3];
#pragma unroll
      for (int p = 0; p < 3; ++p) t1[p] = cfma_1(pa[p], e[p], ub[p][t]);
      SB();
#pragma unroll
      for (int p = 0; p < 3; ++p) e[p] = cfma_2(pa[p], e[p], t1[p]);
      SB();
    }
    LAS char* ep = lds + L_E + ((4 * w + g) * 40 + r) * 8;
    *(LAS f32x2*)ep = e[0];
    *(LAS f32x2*)(ep + 128) = e[1];
    if (r < 8) *(LAS f32x2*)(ep + 256) = e[2];
  }

  f32x4 accK[5][2];
#pragma unroll
  for (int t = 0; t < 5; ++t) { accK[t][0] = f32x4{0, 0, 0, 0}; accK[t][1] = f32x4{0, 0, 0, 0}; }
  const int q4 = r >> 2, pp = r & 3;
  LAS char* kv0 = lds + (8 * g + q4) * 160 + 8 * pp + 640 * (g & 1);
  LAS char* kv1 = lds + (8 * g + q4) * 160 + 8 * pp + 640 * (1 - (g & 1));
  bf16x4 bh[2][5][2];
  LAS char* kb[2][2];
  auto base2 = [&](auto J) {
    constexpr int j = J;
    kb[j & 1][0] = kv0 + KVSLOT[j]; kb[j & 1][1] = kv1 + KVSLOT[j];
    asm volatile("" : "+v"(kb[j & 1][0]), "+v"(kb[j & 1][1]));
  };
  auto rd2 = [&](auto KS, auto I) {
    constexpr int ks = KS, i = I;
    constexpr unsigned o = (ks & 3) * 5120 + 32 * (i >> 1);
    bh[ks & 1][i >> 1][i & 1] = __builtin_amdgcn_ds_read_tr16_b64_v4i16((LAS bf16x4*)(kb[(ks >> 2) & 1][i & 1] + o));
  };
  auto cat = [](bf16x4 a, bf16x4 b) { return __builtin_shufflevector(a, b, 0, 1, 2, 3, 4, 5, 6, 7); };
  auto dma2 = [&](auto J, auto N) {
    constexpr int j = J, n = N;
    if constexpr (j == 0) { if constexpr (n < 10) gkv(IC<2 + n / 5>{}, IC<n % 5>{}); else if constexpr (n == 10) cw = ld16(wo, cwsrc); }
    else if constexpr (j == 1) { if constexpr (n < 5) gkv(IC<4>{}, N); }
    else if constexpr (j == 2) { if constexpr (n < 6) gq(IC<n / 3>{}, IC<n % 3>{}); }
    else if constexpr (j == 3) { if constexpr (n < 6) gq(IC<2 + n / 3>{}, IC<n % 3>{}); else if constexpr (n == 6) store16_sc1(ws, cwdst, cw); }
    else { if constexpr (n < 3) gq(IC<4>{}, N); else if constexpr (n < 3 + T2N) touch_s(t2p, n == 3 ? t2off0 : t2off1, ldsb + L_DUMMY); }
  };
  base2(IC<0>{});
  SYNC(5);
  sfor<0, 10>([&](auto I) { rd2(IC<0>{}, I); });
  SB();
  sfor<0, 20>([&](auto KS) {
    constexpr int ks = KS, j = (ks + 1) >> 2, ph = (ks + 1) & 3;
    if constexpr (ph == 0 && ks + 1 < 20) {
      base2(IC<j>{});
      if constexpr (j == 1) SYNC(11);
      if constexpr (j == 2) SYNC(11);
      if constexpr (j == 3) { asm volatile("s_waitcnt vmcnt(11)" : "+v"(cw)::"memory"); SYNC(11); }
      if constexpr (j == 4) SYNC(13);
    }
    sfor<0, 10>([&](auto I) {
      constexpr int i = I;
      accK[i >> 1][i & 1] = mfma(xa[ks][i & 1], cat(bh[ks & 1][i >> 1][0], bh[ks & 1][i >> 1][1]), accK[i >> 1][i & 1]);
      if constexpr (ks + 1 < 20) rd2(IC<ks + 1>{}, I);
      if constexpr (j == 0) { if constexpr (i % 3 == 1 || i == 9) dma2(IC<0>{}, IC<4 * ks + (i == 9 ? 3 : i / 3)>{}); }
      else if constexpr (ph < 3 && ks < 19 && (i == 1 || i == 4 || i == 7)) dma2(IC<j>{}, IC<3 * ph + (i - 1) / 3>{});
      SB();
    });
  });

  {
    bf16x8 kf[5];
#pragma unroll
    for (int t = 0; t < 5; ++t) {
      unsigned u[4];
#pragma unroll
      for (int h2 = 0; h2 < 4; ++h2) {
        const float a = accK[t][h2 >> 1][(h2 & 1) * 2], b = accK[t][h2 >> 1][(h2 & 1) * 2 + 1];
        if (t == 2 || t == 3) u[h2] = pk2(a, b);
        else if (t < 2) u[h2] = phi2(a, b);
        else { const unsigned ph = phi2(a, b), raw = pk2(a, b); u[h2] = r < 8 ? ph : raw; }
      }
      kf[t] = frag4(u[0], u[1], u[2], u[3]);
      *(LAS bf16x8*)(lds + A3 + (w * 5 + t) * 1024 + lane * 16) = kf[t];
    }
    SYNC(T2N);
    const bf16x8 ones = frag4(0x3f803f80u, 0x3f803f80u, 0x3f803f80u, 0x3f803f80u);
    const char* part = sptr(ws, WS_PART + (cg * 16 + rb) * 848 * 8);
    LAS char* st = lds + A3 + lane * 16;
    auto job = [&](LAS char* pa_, LAS char* pb_, bool onesb) {
      f32x4 d = {0, 0, 0, 0};
#pragma unroll
      for (int sw = 0; sw < 4; ++sw) {
        const bf16x8 a = lds_frag(pa_ + sw * 5120);
        bf16x8 b = lds_frag(pb_ + sw * 5120);
        if (onesb && r < 8) b = ones;
        d = mfma(a, b, d);
      }
      return d;
    };
    if (w < 2) {
      const int h = w;
      const f32x4 d0 = job(st + h * 1024, st + (2 + h) * 1024, false);
      const f32x4 d1 = job(st + 4096, st + (2 + h) * 1024, false);
      const unsigned o0 = (420 * h + 80 * g + r) * 8, o1 = (420 * h + 320 + r) * 8;
      sfor<0, 4>([&](auto I) { constexpr int i = I; gstore<160 * i>(part, o0, d0[i], tag); });
      if (g == h) sfor<0, 4>([&](auto I) { constexpr int i = I; gstore<160 * i>(part, o1, d1[i], tag); });
    } else if (w == 2) {
      const f32x4 d0 = job(st, st + 4096, true);
      const f32x4 d1 = job(st + 1024, st + 4096, true);
      const f32x4 d2 = job(st + 4096, st + 4096, true);
      const bool k0 = (r >> 2) == 2, k1 = (r >> 2) == 3, kq = (r >> 2) == 2 + g;
      const unsigned o0 = (k0 ? 80 * g + 16 + (r & 3) : 400 + 4 * g) * 8, st0 = k0 ? 160 : 8;
      const unsigned o1 = (420 + (k1 ? 80 * g + 16 + (r & 3) : 400 + 4 * g)) * 8, st1 = k1 ? 160 : 8;
      const unsigned o2 = (420 * g + (kq ? 336 + (r & 3) : 416)) * 8, st2 = kq ? 160 : 8;
      if (k0 || r == 0) sfor<0, 4>([&](auto I) { constexpr int i = I; gstore<0>(part, o0 + i * st0, d0[i], tag); });
      if (k1 || r == 0) sfor<0, 4>([&](auto I) { constexpr int i = I; gstore<0>(part, o1 + i * st1, d1[i], tag); });
      if (g < 2 && (kq || r == 0)) sfor<0, 4>([&](auto I) { constexpr int i = I; gstore<0>(part, o2 + i * st2, d2[i], tag); });
    } else {
      if (lane < 40) {
        f32x2 e[16];
#pragma unroll
        for (int s = 0; s < 16; ++s) e[s] = *(LAS f32x2*)(lds + L_E + (s * 40 + lane) * 8);
        f32x2 run = {0.f, 0.f};
#pragma unroll
        for (int s = 0; s < 16; ++s) {
          *(LAS f32x2*)(lds + L_E + (s * 40 + lane) * 8) = run;
          run = cfma(c_a8, run, e[s]);
        }
        const char* ag = sptr(ws, WS_AGG + (cg * 16 + rb) * 640);
        gstore<0>(ag, lane * 16, run.x, tag); gstore<8>(ag, lane * 16, run.y, tag);
      }
    }
  }

  const int T = 4 * xcd + (idx & 3), jt = idx >> 2;
  constexpr unsigned UNIT = 10240, R1 = B0 + 25600;
  const unsigned dA = ldsb + A0 + w * 2 * UNIT, dB = ldsb + R1 + w * UNIT;
  const char* cb = uptr((const char*)((w & 1) ? cim : cre) + (w >> 1) * 128);
  unsigned coff[2];
  {
    const int l3 = lane >> 3;
    coff[0] = (80 * jt + l3) * 1280 + (((lane & 7) ^ (l3 >> 1)) << 4);
    coff[1] = coff[0] ^ 64;
  }
  auto gc = [&](auto M, auto N) {
    constexpr int m = M, n = N;
    if constexpr (m == 2) glds<0, n * 1024, m * 256>(cb + n * 10240, coff[n & 1], dB);
    else glds<0, (m == 1 || m == 4 ? UNIT : 0) + n * 1024, m * 256>(cb + n * 10240, coff[n & 1], dA);
  };

  f32x4 accQ[3][2];
#pragma unroll
  for (int t = 0; t < 3; ++t) { accQ[t][0] = f32x4{0, 0, 0, 0}; accQ[t][1] = f32x4{0, 0, 0, 0}; }
  LAS char* q0 = lds + (8 * g + q4) * 96 + 8 * pp + 384 * (g & 1);
  LAS char* q1 = lds + (8 * g + q4) * 96 + 8 * pp + 384 * (1 - (g & 1));
  auto base3 = [&](auto M) {
    constexpr int m = M;
    kb[m & 1][0] = q0 + QSLOT[m]; kb[m & 1][1] = q1 + QSLOT[m];
    asm volatile("" : "+v"(kb[m & 1][0]), "+v"(kb[m & 1][1]));
  };
  auto rd3 = [&](auto KS, auto I) {
    constexpr int ks = KS, i = I;
    constexpr unsigned o = (ks & 3) * 3072 + 32 * (i >> 1);
    bh[ks & 1][i >> 1][i & 1] = __builtin_amdgcn_ds_read_tr16_b64_v4i16((LAS bf16x4*)(kb[(ks >> 2) & 1][i & 1] + o));
  };
  const int i1 = 53 * rb + lane;
  const bool act1 = lane < 53 && i1 < 840;
  const unsigned x1off = (act1 ? i1 : 0) * 8, x2off = min(lane, 39) * 16;
  const char* x1src = sptr(ws, WS_PART + (cg * 16 + 4 * w) * 6784);
  const char* x2src = sptr(ws, WS_AGG + cg * 16 * 640);
  u32x2 xh[4];
  u32x4 xl[4];
  auto xload = [&](auto N) {
    constexpr int n = N;
    if constexpr (n < 4) xh[n] = ld8_sc1(x1src + n * 6784, x1off);
    else { const int j = w + 4 * (n - 4); xl[n - 4] = ld16u_sc1(x2src + (j < rb ? j : 0) * 640, x2off); }
  };
  base3(IC<0>{});
  SB();
  sfor<0, 6>([&](auto I) { rd3(IC<0>{}, I); });
  SB();
  sfor<0, 20>([&](auto KS) {
    constexpr int ks = KS;
    if constexpr (((ks + 1) & 3) == 0 && ks + 1 < 20) { base3(IC<(ks + 1) / 4>{}); SB(); }
    if constexpr (ks == 1) { asm volatile("s_barrier" ::: "memory"); SB(); }
    sfor<0, 6>([&](auto I) {
      constexpr int i = I;
      accQ[i >> 1][i & 1] = mfma(cat(bh[ks & 1][i >> 1][0], bh[ks & 1][i >> 1][1]), xa[ks][i & 1], accQ[i >> 1][i & 1]);
      if constexpr (ks + 1 < 20) rd3(IC<ks + 1>{}, I);
      if constexpr (ks >= 2 && ks < 12 && (i == 1 || i == 4)) gc(IC<(ks - 2) / 5>{}, IC<2 * ((ks - 2) % 5) + (i == 4)>{});
      if constexpr (ks >= 12 && ks < 16 && (i == 1 || i == 4)) xload(IC<2 * (ks - 12) + (i == 4)>{});
      SB();
    });
  });

  float gv[2];
#pragma unroll
  for (int mt = 0; mt < 2; ++mt) {
    const float s = rbf(accQ[2][mt][0]);
    const float gg = rbf(__builtin_amdgcn_rcpf(1.f + __expf(-s)));
    const int tl = 32 * w + 8 * (r >> 2) + 4 * mt + (r & 3);
    if (g == 2) {
      *(LAS float*)(lds + L_G + tl * 4) = gg;
      if (cg == 0) store4_sc1(ws, WS_G + (128 * rb + tl) * 4, __float_as_uint(gg));
    }
    gv[mt] = __shfl(gg, 32 + r);
  }
  asm volatile("" : "+v"(gv[0]), "+v"(gv[1]));

  asm volatile("s_waitcnt vmcnt(0)"
               : "+v"(xh[0]), "+v"(xh[1]), "+v"(xh[2]), "+v"(xh[3]), "+v"(xl[0]), "+v"(xl[1]), "+v"(xl[2]), "+v"(xl[3])::"memory");
  {
    unsigned spins = 0;
    for (;;) {
      bool ok = true;
#pragma unroll
      for (int n = 0; n < 4; ++n) {
        ok &= xh[n][1] == tag || !act1;
        ok &= (xl[n][1] == tag && xl[n][3] == tag) || w + 4 * n >= rb;
      }
      if (__all(ok)) break;
      if (spin_fail(spins, tmo, tag)) break;
#pragma unroll
      for (int n = 0; n < 4; ++n) {
        const int j = w + 4 * n;
        xh[n] = ld8_wait(x1src + n * 6784, x1off);
        xl[n] = ld16_wait(x2src + (j < rb ? j : 0) * 640, x2off);
      }
    }
    *(LAS float*)(lds + L_H + tid * 4) =
        (__uint_as_float(xh[0][0]) + __uint_as_float(xh[1][0])) + (__uint_as_float(xh[2][0]) + __uint_as_float(xh[3][0]));
    f32x2 c = {0.f, 0.f};
#pragma unroll
    for (int n = 0; n < 4; ++n)
      if (w + 4 * n < rb) c = cfma(pj[n], f32x2{__uint_as_float(xl[n][0]), __uint_as_float(xl[n][2])}, c);
    if (lane < 40) *(LAS f32x2*)(lds + L_C + (w * 40 + lane) * 8) = c;
  }
  LAS char* crA[2];
  LAS char* crB[2];
#pragma unroll
  for (int kk = 0; kk < 2; ++kk) {
    const int lp = r * 128 + (((4 * kk + g) ^ ((r >> 1) & 7)) << 4);
    crA[kk] = lds + A0 + w * 2 * UNIT + lp; crB[kk] = lds + R1 + w * UNIT + lp;
  }
  asm volatile("" : "+v"(crA[0]), "+v"(crA[1]), "+v"(crB[0]), "+v"(crB[1]));
  bf16x8 bu[4][5];
#pragma unroll
  for (int kp = 0; kp < 4; ++kp)
#pragma unroll
    for (int nt = 0; nt < 5; ++nt) bu[kp][nt] = lds_frag(crA[kp & 1] + (kp >> 1) * UNIT + nt * 2048);
  LSYNC();
  if (w == 0) {
    const float s = (*(LAS float*)(lds + L_H + lane * 4) + *(LAS float*)(lds + L_H + 256 + lane * 4)) +
                    (*(LAS float*)(lds + L_H + 512 + lane * 4) + *(LAS float*)(lds + L_H + 768 + lane * 4));
    if (act1) gstore<0>(sptr(ws, WS_FIN + cg * 848 * 8), i1 * 8, s, tag);
  }

  u32x2 fv[4];
  const unsigned foff[4] = {(unsigned)tid * 8, (unsigned)(tid + 256) * 8, (unsigned)(tid + 512) * 8, (unsigned)(tid < 72 ? tid + 768 : 0) * 8};
  const char* fsrc = sptr(ws, WS_FIN + cg * 848 * 8);
  {
    const f32x4 ga = *(LAS f32x4*)(lds + L_G + (32 * w + 8 * g) * 4), gb4 = *(LAS f32x4*)(lds + L_G + (32 * w + 8 * g + 4) * 4);
    f32x2 h[3];
    const unsigned segoff = L_E + (4 * w + g) * 320;
#pragma unroll
    for (int p = 0; p < 3; ++p) {
      LAS char* cp = lds + (p < 2 ? lcp + 128 * p : lcp2);
      const f32x2 cin = (*(LAS f32x2*)(cp + L_C) + *(LAS f32x2*)(cp + L_C + 320)) + (*(LAS f32x2*)(cp + L_C + 640) + *(LAS f32x2*)(cp + L_C + 960));
      h[p] = cfma(pw[p], cin, *(LAS f32x2*)(cp + segoff));
    }
    SB();
    sfor<0, 8>([&](auto TT) {
      constexpr int t = TT;
      f32x2 t1[3];
#pragma unroll
      for (int p = 0; p < 3; ++p) t1[p] = cfma_1(pa[p], h[p], ub[p][t]);
      SB();
#pragma unroll
      for (int p = 0; p < 3; ++p) h[p] = cfma_2(pa[p], h[p], t1[p]);
      SB();
#pragma unroll
      for (int p = 0; p < 3; ++p) t1[p] = cco_1(pc[p], h[p]);
      SB();
#pragma unroll
      for (int p = 0; p < 3; ++p) t1[p] = cco_2(pc[p], h[p], t1[p]);
      SB();
#pragma unroll
      for (int p = 0; p < 3; ++p) ub[p][t] = t1[p] * (t < 4 ? ga[t & 3] : gb4[t & 3]);
      gc(IC<2>{}, TT);
      SB();
    });
#pragma unroll
    for (int p = 0; p < 3; ++p) {
#pragma unroll
      for (int mt = 0; mt < 2; ++mt) {
        const f32x2* y = &ub[p][4 * mt];
        const u32x2 vr = {pk2(y[0].x, y[1].x), pk2(y[2].x, y[3].x)};
        const u32x2 vi = {pk2(y[0].y, y[1].y), pk2(y[2].y, y[3].y)};
        if (p < 2) {
          *(LAS u32x2*)(lds + imw + mt * 3200 + 16 * p * 40) = vr;
          *(LAS u32x2*)(lds + imw + mt * 3200 + (40 + 16 * p) * 40) = vi;
        } else {
          *(LAS u32x2*)(lds + imw2 + mt * 3200) = r < 8 ? vr : vi;
        }
      }
    }
    gc(IC<2>{}, IC<8>{}); gc(IC<2>{}, IC<9>{});
    asm volatile("s_waitcnt lgkmcnt(0)" ::: "memory");
#pragma unroll
    for (int f = 0; f < 5; ++f) {
      LAS char* p0 = lds + (f < 4 ? imr + (f >> 1) * 3200 + (f & 1) * 1280 : imr4);
      const bf16x8 v = lds_frag_tr(p0, p0 + 160);
      store16_sc1(ws, hst[f], __builtin_bit_cast(u32x4, v));
    }
  }

#pragma unroll
  for (int it = 0; it < 4; ++it) fv[it] = ld8_sc1(fsrc, foff[it]);
  bf16x8 qf[2][2];
#pragma unroll
  for (int mt = 0; mt < 2; ++mt) {
    unsigned tl[2];
#pragma unroll
    for (int h2 = 0; h2 < 2; ++h2) {
      const unsigned ph = phi2(accQ[2][mt][2 * h2], accQ[2][mt][2 * h2 + 1]);
      tl[h2] = g < 2 ? ph : 0u;
    }
#pragma unroll
    for (int h = 0; h < 2; ++h)
      qf[h][mt] = frag4(phi2(accQ[h][mt][0], accQ[h][mt][1]), phi2(accQ[h][mt][2], accQ[h][mt][3]), tl[0], tl[1]);
  }
  asm volatile("" : "+v"(qf[0][0]), "+v"(qf[0][1]), "+v"(qf[1][0]), "+v"(qf[1][1]));
  unsigned kdst[4];
#pragma unroll
  for (int it = 0; it < 4; ++it) {
    const int i = tid + 256 * it, h = i >= 420, ii = i - 420 * h;
    const int dd = (ii * 3277) >> 16;
    const int e = ii < 400 ? ii - 20 * dd : 20, d = ii < 400 ? dd : ii - 400;
    kdst[it] = L_KVT + (h * 21 + e) * 40 + d * 2;
  }
  asm volatile("" : "+v"(kdst[0]), "+v"(kdst[1]), "+v"(kdst[2]), "+v"(kdst[3]));
  asm volatile("s_waitcnt vmcnt(0)\n\ts_barrier" : "+v"(fv[0]), "+v"(fv[1]), "+v"(fv[2]), "+v"(fv[3])::"memory");
  if (tid == 0) __hip_atomic_store((GAS unsigned*)(ws + WS_HFLAG) + rb * 16 + cg, tag, __ATOMIC_RELAXED, __HIP_MEMORY_SCOPE_AGENT);
  {
    unsigned spins = 0;
    for (;;) {
      const bool ok = fv[0][1] == tag && fv[1][1] == tag && fv[2][1] == tag && fv[3][1] == tag;
      if (__all(ok)) break;
      if (spin_fail(spins, tmo, tag)) break;
#pragma unroll
      for (int it = 0; it < 4; ++it) fv[it] = ld8_wait(fsrc, foff[it]);
    }
#pragma unroll
    for (int it = 0; it < 4; ++it) {
      const float f = __uint_as_float(fv[it][0]);
      if (it < 3 || tid < 72) *(LAS u16*)(lds + kdst[it]) = (u16)pk2(f, f);
    }
  }
  LSYNC();

  {
    f32x4 num[2][2], nt[2], den[2][2];
#pragma unroll
    for (int mt = 0; mt < 2; ++mt) nt[mt] = f32x4{0, 0, 0, 0};
    sfor<0, 2>([&](auto H) {
      constexpr int h = H;
      LAS char* kbp = lds + L_KVT + h * 840;
      const u32x2 z2 = {0u, 0u};
      const u32x2 ml = *(LAS u32x2*)(kbp + r * 40 + 8 * g), mh = *(LAS u32x2*)(kbp + r * 40 + 32);
      const u32x2 tl = *(LAS u32x2*)(kbp + (16 + (r & 3)) * 40 + 8 * g), th = *(LAS u32x2*)(kbp + (16 + (r & 3)) * 40 + 32);
      const u32x2 dl = *(LAS u32x2*)(kbp + 800 + 8 * g), dh = *(LAS u32x2*)(kbp + 800 + 32);
      const bool gh = g == h, rv = (r >> 2) == h;
      const u32x2 mh2 = gh ? mh : z2, dh2 = gh ? dh : z2, tl2 = rv ? tl : z2, th2 = (rv && gh) ? th : z2;
      const bf16x8 am = frag4(ml[0], ml[1], mh2[0], mh2[1]);
      const bf16x8 at = frag4(tl2[0], tl2[1], th2[0], th2[1]);
      const bf16x8 ad = frag4(dl[0], dl[1], dh2[0], dh2[1]);
#pragma unroll
      for (int mt = 0; mt < 2; ++mt) {
        num[h][mt] = mfma(am, qf[h][mt], f32x4{0, 0, 0, 0});
        nt[mt] = mfma(at, qf[h][mt], nt[mt]);
        den[h][mt] = mfma(ad, qf[h][mt], f32x4{0, 0, 0, 0});
      }
    });
    sfor<0, 2>([&](auto MT) {
      constexpr int mt = MT;
      const float og = rbf(1.f - gv[mt]);
      float sc[2];
#pragma unroll
      for (int h = 0; h < 2; ++h) sc[h] = og * __builtin_amdgcn_rcpf(den[h][mt][0] + 1e-6f);
      const u32x4 ov = {pk2(num[0][mt][0] * sc[0], num[0][mt][1] * sc[0]), pk2(num[0][mt][2] * sc[0], num[0][mt][3] * sc[0]),
                        pk2(num[1][mt][0] * sc[1], num[1][mt][1] * sc[1]), pk2(num[1][mt][2] * sc[1], num[1][mt][3] * sc[1])};
      store16_sc1(ws, ost + mt * 240 * 256 + g * 256, ov);
      const float st = g == 0 ? sc[0] : sc[1];
      const u32x2 tv = {pk2(nt[mt][0] * st, nt[mt][1] * st), pk2(nt[mt][2] * st, nt[mt][3] * st)};
      if (g < 2) store8_sc1(ws, ost + mt * 240 * 256 + 4 * 256 + 8 * g, tv);
    });
  }

  const unsigned hoff = WS_HFLAG + ((T >> 1) * 16 + r) * 4;
  unsigned hf = ld4_sc1(ws, hoff);
  u32x2 exv[5], edv[5];
  unsigned egv[5], eoff[5], elds[5], goff[5];
#pragma unroll
  for (int it = 0; it < 5; ++it) {
    const int item = tid + 256 * it, tp = (item * 3277) >> 16, dq = item - 20 * tp;
    const int mt = tp >> 4, rr = tp & 15;
    const int t = 64 * T + 32 * (mt >> 1) + 8 * (rr >> 2) + 4 * (mt & 1) + (rr & 3);
    const int d = 80 * jt + 4 * dq;
    eoff[it] = (t * 640 + d) * 4; elds[it] = tp * 320 + (((dq & ~3) | ((dq & 3) ^ ((tp >> 1) & 3))) << 4);
    exv[it] = ld8(x, eoff[it] >> 1);
    edv[it] = ld8(dskip, d * 2);
    goff[it] = WS_G + t * 4;
  }
  asm volatile("s_waitcnt vmcnt(10)" : "+v"(hf)::"memory");
  if (!__all(hf == tag)) {
    unsigned spins = 0;
#pragma clang loop unroll(disable)
    for (;;) {
      hf = ld4_wait(ws, hoff);
      if (__all(hf == tag)) break;
      if (spin_fail(spins, tmo, tag)) break;
    }
  }

  const char* abase = sptr(ws, WS_A + 4 * T * 240 * 256);
  const unsigned alane = lane * 16;
  bf16x8 a[4][4];
  auto ga = [&](auto KP) {
    constexpr int kp = KP;
    const unsigned q = kp < 10 ? 32 * (kp >> 1) + 16 * (w >> 1) + 8 * (w & 1) + 4 * (kp & 1) : 160 + 16 * (kp - 10) + 4 * w;
    const char* p = abase + q * 256;
    sfor<0, 4>([&](auto MT) {
      constexpr int mt = MT;
      a[kp & 3][mt] = ld16_sc1(p + mt * 240 * 256, alane);
    });
  };
  ga(IC<0>{}); ga(IC<1>{}); ga(IC<2>{});
#pragma unroll
  for (int it = 0; it < 5; ++it) egv[it] = ld4_sc1(ws, goff[it]);
  sfor<0, 10>([&](auto N) { gc(IC<3>{}, N); });
  asm volatile("s_barrier" ::: "memory");
  if (tid == 0) {
    __hip_atomic_store((GAS unsigned*)(ws + WS_OFLAG) + rb * 16 + cg, tag, __ATOMIC_RELAXED, __HIP_MEMORY_SCOPE_AGENT);
    if (blockIdx.x == 0) __hip_atomic_store((GAS unsigned*)(ws + WS_EPOCH), tag, __ATOMIC_RELAXED, __HIP_MEMORY_SCOPE_AGENT);
  }
  asm volatile("" : "+v"(eoff[0]), "+v"(eoff[1]), "+v"(eoff[2]), "+v"(eoff[3]), "+v"(eoff[4]), "+v"(elds[0]), "+v"(elds[1]), "+v"(elds[2]),
               "+v"(elds[3]), "+v"(elds[4]));
  SB();

  const char* wsrc = sptr(ws, WS_WO + (jt * 20 + w) * 5120 + 2048);
  auto gw_ = [&](auto M, auto N) {
    constexpr int m = M, n = N;
    if constexpr (m < 2) glds<1, m * 5120 + n * 1024, n * 1024 - 2048>(wsrc + m * 4 * 5120, lin, dB);
    else glds<1, (m - 2) * 5120 + n * 1024, n * 1024 - 2048>(wsrc + m * 4 * 5120, lin, dA);
  };
  const int wlp0 = (8 * g + q4 + 4 * (g & 1)) * 160 + 8 * pp, wlp1 = (8 * g + q4 + 4 * (1 - (g & 1))) * 160 + 8 * pp;
  LAS char* wrA[2] = {lds + A0 + w * 2 * UNIT + wlp0, lds + A0 + w * 2 * UNIT + wlp1};
  LAS char* wrB[2] = {lds + R1 + w * UNIT + wlp0, lds + R1 + w * UNIT + wlp1};
  asm volatile("" : "+v"(wrA[0]), "+v"(wrA[1]), "+v"(wrB[0]), "+v"(wrB[1]));
  bf16x8 bf[2][5];
  auto rd = [&](auto KP, auto I) {
    constexpr int kp = KP, i = I;
    if constexpr (kp < 4) {
    } else if constexpr (kp < 10) {
      if constexpr (i < 5) bf[kp & 1][i] = lds_frag(kp < 6 ? crB[kp & 1] + i * 2048 : crA[kp & 1] + (kp < 8 ? 0 : UNIT) + i * 2048);
    } else {
      constexpr int m = kp - 10;
      bh[kp & 1][i >> 1][i & 1] = __builtin_amdgcn_ds_read_tr16_b64_v4i16(
          (LAS bf16x4*)(m < 2 ? wrB[i & 1] + m * 5120 + 32 * (i >> 1) : wrA[i & 1] + (m - 2) * 5120 + 32 * (i >> 1)));
    }
  };
  auto bfrag = [&](auto KP, auto NT) {
    constexpr int kp = KP, nt = NT;
    if constexpr (kp < 4) return bu[kp][nt];
    else if constexpr (kp < 10) return bf[kp & 1][nt];
    else return (bf16x8)__builtin_shufflevector(bh[kp & 1][nt][0], bh[kp & 1][nt][1], 0, 1, 2, 3, 4, 5, 6, 7);
  };
  f32x4 acc[4][5];
#pragma unroll
  for (int mt = 0; mt < 4; ++mt)
#pragma unroll
    for (int nt = 0; nt < 5; ++nt) acc[mt][nt] = f32x4{0, 0, 0, 0};
  LAS char* pw_ = lds + A0 + w * 2 * UNIT + r * 320 + ((g ^ ((r >> 1) & 3)) << 4);
  asm volatile("" : "+v"(pw_));
  f32x4 skip[5];
  auto skipc = [&](auto IT) {
    constexpr int it = IT;
    const unsigned s0 = pk2(blo(exv[it][0]) * blo(edv[it][0]), bhi(exv[it][0]) * bhi(edv[it][0]));
    const unsigned s1 = pk2(blo(exv[it][1]) * blo(edv[it][1]), bhi(exv[it][1]) * bhi(edv[it][1]));
    const float gt = __uint_as_float(egv[it]);
    skip[it] = f32x4{blo(s0) * gt, bhi(s0) * gt, blo(s1) * gt, bhi(s1) * gt};
    asm volatile("" : "+v"(skip[it]));
  };
  unsigned of = 0;
  const unsigned ooff = WS_OFLAG + ((T >> 1) * 16 + r) * 4;
#define AWAIT(N, S) do { asm volatile("s_waitcnt vmcnt(" #N ") lgkmcnt(0)" : "+v"(a[S][0]), "+v"(a[S][1]), "+v"(a[S][2]), "+v"(a[S][3])::"memory"); SB(); } while (0)
  sfor<0, 15>([&](auto KP) {
    constexpr int kp = KP, sl = kp & 3;
    if constexpr (kp == 0) {
      AWAIT(23, sl);
      asm volatile("" : "+v"(exv[0]), "+v"(exv[1]), "+v"(exv[2]), "+v"(exv[3]), "+v"(exv[4]), "+v"(edv[0]), "+v"(edv[1]), "+v"(edv[2]),
                   "+v"(edv[3]), "+v"(edv[4])::"memory");
    }
    if constexpr (kp == 1) AWAIT(26, sl);
    if constexpr (kp == 2) AWAIT(29, sl);
    if constexpr (kp == 3) {
      AWAIT(18, sl);
      asm volatile("" : "+v"(egv[0]), "+v"(egv[1]), "+v"(egv[2]), "+v"(egv[3]), "+v"(egv[4])::"memory");
    }
    if constexpr (kp == 4) AWAIT(15, sl);
    if constexpr (kp == 5 || kp == 11 || kp == 12) AWAIT(13, sl);
    if constexpr (kp == 6) AWAIT(19, sl);
    if constexpr (kp == 7 || kp == 9) AWAIT(18, sl);
    if constexpr (kp == 7) asm volatile("" : "+v"(of)::"memory");
    if constexpr (kp == 8) AWAIT(28, sl);
    if constexpr (kp == 10) AWAIT(23, sl);
    if constexpr (kp == 13) AWAIT(4, sl);
    if constexpr (kp == 14) AWAIT(0, sl);
    if constexpr (kp == 7) {
      if (!__all(of == tag)) {
        unsigned spins = 0;
        for (;;) {
          of = ld4_wait(ws, ooff);
          if (__all(of == tag)) break;
          if (spin_fail(spins, tmo, tag)) break;
        }
      }
      SB();
    }
    if constexpr (kp + 3 < 15) ga(IC<kp + 3>{});
    sfor<0, 20>([&](auto I) {
      constexpr int i = I, nt = i >> 2, mt = i & 3;
      acc[mt][nt] = mfma(bfrag(KP, IC<nt>{}), a[sl][mt], acc[mt][nt]);
      if constexpr (kp + 1 < 15 && i < 10) rd(IC<kp + 1>{}, I);
      if constexpr (kp < 3 && (i == 3 || i == 9 || i == 15 || (kp == 2 && i == 18))) gc(IC<4>{}, IC<3 * kp + (i == 18 ? 3 : i / 6)>{});
      if constexpr ((kp == 5 || kp == 7) && (i & 1) == 1) gw_(IC<kp - 5 + (i >= 10)>{}, IC<(i % 10) / 2>{});
      if constexpr (kp == 9 && (i & 3) == 1) gw_(IC<4>{}, IC<(i >> 2)>{});
      if constexpr (kp == 4 && i == 19) of = ld4_sc1(ws, ooff);
      if constexpr (kp == 14 && i >= 3) *(LAS f32x4*)(pw_ + ((i - 3) & 3) * 5120 + ((i - 3) >> 2) * 64) = acc[(i - 3) & 3][(i - 3) >> 2];
      if constexpr (kp == 13 && (i & 3) == 2) skipc(IC<(i >> 2)>{});
      SB();
    });
  });
#pragma unroll
  for (int i = 17; i < 20; ++i) *(LAS f32x4*)(pw_ + (i & 3) * 5120 + (i >> 2) * 64) = acc[i & 3][i >> 2];
  LSYNC();
#pragma unroll
  for (int it = 0; it < 5; ++it) {
    LAS char* p = lds + A0 + elds[it];
    const f32x4 s0 = *(LAS f32x4*)p, s1 = *(LAS f32x4*)(p + 2 * UNIT), s2 = *(LAS f32x4*)(p + 4 * UNIT), s3 = *(LAS f32x4*)(p + 6 * UNIT);
    *(f32x4*)((char*)out + eoff[it]) = ((s0 + s1) + (s2 + s3)) + skip[it];
  }
  asm volatile(".Lkf_end:");
}

extern "C" void kernel_launch(void* const* d_in, const int* in_sizes, int n_in, void* d_out, int out_size, void* d_ws,
                              size_t ws_size, hipStream_t stream) {
  const u16* x = (const u16*)d_in[0];
  const u16* lre = (const u16*)d_in[1];
  const u16* lim = (const u16*)d_in[2];
  const u16* bre = (const u16*)d_in[3];
  const u16* bim = (const u16*)d_in[4];
  const u16* cre = (const u16*)d_in[5];
  const u16* cim = (const u16*)d_in[6];
  const u16* ldt = (const u16*)d_in[7];
  const u16* dskip = (const u16*)d_in[8];
  const u16* wqkv = (const u16*)d_in[10];
  const u16* wo = (const u16*)d_in[11];
  const u16* gw = (const u16*)d_in[12];
  constexpr int LDS = 163840;
  (void)hipFuncSetAttribute((const void*)k_fused, hipFuncAttributeMaxDynamicSharedMemorySize, LDS);
  k_fused<<<256, 256, LDS, stream>>>(x, lre, lim, ldt, bre, bim, wqkv, gw, cre, cim, wo, dskip, (char*)d_ws, (float*)d_out);
}
```
